```python
import torch

def _softmax(x, axis=-1):
    return torch.softmax(x, dim=axis)
def _gelu(x):
    return torch.nn.functional.gelu(x, approximate="tanh")
def _silu(x):
    return torch.nn.functional.silu(x)
def _sigmoid(x):
    return torch.sigmoid(x)
def _layernorm(x, axis=-1, eps=1e-5):
    m = x.mean(dim=axis, keepdim=True)
    v = x.var(dim=axis, keepdim=True, unbiased=False)
    return (x - m) / torch.sqrt(v + eps)
def _scatter_add(tgt, idx, src, axis=0):
    return tgt.clone().index_add_(axis, idx, src)
def _segment_sum(src, seg, n):
    out = torch.zeros((n,) + src.shape[1:], dtype=src.dtype)
    return out.index_add_(0, seg, src)
def _pad(x, pw):
    flat = tuple(v for pair in reversed(pw) for v in pair)
    return torch.nn.functional.pad(x, flat)

kernel_name = 'concat_heads__sliding_block__ssm_conv_h3__prompt'

def setup_inputs():

    _w = (lambda a: (lambda t: t.to(torch.bfloat16) if t.is_floating_point() else t)(torch.as_tensor(a)))
    def _A_setup():
        B, S, D = 1, 2048, 576
        return {
            "x": _w((lambda *s: torch.randn(*s, dtype=torch.bfloat16) * 0.2)(B, S, D)),
            "wqkv": _w((lambda *s: torch.randn(*s, dtype=torch.bfloat16) * 0.2)(D, 3 * D)),
            "wo": _w((lambda *s: torch.randn(*s, dtype=torch.bfloat16) * 0.05)(D, D)),
        }
    _A_in = _A_setup()

    def _B_setup():
        B, S, D = 1, 2048, 576
        ks = 4
        return {
            "q": _w((lambda *s: torch.randn(*s, dtype=torch.bfloat16) * 0.05)(B, S, D)),
            "k": _w((lambda *s: torch.randn(*s, dtype=torch.bfloat16) * 0.8)(B, S, D)),
            "v": _w((lambda *s: torch.randn(*s, dtype=torch.bfloat16) * 0.8)(B, S, D)),
            "shift_k": _w((lambda *s: torch.randn(*s, dtype=torch.bfloat16) * 0.8)(D, ks) * 0.5),
            "ssm_k": _w((lambda *s: torch.randn(*s, dtype=torch.bfloat16) * 3.2)(D, S) * (1.0 / S ** 0.5)),
            "ssm_D": _w((lambda *s: torch.randn(*s, dtype=torch.bfloat16) * 0.8)(D)),
        }
    _B_in = _B_setup()

    return {
        "A_x": _A_in["x"],
        "A_wqkv": _A_in["wqkv"],
        "A_wo": _A_in["wo"],
        "B_q": _B_in["q"],
        "B_k": _B_in["k"],
        "B_v": _B_in["v"],
        "B_shift_k": _B_in["shift_k"],
        "B_ssm_k": _B_in["ssm_k"],
        "B_ssm_D": _B_in["ssm_D"],
    }

def reference(A_x, A_wqkv, A_wo, B_q, B_k, B_v, B_shift_k, B_ssm_k, B_ssm_D):
    def _A_ref(x, wqkv, wo):
        B, S, D = x.shape
        H = 32; Dh = D // H; W = 256
        qkv = (lambda x, *d: x.permute(*d))((x @ wqkv).reshape(B, S, 3, H, Dh), 2, 0, 3, 1, 4)
        q, k, v = qkv[0], qkv[1], qkv[2]
        scores = torch.einsum("bhqd,bhkd->bhqk", q, k) * (Dh ** -0.5)
        idx = torch.arange(S)
        mask = (idx[:, None] - idx[None, :] >= 0) & (idx[:, None] - idx[None, :] < W)
        scores = torch.where(mask[None, None], scores, -1e9)
        p = _softmax(scores, axis=-1)
        o = torch.einsum("bhqk,bhkd->bhqd", p, v)
        return (lambda x, *d: x.permute(*d))(o, 0, 2, 1, 3).reshape(B, S, D) @ wo

    def _B_ref(q, k, v, shift_k, ssm_k, ssm_D):
        B, S, D = q.shape
        ks = shift_k.shape[-1]
        k_pad = _pad(k, ((0, 0), (ks - 1, 0), (0, 0)))
        k_shift = torch.zeros_like(k)
        for t in range(ks):
            k_shift = k_shift + k_pad[:, t:t + S, :] * shift_k[:, ks - 1 - t][None, None, :]
        kv = k_shift * v
        L = 2 * S
        kv_pad = _pad(kv, ((0, 0), (0, S), (0, 0)))
        kf_pad = _pad(ssm_k, ((0, 0), (0, S)))
        kv_f = (lambda x, axes=(-2,-1): torch.fft.rfft2(x.float(), dim=axes))(kv_pad[:, None, :, :], axes=(1, 2))
        kf_f = (lambda x, axes=(-2,-1): torch.fft.rfft2(x.float(), dim=axes))(kf_pad[:, None, :], axes=(1, 2))
        kf_f = (lambda x, *d: x.permute(*d))(kf_f, 1, 2, 0)
        y_f = kv_f * kf_f[None, :, :, :]
        y = (lambda x, s, axes=(-2,-1): torch.fft.irfft2(x, s=s, dim=axes).to(torch.bfloat16))(y_f, (1, L), axes=(1, 2))[:, 0, :S, :]
        y = y + kv * ssm_D[None, None, :]
        return q * y

    a = _A_ref(A_x, A_wqkv, A_wo)
    b = _B_ref(B_q, B_k, B_v, B_shift_k, B_ssm_k, B_ssm_D)
    return torch.concatenate([a, b], axis=-1)
```

```cpp
#include <hip/hip_runtime.h>
#include <hip/hip_bf16.h>
typedef unsigned short u16;
__device__ __forceinline__ float bf2f(u16 x){ return __uint_as_float(((unsigned)x)<<16); }
__device__ __forceinline__ u16 f2bf(float f){ unsigned u=__float_as_uint(f); u += 0x7fffu + ((u>>16)&1u); return (u16)(u>>16); }
constexpr int S=2048, D=576, H=32, DH=18, W=256;
__global__ void k_gemm(const u16* A, const u16* B, u16* C, int M, int N, int K, int ldc){
  int n = blockIdx.x*blockDim.x+threadIdx.x; int m = blockIdx.y;
  if(n>=N) return;
  float acc=0; for(int k=0;k<K;k++) acc += bf2f(A[m*K+k])*bf2f(B[k*N+n]);
  C[m*ldc+n]=f2bf(acc);
}
__global__ void k_attn(const u16* qkv, u16* o){
  int i = blockIdx.x*blockDim.x+threadIdx.x; int h = blockIdx.y;
  float q[DH]; for(int d=0;d<DH;d++) q[d]=bf2f(qkv[i*3*D+h*DH+d]);
  float m=-1e30f; int j0 = i-W+1<0?0:i-W+1;
  for(int j=j0;j<=i;j++){ float s=0; for(int d=0;d<DH;d++) s+=q[d]*bf2f(qkv[j*3*D+D+h*DH+d]); s*=0.23570226f; m=fmaxf(m,s);}
  float l=0; float acc[DH]; for(int d=0;d<DH;d++) acc[d]=0;
  for(int j=j0;j<=i;j++){ float s=0; for(int d=0;d<DH;d++) s+=q[d]*bf2f(qkv[j*3*D+D+h*DH+d]); s*=0.23570226f; float p=__expf(s-m); l+=p; for(int d=0;d<DH;d++) acc[d]+=p*bf2f(qkv[j*3*D+2*D+h*DH+d]);}
  for(int d=0;d<DH;d++) o[i*D+h*DH+d]=f2bf(acc[d]/l);
}
__global__ void k_kv(const u16* k, const u16* v, const u16* sk, float* kv){
  int d = blockIdx.x*blockDim.x+threadIdx.x; int t = blockIdx.y; if(d>=D) return;
  float a=0; for(int j=0;j<4;j++){ int tt=t-3+j; if(tt>=0) a+= bf2f(k[tt*D+d])*bf2f(sk[d*4+3-j]); }
  kv[t*D+d]=bf2f(f2bf(a*bf2f(v[t*D+d])));
}
__global__ void k_conv(const float* kv, const u16* hk, const u16* q, const u16* Dd, u16* out){
  int d = blockIdx.x*blockDim.x+threadIdx.x; int t = blockIdx.y; if(d>=D) return;
  float y=0; for(int s=0;s<=t;s++) y += kv[s*D+d]*bf2f(hk[d*S+t-s]);
  y += kv[t*D+d]*bf2f(Dd[d]);
  out[t*2*D+D+d]=f2bf(y*bf2f(q[t*D+d]));
}
extern "C" void kernel_launch(void* const* d_in, const int* in_sizes, int n_in, void* d_out, int out_size, void* d_ws, size_t ws_size, hipStream_t stream){
  const u16* x=(const u16*)d_in[0]; const u16* wqkv=(const u16*)d_in[1]; const u16* wo=(const u16*)d_in[2];
  const u16* q=(const u16*)d_in[3]; const u16* k=(const u16*)d_in[4]; const u16* v=(const u16*)d_in[5];
  const u16* sk=(const u16*)d_in[6]; const u16* hk=(const u16*)d_in[7]; const u16* Dd=(const u16*)d_in[8];
  u16* out=(u16*)d_out; char* ws=(char*)d_ws;
  u16* qkv=(u16*)ws; u16* o=(u16*)(ws+(16<<20)); float* kv=(float*)(ws+(32<<20));
  k_gemm<<<dim3((3*D+255)/256,S),256,0,stream>>>(x,wqkv,qkv,S,3*D,D,3*D);
  k_attn<<<dim3(S/64,H),64,0,stream>>>(qkv,o);
  k_gemm<<<dim3((D+255)/256,S),256,0,stream>>>(o,wo,out,S,D,D,2*D);
  k_kv<<<dim3((D+63)/64,S),64,0,stream>>>(k,v,sk,kv);
  k_conv<<<dim3((D+63)/64,S),64,0,stream>>>(kv,hk,q,Dd,out);
}
```

```cpp
#include <hip/hip_runtime.h>
typedef unsigned short u16;
typedef __bf16 bf16x8 __attribute__((ext_vector_type(8)));
typedef __bf16 bf16x2 __attribute__((ext_vector_type(2)));
typedef short s16x4 __attribute__((ext_vector_type(4)));
typedef float f32x2 __attribute__((ext_vector_type(2)));
typedef float f32x4 __attribute__((ext_vector_type(4)));
typedef float f32x16 __attribute__((ext_vector_type(16)));
typedef unsigned u32x2 __attribute__((ext_vector_type(2)));
typedef unsigned u32x4 __attribute__((ext_vector_type(4)));
#define GAS __attribute__((address_space(1)))
#define LAS __attribute__((address_space(3)))
#define DEV __device__ __forceinline__

constexpr int S = 2048, D = 576;
constexpr size_t WS_QKV = 0;
constexpr size_t WS_KVT = 8u << 20;
constexpr size_t WS_ZT = 11u << 20;
constexpr size_t WS_OM = 14u << 20;
constexpr size_t WS_OT = 17u << 20;
constexpr size_t WS_STAMP = 32u << 20;
#define STAMPIT(i)
#define STAMPW(i, w)

DEV unsigned pk_bf16(float a, float b) { f32x2 v = {a, b}; bf16x2 r = __builtin_convertvector(v, bf16x2); return __builtin_bit_cast(unsigned, r); }
DEV void barrier_raw() { __builtin_amdgcn_s_barrier(); }
#define WAIT_VM(n) asm volatile("s_waitcnt vmcnt(" #n ")" ::: "memory")
#define SCHED_FENCE() __builtin_amdgcn_sched_barrier(0)
#define INTERLEAVE12() do { _Pragma("unroll") for (int i_ = 0; i_ < 12; i_++) { __builtin_amdgcn_sched_group_barrier(0x008, 1, 0); __builtin_amdgcn_sched_group_barrier(0x100, 1, 0); } __builtin_amdgcn_sched_group_barrier(0x008, 4, 0); } while (0)
#define WAIT_LGKM0() asm volatile("s_waitcnt lgkmcnt(0)" ::: "memory")

#define RING 3
constexpr int STG = 32768;

struct Params { const u16 *x, *wqkv, *wo, *q, *k, *v, *sk, *hk, *Dd; u16* out; char* ws; };

DEV void p1_dma_stage(const __amdgpu_buffer_rsrc_t& rx, const __amdgpu_buffer_rsrc_t& rw, LAS char* lds, int s, int hw, unsigned vx, unsigned vw0, unsigned vw1) {
  LAS char* xs = lds + (s % RING) * STG; LAS char* wsl = xs + 16384;
#pragma unroll
  for (int u = 0; u < 4; u++)
    __builtin_amdgcn_raw_ptr_buffer_load_lds(rx, xs + (4 * hw + u) * 1024, 16, vx, (4 * hw + u) * 8 * 1152 + s * 128, 0, 0);
#pragma unroll
  for (int u = 0; u < 4; u++)
    __builtin_amdgcn_raw_ptr_buffer_load_lds(rw, wsl + (4 * hw + u) * 1024, 16, (u & 2) ? vw1 : vw0, (64 * s + (4 * hw + u) * 4) * 3456, 0, 0);
}

DEV void p1_block(const Params& p, LAS char* lds, int rb, int c, int wave, int lane) {
  char* ws = p.ws;
  const int g = lane >> 4;
  f32x4 acc[4][4];
  if (wave >= 4) {
    const int hw = wave - 4;
    const __amdgpu_buffer_rsrc_t rx = __builtin_amdgcn_make_buffer_rsrc((void*)(p.x + (size_t)rb * 128 * D), 0, 128 * D * 2, 0x00020000);
    const __amdgpu_buffer_rsrc_t rw = __builtin_amdgcn_make_buffer_rsrc((void*)p.wqkv, 0, D * 3 * D * 2, 0x00020000);
    const unsigned vx = (lane >> 3) * 1152 + (((lane & 7) ^ (lane >> 3)) << 4);
    const int cm = (c == 13) ? 7 : 15;
    const unsigned vw0 = (lane >> 4) * 3456 + c * 256 + ((((lane & 15) ^ ((lane >> 4) << 1)) & cm) << 4);
    const unsigned vw1 = (lane >> 4) * 3456 + c * 256 + ((((lane & 15) ^ (((lane >> 4) | 4) << 1)) & cm) << 4);
#pragma unroll
    for (int s = 0; s < RING - 1; s++) p1_dma_stage(rx, rw, lds, s, hw, vx, vw0, vw1);
#pragma unroll
    for (int s = 0; s < 9; s++) {
      const int inflight = (s + RING - 2 < 8 ? s + RING - 2 : 8) - s;
      if (inflight == 0) WAIT_VM(0); else if (inflight == 1) WAIT_VM(8); else if (inflight == 2) WAIT_VM(16); else WAIT_VM(24);
      barrier_raw();
      if (s + RING - 1 < 9) p1_dma_stage(rx, rw, lds, s + RING - 1, hw, vx, vw0, vw1);
    }
    barrier_raw();
    barrier_raw();
  } else {
    const int wm = wave & 1, wn = wave >> 1;
#pragma unroll
    for (int i = 0; i < 4; i++)
#pragma unroll
      for (int j = 0; j < 4; j++) acc[i][j] = (f32x4){0.f, 0.f, 0.f, 0.f};
    const unsigned xa0 = (64 * wm + (lane & 15)) * 128 + ((g ^ (lane & 7)) << 4);
    const int q = (lane >> 2) & 3, pp = lane & 3, f = q | ((g & 1) << 2);
    unsigned wb[4];
#pragma unroll
    for (int nt = 0; nt < 4; nt++) wb[nt] = 16384 + (8 * g + q) * 256 + (((4 * wn + nt) ^ f) << 5) + pp * 8;
    bf16x8 a[2][4], b[2][4];
    auto load_frags = [&](int buf, int s, int ks) {
      LAS char* base = lds + (s % RING) * STG;
#pragma unroll
      for (int mt = 0; mt < 4; mt++) a[buf][mt] = *(const LAS bf16x8*)(base + ((xa0 ^ (ks << 6)) + mt * 2048));
#pragma unroll
      for (int nt = 0; nt < 4; nt++) {
        s16x4 lo = __builtin_amdgcn_ds_read_tr16_b64_v4i16((LAS s16x4*)(base + wb[nt] + ks * 8192));
        s16x4 hi = __builtin_amdgcn_ds_read_tr16_b64_v4i16((LAS s16x4*)(base + wb[nt] + ks * 8192 + 1024));
        b[buf][nt] = __builtin_bit_cast(bf16x8, __builtin_shufflevector(lo, hi, 0, 1, 2, 3, 4, 5, 6, 7));
      }
    };
    auto mm = [&](int buf) {
#pragma unroll
      for (int mt = 0; mt < 4; mt++)
#pragma unroll
        for (int nt = 0; nt < 4; nt++) acc[mt][nt] = __builtin_amdgcn_mfma_f32_16x16x32_bf16(a[buf][mt], b[buf][nt], acc[mt][nt], 0, 0, 0);
    };
    barrier_raw();
    STAMPW(4, 0);
    load_frags(0, 0, 0);
#pragma unroll
    for (int s = 0; s < 9; s++) {
      SCHED_FENCE();
      load_frags(1, s, 1);
      mm(0);
      INTERLEAVE12();
      SCHED_FENCE();
      WAIT_LGKM0();
      if (s < 8) { barrier_raw(); if (s == 0) STAMPW(5, 0); if (s == 3) STAMPW(6, 0); if (s == 7) STAMPW(7, 0); SCHED_FENCE(); load_frags(0, s + 1, 0); mm(1); INTERLEAVE12(); }
      else mm(1);
    }
    SCHED_FENCE();
    barrier_raw();
    const float qs = 0.23570226039551584f * 1.4426950408889634f;
#pragma unroll
    for (int nt = 0; nt < 4; nt++) {
      const int n = 64 * wn + 16 * nt + (lane & 15);
      const float sc = (c * 128 + n < D) ? qs : 1.0f;
#pragma unroll
      for (int mt = 0; mt < 4; mt++) {
        const int t8 = 8 * wm + 2 * mt + (g >> 1);
        u32x2 v = {pk_bf16(acc[mt][nt][0] * sc, acc[mt][nt][1] * sc), pk_bf16(acc[mt][nt][2] * sc, acc[mt][nt][3] * sc)};
        *(LAS u32x2*)(lds + n * 256 + ((t8 ^ (lane & 15)) << 4) + (g & 1) * 8) = v;
      }
    }
    WAIT_LGKM0();
    barrier_raw();
  }
  STAMPIT(2);
  const __amdgpu_buffer_rsrc_t rq = __builtin_amdgcn_make_buffer_rsrc((void*)(ws + WS_QKV), 0, 1728 * S * 2, 0x00020000);
#pragma unroll
  for (int u = 0; u < 4; u++) {
    const int n = (wave * 4 + u) * 4 + (lane >> 4);
    u32x4 v = *(const LAS u32x4*)(lds + n * 256 + (((lane & 15) ^ (n & 15)) << 4));
    if (c * 128 + n < 3 * D)
      __builtin_amdgcn_raw_buffer_store_b128(v, rq, (unsigned)((c * 128 + n) * (S * 2) + rb * 256 + (lane & 15) * 16), 0, 16);
  }
}

DEV f32x2 unpk(unsigned w) { f32x2 r; r.x = __uint_as_float(w << 16); r.y = __uint_as_float(w & 0xffff0000u); return r; }

DEV void b1_block(const Params& p, LAS char* lds, int bb, int wave, int lane) {
  char* ws = p.ws;
  const int T0 = bb * 64;
  const GAS u32x4* kp = (const GAS u32x4*)p.k; const GAS u32x4* vp = (const GAS u32x4*)p.v;
  const int tid = wave * 64 + lane;
  u32x4 kr[11], vr[8], kx[4], vx;
  const int tb = T0 + 8 * wave;
#pragma unroll
  for (int i = 0; i < 11; i++) { const int t = tb - 3 + i; kr[i] = kp[(t < 0 ? 0 : t) * 72 + lane]; }
#pragma unroll
  for (int i = 0; i < 8; i++) vr[i] = vp[(tb + i) * 72 + lane];
  const int ce = 64 + (tid & 7), te = T0 + (tid >> 3);
#pragma unroll
  for (int i = 0; i < 4; i++) { const int t = te - 3 + i; kx[i] = kp[(t < 0 ? 0 : t) * 72 + ce]; }
  vx = vp[te * 72 + ce];
  const GAS u32x4* skp = (const GAS u32x4*)p.sk;
  u32x4 sw[4], sx[4];
#pragma unroll
  for (int i = 0; i < 4; i++) { sw[i] = skp[lane * 4 + i]; sx[i] = skp[ce * 4 + i]; }
  if (tb < 3 + 0 || tb == 0) {
#pragma unroll
    for (int i = 0; i < 3; i++) if (tb - 3 + i < 0) kr[i] = (u32x4){0, 0, 0, 0};
  }
#pragma unroll
  for (int i = 0; i < 3; i++) if (te - 3 + i < 0) kx[i] = (u32x4){0, 0, 0, 0};
  auto wts = [&](const u32x4* sv, f32x2 (&w)[4][4]) {
#pragma unroll
    for (int pr = 0; pr < 4; pr++) {
      f32x2 a0 = unpk(sv[pr][0]), a1 = unpk(sv[pr][1]), b0 = unpk(sv[pr][2]), b1 = unpk(sv[pr][3]);
      w[pr][0] = (f32x2){a0.x, b0.x}; w[pr][1] = (f32x2){a0.y, b0.y}; w[pr][2] = (f32x2){a1.x, b1.x}; w[pr][3] = (f32x2){a1.y, b1.y};
    }
  };
  {
    f32x2 w[4][4]; wts(sw, w);
#pragma unroll
    for (int pr = 0; pr < 4; pr++) {
      f32x2 kf[11];
#pragma unroll
      for (int i = 0; i < 11; i++) kf[i] = unpk(kr[i][pr]);
      f32x2 kv[8];
#pragma unroll
      for (int i = 0; i < 8; i++) { f32x2 s = kf[i] * w[pr][3] + kf[i + 1] * w[pr][2]; s += kf[i + 2] * w[pr][1]; s += kf[i + 3] * w[pr][0]; kv[i] = s * unpk(vr[i][pr]); }
      u32x4 o0 = {pk_bf16(kv[7].x, kv[6].x), pk_bf16(kv[5].x, kv[4].x), pk_bf16(kv[3].x, kv[2].x), pk_bf16(kv[1].x, kv[0].x)};
      u32x4 o1 = {pk_bf16(kv[7].y, kv[6].y), pk_bf16(kv[5].y, kv[4].y), pk_bf16(kv[3].y, kv[2].y), pk_bf16(kv[1].y, kv[0].y)};
      const int slot = ((7 - wave) ^ (lane & 7)) << 4;
      *(LAS u32x4*)(lds + (8 * lane + 2 * pr) * 128 + slot) = o0;
      *(LAS u32x4*)(lds + (8 * lane + 2 * pr + 1) * 128 + slot) = o1;
    }
  }
  {
    f32x2 w[4][4]; wts(sx, w);
    const int xl = 63 - (tid >> 3);
#pragma unroll
    for (int pr = 0; pr < 4; pr++) {
      f32x2 s = unpk(kx[0][pr]) * w[pr][3] + unpk(kx[1][pr]) * w[pr][2]; s += unpk(kx[2][pr]) * w[pr][1]; s += unpk(kx[3][pr]) * w[pr][0];
      s = s * unpk(vx[pr]);
      const unsigned o = pk_bf16(s.x, s.y);
      const int off = ((((xl >> 3) ^ (ce & 7)) << 4) + (xl & 7) * 2);
      *(LAS u16*)(lds + (8 * ce + 2 * pr) * 128 + off) = (u16)o;
      *(LAS u16*)(lds + (8 * ce + 2 * pr + 1) * 128 + off) = (u16)(o >> 16);
    }
  }
  __syncthreads();
  STAMPIT(2);
  const __amdgpu_buffer_rsrc_t rk = __builtin_amdgcn_make_buffer_rsrc((void*)(ws + WS_KVT), 0, D * S * 2, 0x00020000);
#pragma unroll
  for (int u = 0; u < 9; u++) {
    const int ii = wave * 9 + u;
    const int ch = ii * 8 + (lane >> 3);
    u32x4 v = *(const LAS u32x4*)(lds + ch * 128 + (lane & 7) * 16);
    const int chunk = (lane & 7) ^ (ii & 7);
    __builtin_amdgcn_raw_buffer_store_b128(v, rk, (unsigned)(ch * (S * 2) + (1984 - T0) * 2 + chunk * 16), 0, 16);
  }
}

__global__ void __launch_bounds__(512, 2) k1(Params p) {
  extern __shared__ __attribute__((aligned(16))) char smem[];
  LAS char* lds = (LAS char*)smem;
  char* ws = p.ws;
  const int wave = __builtin_amdgcn_readfirstlane(threadIdx.x >> 6), lane = threadIdx.x & 63;
  const int b = blockIdx.x, xcd = b & 7, loc = b >> 3;
  STAMPIT(0);
  if (loc < 28) p1_block(p, lds, (xcd >> 1) * 4 + (loc & 3), (xcd & 1) * 7 + (loc >> 2), wave, lane);
  else b1_block(p, lds, xcd * 4 + (loc - 28), wave, lane);
  STAMPIT(3);
}

constexpr int CV_HP = 4160, CV_UR = 4 * CV_HP, CV_SZ = CV_UR + 4576;
constexpr int L_CONV = 0;
constexpr int L_XCH = 2 * CV_SZ;
constexpr int L_ATT = 58880;
constexpr int A_K = 0, A_V = 36 * 768, A_ONES = 72 * 768, A_Q = 73 * 768, A_SZ = A_Q + 36 * 256;

DEV void conv_stage(const Params& p, LAS char* lds, int ch0, int tid) {
  char* ws = p.ws;
  const GAS u32x4* hk = (const GAS u32x4*)p.hk;
  const GAS u32x4* kvt = (const GAS u32x4*)(ws + WS_KVT);
#pragma unroll
  for (int it = 0; it < 2; it++) {
    const int item = it * 512 + tid;
    if (item < 516) {
      const int cs = item >= 258, m = item - cs * 258;
      const u32x4 z4 = {0, 0, 0, 0};
      const int i0 = m - 2, i1 = m - 1;
      u32x4 c0 = hk[(ch0 + cs) * 256 + (i0 < 0 ? 0 : (i0 > 255 ? 255 : i0))], c1 = hk[(ch0 + cs) * 256 + (i1 < 0 ? 0 : (i1 > 255 ? 255 : i1))];
      if (i0 < 0 || i0 > 255) c0 = z4;
      if (i1 < 0 || i1 > 255) c1 = z4;
      const unsigned a0 = __builtin_amdgcn_alignbit(c0[1], c0[0], 16), a1 = __builtin_amdgcn_alignbit(c0[2], c0[1], 16), a2 = __builtin_amdgcn_alignbit(c0[3], c0[2], 16),
                     a3 = __builtin_amdgcn_alignbit(c1[0], c0[3], 16), a4 = __builtin_amdgcn_alignbit(c1[1], c1[0], 16);
      LAS char* b = lds + L_CONV + cs * CV_SZ + m * 16;
      *(LAS u32x4*)(b) = (u32x4){a0, a1, a2, a3};
      *(LAS u32x4*)(b + CV_HP) = (u32x4){c0[1], c0[2], c0[3], c1[0]};
      *(LAS u32x4*)(b + 2 * CV_HP) = (u32x4){a1, a2, a3, a4};
      *(LAS u32x4*)(b + 3 * CV_HP) = (u32x4){c0[2], c0[3], c1[0], c1[1]};
    }
  }
#pragma unroll
  for (int it = 0; it < 2; it++) {
    const int item = it * 512 + tid;
    if (item < 572) {
      const int cs = item >= 286, m = item - cs * 286;
      u32x4 v = {0, 0, 0, 0};
      if (m < 256) v = kvt[(ch0 + cs) * 256 + m];
      *(LAS u32x4*)(lds + L_CONV + cs * CV_SZ + CV_UR + m * 16) = v;
    }
  }
}

DEV void conv_mfma(LAS char* lds, int wave, int lane, f32x4 (&acc)[8]) {
  const int cs = wave >> 1, half = wave & 1;
  const int r = lane & 15, g = lane >> 4;
  LAS char* hp = lds + L_CONV + cs * CV_SZ + (r & 3) * CV_HP + ((r & ~3) + 8 * g + half * 128) * 2;
  LAS char* ur = lds + L_CONV + cs * CV_SZ + CV_UR + (2032 - 16 * r + 8 * g + half * 128) * 2;
#pragma unroll
  for (int i = 0; i < 8; i++) acc[i] = (f32x4){0.f, 0.f, 0.f, 0.f};
#pragma unroll
  for (int ph = 0; ph < 4; ph++) {
    bf16x8 A[8], F[8];
#pragma unroll
    for (int a = 0; a < 8; a++) {
      u32x2 lo = *(const LAS u32x2*)(hp + (256 * a + 32 * ph) * 2), hi = *(const LAS u32x2*)(hp + (256 * a + 32 * ph) * 2 + 8);
      A[a] = __builtin_bit_cast(bf16x8, (u32x4){lo[0], lo[1], hi[0], hi[1]});
      F[a] = *(const LAS bf16x8*)(ur + (32 * ph - 256 * a) * 2);
    }
#pragma unroll
    for (int a = 0; a < 8; a++)
#pragma unroll
      for (int n = 0; n + a < 8; n++) acc[a + n] = __builtin_amdgcn_mfma_f32_16x16x32_bf16(A[a], F[n], acc[a + n], 0, 0, 0);
  }
}
DEV void conv_xchg_write(LAS char* lds, int wave, int lane, f32x4 (&acc)[8]) {
  const int half = wave & 1;
  LAS char* x = lds + L_XCH + wave * 4096 + lane * 16;
#pragma unroll
  for (int i = 0; i < 4; i++) {
    f32x4 a = acc[i], b = acc[4 + i];
    asm volatile("" : "+v"(a), "+v"(b));
    *(LAS f32x4*)(x + i * 1024) = half ? a : b;
  }
}
DEV void conv_finish(const Params& p, LAS char* lds, int ch0, int wave, int lane, f32x4 (&acc)[8]) {
  char* ws = p.ws;
  const int cs = wave >> 1, half = wave & 1, r = lane & 15, g = lane >> 4;
  LAS char* x = lds + L_XCH + (wave ^ 1) * 4096 + lane * 16;
  const float Dv = __uint_as_float(((unsigned)p.Dd[ch0 + cs]) << 16);
  LAS char* zst = lds + L_CONV + cs * CV_SZ + half * 2048;
  LAS char* ur = lds + L_CONV + cs * CV_SZ + CV_UR;
#pragma unroll
  for (int i = 0; i < 4; i++) {
    f32x4 ya = acc[i], yb = acc[4 + i];
    asm volatile("" : "+v"(ya), "+v"(yb));
    f32x4 y = (half ? yb : ya) + *(const LAS f32x4*)(x + i * 1024);
    const int t = 256 * (4 * half + i) + 16 * r + 4 * g;
    u32x2 u = *(const LAS u32x2*)(ur + (2044 - t) * 2);
    y[3] += __uint_as_float(u[0] << 16) * Dv; y[2] += __uint_as_float(u[0] & 0xffff0000u) * Dv;
    y[1] += __uint_as_float(u[1] << 16) * Dv; y[0] += __uint_as_float(u[1] & 0xffff0000u) * Dv;
    *(LAS u32x2*)(zst + (256 * i + 16 * r + 4 * g) * 2) = (u32x2){pk_bf16(y[0], y[1]), pk_bf16(y[2], y[3])};
  }
  WAIT_LGKM0();
  const __amdgpu_buffer_rsrc_t rz = __builtin_amdgcn_make_buffer_rsrc((void*)(ws + WS_ZT), 0, D * S * 2, 0x00020000);
#pragma unroll
  for (int i = 0; i < 2; i++) {
    u32x4 v = *(const LAS u32x4*)(zst + i * 1024 + lane * 16);
    __builtin_amdgcn_raw_buffer_store_b128(v, rz, (unsigned)((ch0 + cs) * 4096 + half * 2048 + i * 1024 + lane * 16), 0, 16);
  }
}

DEV void att_dma(const Params& p, LAS char* att, int rb, int j, int w, int nw, int lane) {
  char* ws = p.ws;
  const __amdgpu_buffer_rsrc_t rq = __builtin_amdgcn_make_buffer_rsrc((void*)(ws + WS_QKV), 0, 1728 * S * 2, 0x00020000);
  const int tok0 = 128 * (rb - 2);
  for (int i = w; i < 63; i += nw) {
    if (i < 54) {
      const int isv = i >= 27, ii = i - 27 * isv;
      const int lin = 64 * ii + lane, row = lin / 48, sl = lin - row * 48;
      int gch;
      if (!isv) gch = (((sl >> 2) ^ (row & 3)) << 2) | (sl & 3); else gch = sl ^ (row & 15);
      const unsigned off = (unsigned)(((isv ? 1152 : 576) + 36 * j + row) * 4096 + tok0 * 2 + gch * 16);
      __builtin_amdgcn_raw_ptr_buffer_load_lds(rq, att + (isv ? A_V : A_K) + ii * 1024, 16, off, 0, 0, 0);
    } else {
      const int ii = i - 54, row = 4 * ii + (lane >> 4), sl = lane & 15;
      const int gch = (((sl >> 2) ^ (row & 3)) << 2) | (sl & 3);
      const unsigned off = (unsigned)((36 * j + row) * 4096 + rb * 256 + gch * 16);
      __builtin_amdgcn_raw_ptr_buffer_load_lds(rq, att + A_Q + ii * 1024, 16, off, 0, 0, 0);
    }
  }
}
DEV bf16x8 cat44(s16x4 lo, s16x4 hi) { return __builtin_bit_cast(bf16x8, __builtin_shufflevector(lo, hi, 0, 1, 2, 3, 4, 5, 6, 7)); }
DEV s16x4 trr(LAS char* a) { return __builtin_amdgcn_ds_read_tr16_b64_v4i16((LAS s16x4*)a); }
#define MFMA32(a, b, c) __builtin_amdgcn_mfma_f32_32x32x16_bf16((a), (b), (c), 0, 0, 0)

DEV void att_wave(const Params& p, LAS char* att, int rb, int j, int wave, int lane) {
  char* ws = p.ws;
  const int hh = wave >> 2, qt = wave & 3;
  const int c = lane & 31, h = lane >> 5;
  const int kh = (lane >> 4) & 1, q4 = (lane >> 2) & 3, p4 = lane & 3, pi = ((p4 & 1) << 1) | (p4 >> 1);
  const int mmin = rb >= 2 ? 0 : (4 * (2 - rb) - qt);
  bf16x8 q0, q1;
  {
    const int row = 18 * hh + 8 * h + q4, rowt = 18 * hh + 16 + (q4 & 1);
    LAS char* a = att + A_Q + row * 256 + ((qt ^ (row & 3)) << 6) + kh * 32 + p4 * 8;
    q0 = cat44(trr(a), trr(a + 1024));
    s16x4 t = trr(att + A_Q + rowt * 256 + ((qt ^ (rowt & 3)) << 6) + kh * 32 + p4 * 8);
    u32x2 tt = __builtin_bit_cast(u32x2, t);
    q1 = __builtin_bit_cast(bf16x8, (u32x4){h ? 0u : tt[0], 0u, 0u, 0u});
  }
  LAS char *ak[4], *at[4], *av[4];
  {
    const int row = 18 * hh + 8 * h + q4, rowt = 18 * hh + 16 + (q4 & 1);
    const int vrow = c < 18 ? 18 * hh + c : ((c >= 20 && c < 28) ? 18 * hh : 36);
    const int vkey = vrow == 36 ? (hh ? 0 : 2) : (vrow & 15);
#pragma unroll
    for (int mm = 0; mm < 4; mm++) {
      const int kt = qt + mm;
      ak[mm] = att + A_K + row * 768 + ((kt >> 2) << 8) + (((kt & 3) ^ (row & 3)) << 6) + kh * 32 + pi * 8;
      at[mm] = att + A_K + rowt * 768 + ((kt >> 2) << 8) + (((kt & 3) ^ (rowt & 3)) << 6) + kh * 32 + pi * 8;
      av[mm] = att + A_V + vrow * 768 + ((kt >> 2) << 8) + (((4 * (kt & 3) + h) ^ vkey) << 4);
    }
  }
  auto scores = [&](int m) -> f32x16 {
    LAS char* a = ak[m & 3] + (m >> 2) * 256;
    bf16x8 k0 = cat44(trr(a), trr(a + 4 * 768));
    s16x4 t = trr(at[m & 3] + (m >> 2) * 256);
    f32x16 s;
#pragma unroll
    for (int i = 0; i < 16; i++) s[i] = 0.f;
    s = MFMA32(k0, q0, s);
    s = MFMA32(cat44(t, t), q1, s);
    return s;
  };
  const float NEG = -__builtin_inff();
  f32x16 sc[8];
  const int th = c - 8 * h;
  {
    f32x16 sd = scores(8);
    if (mmin == 0) { f32x16 sf = scores(0);
#pragma unroll
      for (int i = 0; i < 16; i++) sc[0][i] = (16 * (i >> 3) + (i & 7) > th) ? sf[i] : sd[i];
    } else {
#pragma unroll
      for (int i = 0; i < 16; i++) sc[0][i] = (16 * (i >> 3) + (i & 7) > th) ? NEG : sd[i];
    }
  }
#pragma unroll
  for (int m = 1; m < 8; m++) {
    if (m >= mmin) sc[m] = scores(m);
    else {
#pragma unroll
      for (int i = 0; i < 16; i++) sc[m][i] = NEG;
    }
  }
  float mx = NEG;
#pragma unroll
  for (int m = 0; m < 8; m++)
#pragma unroll
    for (int i = 0; i < 16; i += 2) mx = fmaxf(mx, fmaxf(sc[m][i], sc[m][i + 1]));
  { auto r = __builtin_amdgcn_permlane32_swap(__float_as_uint(mx), __float_as_uint(mx), false, false); mx = fmaxf(__uint_as_float(r[0]), __uint_as_float(r[1])); }
  f32x16 o;
#pragma unroll
  for (int i = 0; i < 16; i++) o[i] = 0.f;
  auto pv = [&](int m, u32x4 p0, u32x4 p1) {
    LAS char* a = av[m & 3] + (m >> 2) * 256;
    bf16x8 v0 = *(const LAS bf16x8*)a;
    bf16x8 v1 = *(const LAS bf16x8*)((LAS char*)((unsigned)(size_t)a ^ 32u));
    o = MFMA32(v0, __builtin_bit_cast(bf16x8, p0), o);
    o = MFMA32(v1, __builtin_bit_cast(bf16x8, p1), o);
  };
  auto expack = [&](const f32x16& s, u32x4& p0, u32x4& p1) {
#pragma unroll
    for (int k = 0; k < 4; k++) {
      p0[k] = pk_bf16(__builtin_amdgcn_exp2f(s[2 * k] - mx), __builtin_amdgcn_exp2f(s[2 * k + 1] - mx));
      p1[k] = pk_bf16(__builtin_amdgcn_exp2f(s[8 + 2 * k] - mx), __builtin_amdgcn_exp2f(s[8 + 2 * k + 1] - mx));
    }
  };
  {
    u32x4 p0, p1; expack(sc[0], p0, p1);
    u32x4 f0, f1, d0, d1;
#pragma unroll
    for (int k = 0; k < 4; k++) {
      const unsigned m0 = ((2 * k > th) ? 0xffffu : 0u) | ((2 * k + 1 > th) ? 0xffff0000u : 0u);
      const unsigned m1 = ((16 + 2 * k > th) ? 0xffffu : 0u) | ((16 + 2 * k + 1 > th) ? 0xffff0000u : 0u);
      f0[k] = p0[k] & m0; d0[k] = p0[k] & ~m0; f1[k] = p1[k] & m1; d1[k] = p1[k] & ~m1;
    }
    pv(8, d0, d1);
    if (mmin == 0) pv(0, f0, f1);
  }
#pragma unroll
  for (int m = 1; m < 8; m++) if (m >= mmin) { u32x4 p0, p1; expack(sc[m], p0, p1); pv(m, p0, p1); }
  float l;
  { auto r = __builtin_amdgcn_permlane32_swap(__float_as_uint(o[10]), __float_as_uint(o[10]), false, false); l = __uint_as_float(r[0]); }
  const float inv = __builtin_amdgcn_rcpf(l);
  u32x4 om = {pk_bf16(o[0] * inv, o[1] * inv), pk_bf16(o[2] * inv, o[3] * inv), pk_bf16(o[4] * inv, o[5] * inv), pk_bf16(o[6] * inv, o[7] * inv)};
  const __amdgpu_buffer_rsrc_t ro = __builtin_amdgcn_make_buffer_rsrc((void*)(ws + WS_OM), 0, 128 * 16 * 1024, 0x00020000);
  const int qt16 = 8 * rb + 2 * qt + (c >> 4);
  __builtin_amdgcn_raw_buffer_store_b128(om, ro, (unsigned)(((qt16 * 16 + j) * 64 + (c & 15) + 16 * (2 * hh + h)) * 16), 0, 16);
  if (h == 0) __hip_atomic_store((GAS unsigned*)(ws + WS_OT) + (2 * j + hh) * S + 128 * rb + 32 * qt + c, pk_bf16(o[8] * inv, o[9] * inv), __ATOMIC_RELAXED, __HIP_MEMORY_SCOPE_AGENT);
}

__global__ void __launch_bounds__(512, 2) k2(Params p) {
  extern __shared__ __attribute__((aligned(16))) char smem[];
  LAS char* lds = (LAS char*)smem;
  char* ws = p.ws;
  const int wave = __builtin_amdgcn_readfirstlane(threadIdx.x >> 6), lane = threadIdx.x & 63, tid = threadIdx.x;
  const int b = blockIdx.x, xcd = b & 7, loc = b >> 3;
  const int rb = loc >= 28 ? ((loc - 28) >> 1) : 2 + (loc >> 1), j = 2 * xcd + (loc & 1);
  STAMPIT(0);
  LAS char* att = lds + L_ATT;
  att_dma(p, att, rb, j, wave, 8, lane);
  if (tid < 48) *(LAS u32x4*)(att + A_ONES + tid * 16) = (u32x4){0x3f803f80u, 0x3f803f80u, 0x3f803f80u, 0x3f803f80u};
  const int nround = loc >= 28 ? 2 : 1;
  for (int rd = 0; rd < nround; rd++) {
    const int ch0 = loc >= 28 ? 448 + 4 * (xcd * 4 + loc - 28) + 2 * rd : 2 * (xcd * 28 + loc);
    conv_stage(p, lds, ch0, tid);
    __syncthreads();
    f32x4 acc[8];
    if (wave < 4) { conv_mfma(lds, wave, lane, acc); conv_xchg_write(lds, wave, lane, acc); }
    __syncthreads();
    if (wave < 4) conv_finish(p, lds, ch0, wave, lane, acc);
    __syncthreads();
  }
  STAMPIT(1);
  WAIT_VM(0);
  __syncthreads();
  STAMPIT(2);
  att_wave(p, att, rb, j, wave, lane);
  STAMPIT(3);
}

constexpr int WO_RS = 160;
constexpr int L_WO = 0;
constexpr int L_Z = 92160;
DEV void wo_dma(const Params& p, LAS char* wl, int ns, int w, int nw, int lane) {
  const __amdgpu_buffer_rsrc_t rw = __builtin_amdgcn_make_buffer_rsrc((void*)p.wo, 0, D * D * 2, 0x00020000);
  for (int i = w; i < 90; i += nw) {
    const int lin = 64 * i + lane, row = lin / 10, ch = lin - row * 10;
    const unsigned off = (unsigned)(row * 1152 + ns * 144 + (ch > 8 ? 8 : ch) * 16);
    __builtin_amdgcn_raw_ptr_buffer_load_lds(rw, wl + i * 1024, 16, off, 0, 0, 0);
  }
}
DEV void p3_wave(const Params& p, LAS char* wl, int qb, int ns, int w, int lane, bool loads_only_dummy) {
  char* ws = p.ws;
  const int r = lane & 15, g = lane >> 4, q4 = (lane >> 2) & 3, p4 = lane & 3;
  const int qt16 = 4 * qb + w;
  const __amdgpu_buffer_rsrc_t ro = __builtin_amdgcn_make_buffer_rsrc((void*)(ws + WS_OM), 0, 128 * 16 * 1024, 0x00020000);
  const __amdgpu_buffer_rsrc_t rt = __builtin_amdgcn_make_buffer_rsrc((void*)(ws + WS_OT), 0, 32 * S * 4, 0x00020000);
  u32x4 of[18];
#pragma unroll
  for (int s = 0; s < 16; s++) of[s] = __builtin_amdgcn_raw_buffer_load_b128(ro, (unsigned)(lane * 16), (unsigned)((qt16 * 16 + s) * 1024), 16);
#pragma unroll
  for (int s = 0; s < 2; s++)
#pragma unroll
    for (int w4 = 0; w4 < 4; w4++) of[16 + s][w4] = __builtin_amdgcn_raw_buffer_load_b32(rt, (unsigned)(((4 * g + w4) * S + 16 * qt16 + r) * 4), (unsigned)(16 * s * S * 4), 16);
  WAIT_LGKM0();
  barrier_raw();
  f32x4 acc[5];
#pragma unroll
  for (int i = 0; i < 5; i++) acc[i] = (f32x4){0.f, 0.f, 0.f, 0.f};
  LAS char* am = wl + (18 * (g >> 1) + 4 * (g & 1) + q4) * WO_RS + p4 * 8;
  LAS char* atl = wl + (18 * (4 * g + (q4 >> 1)) + 16 + (q4 & 1)) * WO_RS + p4 * 8;
#pragma unroll
  for (int s = 0; s < 18; s++) {
#pragma unroll
    for (int nt = 0; nt < 5; nt++) {
      LAS char* a = (s < 16 ? am + 36 * s * WO_RS : atl + 18 * 16 * (s - 16) * WO_RS) + nt * 32;
      const int tstep = s < 16 ? 8 * WO_RS : 36 * WO_RS;
      bf16x8 A = cat44(trr(a), trr(a + tstep));
      acc[nt] = __builtin_amdgcn_mfma_f32_16x16x32_bf16(A, __builtin_bit_cast(bf16x8, of[s]), acc[nt], 0, 0, 0);
    }
  }
  GAS u32x2* out = (GAS u32x2*)(p.out + (size_t)(16 * qt16 + r) * 1152 + ns * 72 + 4 * g);
#pragma unroll
  for (int nt = 0; nt < 5; nt++)
    if (nt < 4 || g < 2) out[nt * 4] = (u32x2){pk_bf16(acc[nt][0], acc[nt][1]), pk_bf16(acc[nt][2], acc[nt][3])};
}
DEV void b3_load(const Params& p, LAS char* zl, int tb, int cb, int w, int nw, int lane) {
  char* ws = p.ws;
  const __amdgpu_buffer_rsrc_t rz = __builtin_amdgcn_make_buffer_rsrc((void*)(ws + WS_ZT), 0, D * S * 2, 0x00020000);
  for (int i = w; i < 8; i += nw) {
    const int d = 64 * cb + 8 * i + (lane >> 3);
    __builtin_amdgcn_raw_ptr_buffer_load_lds(rz, zl + i * 1024, 16, (unsigned)(d * 4096 + tb * 128 + (((lane & 7) ^ i) << 4)), 0, 0, 0);
  }
}
DEV void b3_do(const Params& p, LAS char* zl, int tb, int cb, int w, int nw, int lane) {
  const GAS u32x4* qp = (const GAS u32x4*)p.q;
  for (int i = w; i < 8; i += nw) {
    const int tl = 8 * i + (lane >> 3), c8 = lane & 7, t = 64 * tb + tl;
    u32x4 qv = qp[t * 72 + 8 * cb + c8];
    u32x4 o;
#pragma unroll
    for (int e = 0; e < 4; e++) {
      const unsigned z0 = *(const LAS u16*)(zl + (8 * c8 + 2 * e) * 128 + ((i ^ c8) << 4) + (lane >> 3) * 2);
      const unsigned z1 = *(const LAS u16*)(zl + (8 * c8 + 2 * e + 1) * 128 + ((i ^ c8) << 4) + (lane >> 3) * 2);
      f32x2 qq = unpk(qv[e]);
      o[e] = pk_bf16(qq.x * __uint_as_float(z0 << 16), qq.y * __uint_as_float(z1 << 16));
    }
    *(GAS u32x4*)(p.out + (size_t)t * 1152 + 576 + 64 * cb + 8 * c8) = o;
  }
}
__global__ void __launch_bounds__(512, 2) k3(Params p) {
  extern __shared__ __attribute__((aligned(16))) char smem[];
  LAS char* lds = (LAS char*)smem;
  char* ws = p.ws;
  const int wave = __builtin_amdgcn_readfirstlane(threadIdx.x >> 6), lane = threadIdx.x & 63;
  const int b = blockIdx.x, xcd = b & 7, loc = b >> 3;
  const int qb = 4 * xcd + (loc & 3), ns = loc >> 2;
  STAMPIT(0);
  wo_dma(p, lds + L_WO, ns, wave, 8, lane);
  if (wave >= 4) {
    b3_load(p, lds + L_Z, b / 9, b % 9, wave - 4, 4, lane);
    if (b < 32) b3_load(p, lds + L_Z + 8192, (256 + b) / 9, (256 + b) % 9, wave - 4, 4, lane);
    WAIT_VM(0);
    barrier_raw();
    b3_do(p, lds + L_Z, b / 9, b % 9, wave - 4, 4, lane);
    if (b < 32) b3_do(p, lds + L_Z + 8192, (256 + b) / 9, (256 + b) % 9, wave - 4, 4, lane);
  } else {
    WAIT_VM(0);
    p3_wave(p, lds + L_WO, qb, ns, wave, lane, false);
  }
  STAMPIT(3);
}

extern "C" void kernel_launch(void* const* d_in, const int* in_sizes, int n_in, void* d_out, int out_size, void* d_ws, size_t ws_size, hipStream_t stream) {
  Params p;
  p.x = (const u16*)d_in[0]; p.wqkv = (const u16*)d_in[1]; p.wo = (const u16*)d_in[2]; p.q = (const u16*)d_in[3]; p.k = (const u16*)d_in[4]; p.v = (const u16*)d_in[5];
  p.sk = (const u16*)d_in[6]; p.hk = (const u16*)d_in[7]; p.Dd = (const u16*)d_in[8]; p.out = (u16*)d_out; p.ws = (char*)d_ws;
  constexpr int LDS_BYTES = 160 * 1024;
  (void)hipFuncSetAttribute((const void*)k1, hipFuncAttributeMaxDynamicSharedMemorySize, LDS_BYTES);
  (void)hipFuncSetAttribute((const void*)k2, hipFuncAttributeMaxDynamicSharedMemorySize, LDS_BYTES);
  (void)hipFuncSetAttribute((const void*)k3, hipFuncAttributeMaxDynamicSharedMemorySize, LDS_BYTES);
  k1<<<256, 512, LDS_BYTES, stream>>>(p);
  k2<<<256, 512, LDS_BYTES, stream>>>(p);
  k3<<<256, 512, LDS_BYTES, stream>>>(p);
}
```

```cpp
#include <hip/hip_runtime.h>
typedef unsigned short u16;
typedef unsigned long long u64;
typedef __bf16 bf16x8 __attribute__((ext_vector_type(8)));
typedef __bf16 bf16x2 __attribute__((ext_vector_type(2)));
typedef short s16x4 __attribute__((ext_vector_type(4)));
typedef float f32x2 __attribute__((ext_vector_type(2)));
typedef float f32x4 __attribute__((ext_vector_type(4)));
typedef float f32x16 __attribute__((ext_vector_type(16)));
typedef unsigned u32x2 __attribute__((ext_vector_type(2)));
typedef unsigned u32x4 __attribute__((ext_vector_type(4)));
#define GAS __attribute__((address_space(1)))
#define LAS __attribute__((address_space(3)))
#define DEV __device__ __forceinline__

constexpr int S = 2048, D = 576;
constexpr size_t WS_QKV = 0;
constexpr size_t WS_KVT = 8u << 20;
constexpr size_t WS_ZT = 11u << 20;
constexpr size_t WS_OM = 14u << 20;
constexpr size_t WS_OT = 17u << 20;
constexpr size_t WS_FLAG = 18u << 20;
constexpr int F_GEN = 0, F_SEEN = 128, F_KV = 512, F_Q = 1024, F_Z = 2048, F_O = 4096;
constexpr size_t WS_STAMP = 32u << 20;
#define STAMPW(i, w)
#define STAMP2(i, w)

DEV unsigned pk_bf16(float a, float b) { f32x2 v = {a, b}; bf16x2 r = __builtin_convertvector(v, bf16x2); return __builtin_bit_cast(unsigned, r); }
DEV f32x2 unpk(unsigned w) { f32x2 r; r.x = __uint_as_float(w << 16); r.y = __uint_as_float(w & 0xffff0000u); return r; }
DEV void barrier_raw() { __builtin_amdgcn_s_barrier(); }
#define WAIT_VM(n) asm volatile("s_waitcnt vmcnt(" #n ")" ::: "memory")
#define WAIT_LGKM0() asm volatile("s_waitcnt lgkmcnt(0)" ::: "memory")
#define SCHED_FENCE() __builtin_amdgcn_sched_barrier(0)
#define FENCE() do { asm volatile("" ::: "memory"); __builtin_amdgcn_sched_barrier(0); } while (0)
#define INTERLEAVE12() do { _Pragma("unroll") for (int i_ = 0; i_ < 12; i_++) { __builtin_amdgcn_sched_group_barrier(0x008, 1, 0); __builtin_amdgcn_sched_group_barrier(0x100, 1, 0); } __builtin_amdgcn_sched_group_barrier(0x008, 4, 0); } while (0)
constexpr int AUX_SC1 = 16;
constexpr int SPIN_MAX = 20000;

struct Params { const u16 *x, *wqkv, *wo, *q, *k, *v, *sk, *hk, *Dd; u16* out; char* ws; };

constexpr int STG = 32768;
constexpr int CV_HP = 4160, CV_UR = 4 * CV_HP, CV_SZ = CV_UR + 4576;
constexpr int L_CONV = 0;
constexpr int L_XCH = 2 * CV_SZ;
constexpr int L_MISC = 58880;
constexpr int L_QRDY = L_MISC + 32, L_TAG = L_MISC + 40;
constexpr int L_RING = 65536;
constexpr int L_CONV2 = 65536, L_XCH2 = L_CONV2 + 2 * CV_SZ;
constexpr int L_ATT = 98304;
constexpr int A_K = 0, A_V = 36 * 768, A_ONES = 72 * 768, A_Q = 73 * 768;
constexpr int WO_RS = 160, L_WO = 0;
constexpr int L_Z = L_ATT;

DEV GAS u64* flagp(const Params& p, int idx) { return (GAS u64*)(p.ws + WS_FLAG) + idx * 16; }
DEV u64 ld_flag(GAS u64* a) { return __hip_atomic_load(a, __ATOMIC_RELAXED, __HIP_MEMORY_SCOPE_AGENT); }
DEV void st_flag(GAS u64* a, u64 v) { __hip_atomic_store(a, v, __ATOMIC_RELAXED, __HIP_MEMORY_SCOPE_AGENT); }
DEV u64 mk_tag(u64 gen) { return gen * 6364136223846793005ull + 1442695040888963407ull; }
template <int SLEEP = 4>
DEV void poll_block(const Params& p, int idx, bool active, u64 tag) {
  GAS u64* a = flagp(p, idx);
#pragma unroll 1
  for (int it = 0; it < SPIN_MAX; it++) {
    u64 v = tag;
    if (active) v = ld_flag(a);
    if (__builtin_amdgcn_ballot_w64(v != tag) == 0) break;
    __builtin_amdgcn_s_sleep(SLEEP);
  }
  asm volatile("" ::: "memory");
}
DEV u64 lds_tag(LAS char* lds) {
  const u32x2 t = *(const LAS u32x2*)(lds + L_TAG);
  return ((u64)(unsigned)__builtin_amdgcn_readfirstlane(t[1]) << 32) | (unsigned)__builtin_amdgcn_readfirstlane(t[0]);
}
DEV void lds_wait(LAS char* w) {
#pragma unroll 1
  for (int it = 0; it < (1 << 24); it++) {
    if (*(volatile LAS unsigned*)w == 1u) break;
    __builtin_amdgcn_s_sleep(1);
  }
  asm volatile("" ::: "memory");
}

DEV void p1_dma_stage(const __amdgpu_buffer_rsrc_t& rx, const __amdgpu_buffer_rsrc_t& rw, LAS char* lds, int s, int hw, unsigned vx, unsigned vw0, unsigned vw1) {
  LAS char* xs = lds + L_RING + (s % 3) * STG; LAS char* wsl = xs + 16384;
#pragma unroll
  for (int u = 0; u < 4; u++)
    __builtin_amdgcn_raw_ptr_buffer_load_lds(rx, xs + (4 * hw + u) * 1024, 16, vx, (4 * hw + u) * 8 * 1152 + s * 128, 0, 0);
#pragma unroll
  for (int u = 0; u < 4; u++)
    __builtin_amdgcn_raw_ptr_buffer_load_lds(rw, wsl + (4 * hw + u) * 1024, 16, (u & 2) ? vw1 : vw0, (64 * s + (4 * hw + u) * 4) * 3456, 0, 0);
}

struct B1Regs { u32x4 kr[5], vr[2], sw[4]; };
DEV void b1_load(const Params& p, B1Regs& r, int tile, int tid4) {
  const int tb = tile / 9, cb = tile - tb * 9, tp = tid4 >> 3, c8 = tid4 & 7;
  const GAS u32x4* kp = (const GAS u32x4*)p.k; const GAS u32x4* vp = (const GAS u32x4*)p.v; const GAS u32x4* skp = (const GAS u32x4*)p.sk;
  const int t0 = 64 * tb + 2 * tp, ch = 8 * cb + c8;
#pragma unroll
  for (int i = 0; i < 5; i++) { const int t = t0 - 3 + i; r.kr[i] = kp[(t < 0 ? 0 : t) * 72 + ch]; }
#pragma unroll
  for (int i = 0; i < 2; i++) r.vr[i] = vp[(t0 + i) * 72 + ch];
#pragma unroll
  for (int i = 0; i < 4; i++) r.sw[i] = skp[ch * 4 + i];
}
DEV void b1_compute(B1Regs& r, LAS char* stg, int tile, int tid4) {
  const int tb = tile / 9, tp = tid4 >> 3, c8 = tid4 & 7;
  const int t0 = 64 * tb + 2 * tp;
#pragma unroll
  for (int i = 0; i < 3; i++) if (t0 - 3 + i < 0) r.kr[i] = (u32x4){0, 0, 0, 0};
  LAS char* o = stg + c8 * 1024 + ((((31 - tp) >> 2) ^ c8) << 4) + ((31 - tp) & 3) * 4;
#pragma unroll
  for (int pr = 0; pr < 4; pr++) {
    const f32x2 a0 = unpk(r.sw[pr][0]), a1 = unpk(r.sw[pr][1]), b0 = unpk(r.sw[pr][2]), b1 = unpk(r.sw[pr][3]);
    const f32x2 w0 = {a0.x, b0.x}, w1 = {a0.y, b0.y}, w2 = {a1.x, b1.x}, w3 = {a1.y, b1.y};
    f32x2 kf[5];
#pragma unroll
    for (int i = 0; i < 5; i++) kf[i] = unpk(r.kr[i][pr]);
    f32x2 s0 = kf[0] * w3 + kf[1] * w2; s0 += kf[2] * w1; s0 += kf[3] * w0; s0 = s0 * unpk(r.vr[0][pr]);
    f32x2 s1 = kf[1] * w3 + kf[2] * w2; s1 += kf[3] * w1; s1 += kf[4] * w0; s1 = s1 * unpk(r.vr[1][pr]);
    *(LAS unsigned*)(o + (2 * pr) * 128) = pk_bf16(s1.x, s0.x);
    *(LAS unsigned*)(o + (2 * pr + 1) * 128) = pk_bf16(s1.y, s0.y);
  }
}
DEV void b1_store(const Params& p, LAS char* stg, int tile, int w4, int lane) {
  const int tb = tile / 9, cb = tile - tb * 9;
  const __amdgpu_buffer_rsrc_t rk = __builtin_amdgcn_make_buffer_rsrc((void*)(p.ws + WS_KVT), 0, D * S * 2, 0x00020000);
#pragma unroll
  for (int u = 0; u < 2; u++) {
    const int ii = w4 * 2 + u, ch = 8 * ii + (lane >> 3);
    u32x4 v = *(const LAS u32x4*)(stg + ch * 128 + (lane & 7) * 16);
    __builtin_amdgcn_raw_buffer_store_b128(v, rk, (unsigned)((64 * cb + ch) * 4096 + (1984 - 64 * tb) * 2 + (((lane & 7) ^ ii) << 4)), 0, AUX_SC1);
  }
}

struct HkRegs { u32x4 c0[3]; u32x2 c1[3]; };
DEV void hk_load(const Params& p, HkRegs& r, int ch0, int tid4) {
  const GAS u32x4* hk = (const GAS u32x4*)p.hk;
#pragma unroll
  for (int cs = 0; cs < 2; cs++) {
    const int i0 = tid4 - 2, i1 = tid4 - 1;
    r.c0[cs] = hk[(ch0 + cs) * 256 + (i0 < 0 ? 0 : i0)];
    r.c1[cs] = *(const GAS u32x2*)(hk + (ch0 + cs) * 256 + (i1 < 0 ? 0 : i1));
  }
  const int cs = (tid4 >> 1) & 1, i0 = 254 + (tid4 & 1);
  r.c0[2] = hk[(ch0 + cs) * 256 + i0];
  r.c1[2] = *(const GAS u32x2*)(hk + (ch0 + cs) * 256 + 255);
}
DEV void hk_put(LAS char* b, u32x4 c0, u32x2 c1) {
  const unsigned a0 = __builtin_amdgcn_alignbit(c0[1], c0[0], 16), a1 = __builtin_amdgcn_alignbit(c0[2], c0[1], 16), a2 = __builtin_amdgcn_alignbit(c0[3], c0[2], 16),
                 a3 = __builtin_amdgcn_alignbit(c1[0], c0[3], 16), a4 = __builtin_amdgcn_alignbit(c1[1], c1[0], 16);
  *(LAS u32x4*)(b) = (u32x4){a0, a1, a2, a3};
  *(LAS u32x4*)(b + CV_HP) = (u32x4){c0[1], c0[2], c0[3], c1[0]};
  *(LAS u32x4*)(b + 2 * CV_HP) = (u32x4){a1, a2, a3, a4};
  *(LAS u32x4*)(b + 3 * CV_HP) = (u32x4){c0[2], c0[3], c1[0], c1[1]};
}
DEV void hk_write_main(HkRegs& r, LAS char* cv, int tid4, int cs) {
  const u32x4 z4 = {0, 0, 0, 0};
  const u32x2 z2 = {0, 0};
  u32x4 c0 = r.c0[cs]; u32x2 c1 = r.c1[cs];
  if (tid4 < 2) c0 = z4;
  if (tid4 < 1) c1 = z2;
  hk_put(cv + cs * CV_SZ + tid4 * 16, c0, c1);
}
DEV void hk_write_edge(HkRegs& r, LAS char* cv, int tid4) {
  const u32x4 z4 = {0, 0, 0, 0};
  const u32x2 z2 = {0, 0};
  if (tid4 < 4) hk_put(cv + ((tid4 >> 1) & 1) * CV_SZ + (256 + (tid4 & 1)) * 16, r.c0[2], (tid4 & 1) ? z2 : r.c1[2]);
  if (tid4 < 60) { const int cs = tid4 >= 30; *(LAS u32x4*)(cv + cs * CV_SZ + CV_UR + (256 + tid4 - 30 * cs) * 16) = z4; }
}
DEV void ur_dma(const Params& p, LAS char* cv, int ch0, int w4, int lane) {
  const __amdgpu_buffer_rsrc_t rk = __builtin_amdgcn_make_buffer_rsrc((void*)(p.ws + WS_KVT), 0, D * S * 2, 0x00020000);
  const int cs = w4 >> 1;
#pragma unroll
  for (int u = 0; u < 2; u++) {
    const int pc = (w4 & 1) * 2 + u;
    __builtin_amdgcn_raw_ptr_buffer_load_lds(rk, cv + cs * CV_SZ + CV_UR + pc * 1024, 16, (unsigned)(lane * 16), (ch0 + cs) * 4096 + pc * 1024, 0, AUX_SC1);
  }
}
DEV void conv_run(const Params& p, LAS char* cv, LAS char* xch, LAS char* xfl, int ch0, int w4, int lane) {
  const int cs = w4 >> 1, half = w4 & 1;
  const int r = lane & 15, g = lane >> 4;
  unsigned dbits = p.Dd[ch0 + cs];
  FENCE();
  f32x4 acc[8];
  {
    LAS char* hp = cv + cs * CV_SZ + (r & 3) * CV_HP + ((r & ~3) + 8 * g + half * 128) * 2;
    LAS char* ur = cv + cs * CV_SZ + CV_UR + (2032 - 16 * r + 8 * g + half * 128) * 2;
#pragma unroll
    for (int i = 0; i < 8; i++) acc[i] = (f32x4){0.f, 0.f, 0.f, 0.f};
#pragma unroll
    for (int ph = 0; ph < 4; ph++) {
      bf16x8 A[8], F[8];
#pragma unroll
      for (int a = 0; a < 8; a++) {
        u32x2 lo = *(const LAS u32x2*)(hp + (256 * a + 32 * ph) * 2), hi = *(const LAS u32x2*)(hp + (256 * a + 32 * ph) * 2 + 8);
        A[a] = __builtin_bit_cast(bf16x8, (u32x4){lo[0], lo[1], hi[0], hi[1]});
        F[a] = *(const LAS bf16x8*)(ur + (32 * ph - 256 * a) * 2);
      }
#pragma unroll
      for (int a = 0; a < 8; a++)
#pragma unroll
        for (int n = 0; n + a < 8; n++) acc[a + n] = __builtin_amdgcn_mfma_f32_16x16x32_bf16(A[a], F[n], acc[a + n], 0, 0, 0);
    }
  }
  STAMPW(5, 0);
  {
    LAS char* x = xch + w4 * 4096 + lane * 16;
#pragma unroll
    for (int i = 0; i < 4; i++) {
      f32x4 a = acc[i], b = acc[4 + i];
      asm volatile("" : "+v"(a), "+v"(b));
      *(LAS f32x4*)(x + i * 1024) = half ? a : b;
    }
    WAIT_LGKM0();
    if (lane == 0) *(volatile LAS unsigned*)(xfl + w4 * 4) = 1u;
    lds_wait(xfl + (w4 ^ 1) * 4);
  }
  LAS char* x = xch + (w4 ^ 1) * 4096 + lane * 16;
  const float Dv = __uint_as_float(dbits << 16);
  LAS char* zst = cv + cs * CV_SZ + half * 2048;
  LAS char* ur = cv + cs * CV_SZ + CV_UR;
#pragma unroll
  for (int i = 0; i < 4; i++) {
    f32x4 ya = acc[i], yb = acc[4 + i];
    asm volatile("" : "+v"(ya), "+v"(yb));
    f32x4 y = (half ? yb : ya) + *(const LAS f32x4*)(x + i * 1024);
    const int t = 256 * (4 * half + i) + 16 * r + 4 * g;
    u32x2 u = *(const LAS u32x2*)(ur + (2044 - t) * 2);
    y[3] += __uint_as_float(u[0] << 16) * Dv; y[2] += __uint_as_float(u[0] & 0xffff0000u) * Dv;
    y[1] += __uint_as_float(u[1] << 16) * Dv; y[0] += __uint_as_float(u[1] & 0xffff0000u) * Dv;
    *(LAS u32x2*)(zst + (256 * i + 16 * r + 4 * g) * 2) = (u32x2){pk_bf16(y[0], y[1]), pk_bf16(y[2], y[3])};
  }
  WAIT_LGKM0();
  const __amdgpu_buffer_rsrc_t rz = __builtin_amdgcn_make_buffer_rsrc((void*)(p.ws + WS_ZT), 0, D * S * 2, 0x00020000);
#pragma unroll
  for (int i = 0; i < 2; i++) {
    u32x4 v = *(const LAS u32x4*)(zst + i * 1024 + lane * 16);
    __builtin_amdgcn_raw_buffer_store_b128(v, rz, (unsigned)((ch0 + cs) * 4096 + half * 2048 + i * 1024 + lane * 16), 0, AUX_SC1);
  }
}

struct AttOff { unsigned k[3], v[3], q; };
DEV void att_off(AttOff& o, int rb, int j, int lane) {
  const int tok2 = 256 * (rb - 2);
#pragma unroll
  for (int t = 0; t < 3; t++) {
    const int lin = 64 * t + lane, row = lin / 48, sl = lin - row * 48;
    o.k[t] = (unsigned)((576 + 36 * j + row) * 4096 + tok2 + (((((sl >> 2) ^ row) << 2) | (sl & 3)) << 4));
    o.v[t] = (unsigned)((1152 + 36 * j + row) * 4096 + tok2 + ((sl ^ row) << 4));
  }
  const int row = lane >> 4, sl = lane & 15;
  o.q = (unsigned)((36 * j + row) * 4096 + rb * 256 + (((((sl >> 2) ^ row) << 2) | (sl & 3)) << 4));
}
DEV void att_dma(const Params& p, LAS char* att, const AttOff& o, int w) {
  const __amdgpu_buffer_rsrc_t rq = __builtin_amdgcn_make_buffer_rsrc((void*)(p.ws + WS_QKV), 0, 1728 * S * 2, 0x00020000);
#pragma unroll
  for (int u = 0; u < 3; u++) {
    const int a = w + 4 * u;
    if (u < 2 || w == 0) {
#pragma unroll
      for (int t = 0; t < 3; t++) __builtin_amdgcn_raw_ptr_buffer_load_lds(rq, att + A_K + (3 * a + t) * 1024, 16, o.k[t], a * 16384, 0, AUX_SC1);
    }
    const int av = u < 2 ? a : 8;
    if (u < 2 || w == 1) {
#pragma unroll
      for (int t = 0; t < 3; t++) __builtin_amdgcn_raw_ptr_buffer_load_lds(rq, att + A_V + (3 * av + t) * 1024, 16, o.v[t] ^ ((av & 3) << 6), av * 16384, 0, AUX_SC1);
    }
  }
  const int q0 = w == 0 ? 0 : (w == 1 ? 1 : (w == 2 ? 2 : 6)), qn = w < 2 ? 1 : (w == 2 ? 4 : 3);
#pragma unroll
  for (int u = 0; u < 4; u++)
    if (u < qn) __builtin_amdgcn_raw_ptr_buffer_load_lds(rq, att + A_Q + (q0 + u) * 1024, 16, o.q, (q0 + u) * 16384, 0, AUX_SC1);
}
DEV void wo_dma(const Params& p, LAS char* wl, int ns, int w, int lane) {
  const __amdgpu_buffer_rsrc_t rw = __builtin_amdgcn_make_buffer_rsrc((void*)p.wo, 0, D * D * 2, 0x00020000);
  const int lin = 64 * w + lane;
  int row = lin / 10, ch = lin - row * 10;
  unsigned off = (unsigned)(row * 1152 + ns * 144 + ch * 16);
#pragma unroll
  for (int u = 0; u < 12; u++) {
    if (u < 11 || w < 2) __builtin_amdgcn_raw_ptr_buffer_load_lds(rw, wl + (w + 8 * u) * 1024, 16, off, 0, 0, 0);
    ch += 2; off += 51 * 1152 + 32;
    if (ch >= 10) { ch -= 10; off += 1152 - 160; }
  }
}
DEV bf16x8 cat44(s16x4 lo, s16x4 hi) { return __builtin_bit_cast(bf16x8, __builtin_shufflevector(lo, hi, 0, 1, 2, 3, 4, 5, 6, 7)); }
DEV s16x4 trr(LAS char* a) { return __builtin_amdgcn_ds_read_tr16_b64_v4i16((LAS s16x4*)a); }
#define MFMA32(a, b, c) __builtin_amdgcn_mfma_f32_32x32x16_bf16((a), (b), (c), 0, 0, 0)

DEV void att_wave(const Params& p, LAS char* att, LAS char* wl, int ns, int rb, int j, int wave, int lane, int zidx, u64 tag) {
  const int hh = wave >> 2, qt = wave & 3;
  const int c = lane & 31, h = lane >> 5;
  const int kh = (lane >> 4) & 1, q4 = (lane >> 2) & 3, p4 = lane & 3, pi = ((p4 & 1) << 1) | (p4 >> 1);
  const int mmin = rb >= 2 ? 0 : (4 * (2 - rb) - qt);
  bf16x8 q0, q1;
  {
    const int row = 18 * hh + 8 * h + q4, rowt = 18 * hh + 16 + (q4 & 1);
    LAS char* a = att + A_Q + row * 256 + ((qt ^ (row & 3)) << 6) + kh * 32 + p4 * 8;
    q0 = cat44(trr(a), trr(a + 1024));
    s16x4 t = trr(att + A_Q + rowt * 256 + ((qt ^ (rowt & 3)) << 6) + kh * 32 + p4 * 8);
    u32x2 tt = __builtin_bit_cast(u32x2, t);
    q1 = __builtin_bit_cast(bf16x8, (u32x4){h ? 0u : tt[0], 0u, 0u, 0u});
  }
  LAS char *ak[4], *at[4], *av[4];
  {
    const int row = 18 * hh + 8 * h + q4, rowt = 18 * hh + 16 + (q4 & 1);
    const int vrow = c < 18 ? 18 * hh + c : ((c >= 20 && c < 28) ? 18 * hh : 36);
    const int vkey = vrow == 36 ? (hh ? 0 : 2) : (vrow & 15);
#pragma unroll
    for (int mm = 0; mm < 4; mm++) {
      const int kt = qt + mm;
      ak[mm] = att + A_K + row * 768 + ((kt >> 2) << 8) + (((kt & 3) ^ (row & 3)) << 6) + kh * 32 + pi * 8;
      at[mm] = att + A_K + rowt * 768 + ((kt >> 2) << 8) + (((kt & 3) ^ (rowt & 3)) << 6) + kh * 32 + pi * 8;
      av[mm] = att + A_V + vrow * 768 + ((kt >> 2) << 8) + (((4 * (kt & 3) + h) ^ vkey) << 4);
    }
  }
  auto scores = [&](int m) -> f32x16 {
    LAS char* a = ak[m & 3] + (m >> 2) * 256;
    bf16x8 k0 = cat44(trr(a), trr(a + 4 * 768));
    s16x4 t = trr(at[m & 3] + (m >> 2) * 256);
    f32x16 s;
#pragma unroll
    for (int i = 0; i < 16; i++) s[i] = 0.f;
    s = MFMA32(k0, q0, s);
    s = MFMA32(cat44(t, t), q1, s);
    return s;
  };
  const float NEG = -__builtin_inff();
  f32x16 sc[8];
  const int th = c - 8 * h;
  {
    f32x16 sd = scores(8);
    if (mmin == 0) { f32x16 sf = scores(0);
#pragma unroll
      for (int i = 0; i < 16; i++) sc[0][i] = (16 * (i >> 3) + (i & 7) > th) ? sf[i] : sd[i];
    } else {
#pragma unroll
      for (int i = 0; i < 16; i++) sc[0][i] = (16 * (i >> 3) + (i & 7) > th) ? NEG : sd[i];
    }
  }
#pragma unroll
  for (int m = 1; m < 8; m++) {
    if (m >= mmin) sc[m] = scores(m);
    else {
#pragma unroll
      for (int i = 0; i < 16; i++) sc[m][i] = NEG;
    }
  }
  float mx = NEG;
#pragma unroll
  for (int m = 0; m < 8; m++)
#pragma unroll
    for (int i = 0; i < 16; i += 2) mx = fmaxf(mx, fmaxf(sc[m][i], sc[m][i + 1]));
  { auto r = __builtin_amdgcn_permlane32_swap(__float_as_uint(mx), __float_as_uint(mx), false, false); mx = fmaxf(__uint_as_float(r[0]), __uint_as_float(r[1])); }
  FENCE();
  if (zidx >= 0) {
    WAIT_VM(0);
    if (lane == 0) st_flag(flagp(p, zidx), tag);
  }
  wo_dma(p, wl, ns, wave, lane);
  FENCE();
  f32x16 o;
#pragma unroll
  for (int i = 0; i < 16; i++) o[i] = 0.f;
  auto pv = [&](int m, u32x4 p0, u32x4 p1) {
    LAS char* a = av[m & 3] + (m >> 2) * 256;
    bf16x8 v0 = *(const LAS bf16x8*)a;
    bf16x8 v1 = *(const LAS bf16x8*)((LAS char*)((unsigned)(size_t)a ^ 32u));
    o = MFMA32(v0, __builtin_bit_cast(bf16x8, p0), o);
    o = MFMA32(v1, __builtin_bit_cast(bf16x8, p1), o);
  };
  auto expack = [&](const f32x16& s, u32x4& p0, u32x4& p1) {
#pragma unroll
    for (int k = 0; k < 4; k++) {
      p0[k] = pk_bf16(__builtin_amdgcn_exp2f(s[2 * k] - mx), __builtin_amdgcn_exp2f(s[2 * k + 1] - mx));
      p1[k] = pk_bf16(__builtin_amdgcn_exp2f(s[8 + 2 * k] - mx), __builtin_amdgcn_exp2f(s[8 + 2 * k + 1] - mx));
    }
  };
  {
    u32x4 p0, p1; expack(sc[0], p0, p1);
    u32x4 f0, f1, d0, d1;
#pragma unroll
    for (int k = 0; k < 4; k++) {
      const unsigned m0 = ((2 * k > th) ? 0xffffu : 0u) | ((2 * k + 1 > th) ? 0xffff0000u : 0u);
      const unsigned m1 = ((16 + 2 * k > th) ? 0xffffu : 0u) | ((16 + 2 * k + 1 > th) ? 0xffff0000u : 0u);
      f0[k] = p0[k] & m0; d0[k] = p0[k] & ~m0; f1[k] = p1[k] & m1; d1[k] = p1[k] & ~m1;
    }
    pv(8, d0, d1);
    if (mmin == 0) pv(0, f0, f1);
  }
#pragma unroll
  for (int m = 1; m < 8; m++) if (m >= mmin) { u32x4 p0, p1; expack(sc[m], p0, p1); pv(m, p0, p1); }
  float l;
  { auto r = __builtin_amdgcn_permlane32_swap(__float_as_uint(o[10]), __float_as_uint(o[10]), false, false); l = __uint_as_float(r[0]); }
  const float inv = __builtin_amdgcn_rcpf(l);
  u32x4 om = {pk_bf16(o[0] * inv, o[1] * inv), pk_bf16(o[2] * inv, o[3] * inv), pk_bf16(o[4] * inv, o[5] * inv), pk_bf16(o[6] * inv, o[7] * inv)};
  const __amdgpu_buffer_rsrc_t ro = __builtin_amdgcn_make_buffer_rsrc((void*)(p.ws + WS_OM), 0, 128 * 16 * 1024, 0x00020000);
  const __amdgpu_buffer_rsrc_t rt = __builtin_amdgcn_make_buffer_rsrc((void*)(p.ws + WS_OT), 0, 32 * S * 4, 0x00020000);
  const int qt16 = 8 * rb + 2 * qt + (c >> 4);
  __builtin_amdgcn_raw_buffer_store_b128(om, ro, (unsigned)(((qt16 * 16 + j) * 64 + (c & 15) + 16 * (2 * hh + h)) * 16), 0, AUX_SC1);
  __builtin_amdgcn_raw_buffer_store_b32(pk_bf16(o[8] * inv, o[9] * inv), rt, h ? 0x7ffffff0u : (unsigned)(((2 * j + hh) * S + 128 * rb + 32 * qt + c) * 4), 0, AUX_SC1);
}

DEV void p3_wave(const Params& p, LAS char* wl, int qb, int ns, int w, int lane, u64 tag) {
  const int r = lane & 15, g = lane >> 4, q4 = (lane >> 2) & 3, p4 = lane & 3;
  const int qt16 = 4 * qb + w;
  poll_block(p, F_O + (qt16 >> 1) * 32 + (lane & 31), true, tag);
  STAMPW(11, 0);
  const __amdgpu_buffer_rsrc_t ro = __builtin_amdgcn_make_buffer_rsrc((void*)(p.ws + WS_OM), 0, 128 * 16 * 1024, 0x00020000);
  const __amdgpu_buffer_rsrc_t rt = __builtin_amdgcn_make_buffer_rsrc((void*)(p.ws + WS_OT), 0, 32 * S * 4, 0x00020000);
  u32x4 of[18];
#pragma unroll
  for (int s = 0; s < 16; s++) of[s] = __builtin_amdgcn_raw_buffer_load_b128(ro, (unsigned)(lane * 16), (unsigned)((qt16 * 16 + s) * 1024), AUX_SC1);
#pragma unroll
  for (int s = 0; s < 2; s++)
#pragma unroll
    for (int w4 = 0; w4 < 4; w4++) of[16 + s][w4] = __builtin_amdgcn_raw_buffer_load_b32(rt, (unsigned)(((4 * g + w4) * S + 16 * qt16 + r) * 4), (unsigned)(16 * s * S * 4), AUX_SC1);
  FENCE();
  f32x4 acc[5];
#pragma unroll
  for (int i = 0; i < 5; i++) acc[i] = (f32x4){0.f, 0.f, 0.f, 0.f};
  LAS char* am = wl + (18 * (g >> 1) + 4 * (g & 1) + q4) * WO_RS + p4 * 8;
  LAS char* atl = wl + (18 * (4 * g + (q4 >> 1)) + 16 + (q4 & 1)) * WO_RS + p4 * 8;
#pragma unroll
  for (int s = 0; s < 18; s++) {
#pragma unroll
    for (int nt = 0; nt < 5; nt++) {
      LAS char* a = (s < 16 ? am + 36 * s * WO_RS : atl + 18 * 16 * (s - 16) * WO_RS) + nt * 32;
      const int tstep = s < 16 ? 8 * WO_RS : 36 * WO_RS;
      bf16x8 A = cat44(trr(a), trr(a + tstep));
      acc[nt] = __builtin_amdgcn_mfma_f32_16x16x32_bf16(A, __builtin_bit_cast(bf16x8, of[s]), acc[nt], 0, 0, 0);
    }
  }
  STAMPW(12, 0);
  GAS u32x2* out = (GAS u32x2*)(p.out + (size_t)(16 * qt16 + r) * 1152 + ns * 72 + 4 * g);
#pragma unroll
  for (int nt = 0; nt < 5; nt++)
    if (nt < 4 || g < 2) out[nt * 4] = (u32x2){pk_bf16(acc[nt][0], acc[nt][1]), pk_bf16(acc[nt][2], acc[nt][3])};
}
DEV void b3_wave(const Params& p, LAS char* zl, int tile, int w4, int lane, u64 tag) {
  const int tb = tile / 9, cb = tile - tb * 9;
  const int ch0 = 64 * cb + 16 * w4, c = lane & 1, r32 = lane >> 1;
  const GAS u32x4* qp = (const GAS u32x4*)p.q;
  u32x4 qv[2];
#pragma unroll
  for (int u = 0; u < 2; u++) qv[u] = qp[(64 * tb + 32 * u + r32) * 72 + (ch0 >> 3) + c];
  poll_block(p, F_Z + (tb >> 4) * D + ch0 + (lane & 15), true, tag);
  const __amdgpu_buffer_rsrc_t rz = __builtin_amdgcn_make_buffer_rsrc((void*)(p.ws + WS_ZT), 0, D * S * 2, 0x00020000);
#pragma unroll
  for (int u = 0; u < 2; u++)
    __builtin_amdgcn_raw_ptr_buffer_load_lds(rz, zl + u * 1024, 16, (unsigned)((ch0 + 8 * u + (lane >> 3)) * 4096 + tb * 128 + (((lane & 7) ^ (4 * u)) << 4)), 0, 0, AUX_SC1);
  WAIT_VM(0);
#pragma unroll
  for (int u = 0; u < 2; u++) {
    const int tl = 32 * u + r32;
    LAS char* zr = zl + c * 1024 + (((tl >> 3) ^ (4 * c)) << 4) + (tl & 7) * 2;
    u32x4 o;
#pragma unroll
    for (int e = 0; e < 4; e++) {
      const unsigned z0 = *(const LAS u16*)(zr + (2 * e) * 128), z1 = *(const LAS u16*)(zr + (2 * e + 1) * 128);
      const f32x2 qq = unpk(qv[u][e]);
      o[e] = pk_bf16(qq.x * __uint_as_float(z0 << 16), qq.y * __uint_as_float(z1 << 16));
    }
    *(GAS u32x4*)(p.out + (size_t)(64 * tb + tl) * 1152 + 576 + ch0 + 8 * c) = o;
  }
}

__global__ void __launch_bounds__(512, 2) fused(Params p) {
  extern __shared__ __attribute__((aligned(16))) char smem[];
  LAS char* lds = (LAS char*)smem;
  const int wave = __builtin_amdgcn_readfirstlane(threadIdx.x >> 6), lane = threadIdx.x & 63;
  const int b = blockIdx.x, xcd = b & 7, loc = b >> 3;
  const bool heavy = loc < 28;
  const int w4 = wave & 3, tid4 = w4 * 64 + lane;
  const bool isH = wave >= 4;
  STAMPW(0, 0);
  u64 tag;
  const int arb = heavy ? 2 + (loc >> 1) : ((loc - 28) >> 1), aj = 2 * xcd + (loc & 1);
  const int li = xcd * 4 + (loc - 28);
  const int tile0 = heavy ? xcd * 28 + loc : 224 + 2 * li;
  const int cch = heavy ? 2 * (xcd * 28 + loc) : 448 + 4 * li;
  int zidx = -1;
  LAS char* att = lds + L_ATT;

  if (heavy) {
    const int rb = (xcd >> 1) * 4 + (loc & 3), c = (xcd & 1) * 7 + (loc >> 2);
    if (isH) {
      const int hw = w4;
      const __amdgpu_buffer_rsrc_t rx = __builtin_amdgcn_make_buffer_rsrc((void*)(p.x + (size_t)rb * 128 * D), 0, 128 * D * 2, 0x00020000);
      const __amdgpu_buffer_rsrc_t rw = __builtin_amdgcn_make_buffer_rsrc((void*)p.wqkv, 0, D * 3 * D * 2, 0x00020000);
      const unsigned vx = (lane >> 3) * 1152 + (((lane & 7) ^ (lane >> 3)) << 4);
      const int cm = (c == 13) ? 7 : 15;
      const unsigned vw0 = (lane >> 4) * 3456 + c * 256 + ((((lane & 15) ^ ((lane >> 4) << 1)) & cm) << 4);
      const unsigned vw1 = (lane >> 4) * 3456 + c * 256 + ((((lane & 15) ^ (((lane >> 4) | 4) << 1)) & cm) << 4);
      FENCE();
      p1_dma_stage(rx, rw, lds, 0, hw, vx, vw0, vw1);
      p1_dma_stage(rx, rw, lds, 1, hw, vx, vw0, vw1);
      FENCE();
      HkRegs hr;
      hk_load(p, hr, cch, tid4);
      FENCE();
      WAIT_VM(14); barrier_raw();
      p1_dma_stage(rx, rw, lds, 2, hw, vx, vw0, vw1); FENCE();
      WAIT_VM(14); barrier_raw();
      p1_dma_stage(rx, rw, lds, 3, hw, vx, vw0, vw1); FENCE();
      hk_write_main(hr, lds + L_CONV, tid4, 0); FENCE();
      WAIT_VM(8); barrier_raw();
      p1_dma_stage(rx, rw, lds, 4, hw, vx, vw0, vw1); FENCE();
      hk_write_main(hr, lds + L_CONV, tid4, 1); FENCE();
      WAIT_VM(8); barrier_raw();
      p1_dma_stage(rx, rw, lds, 5, hw, vx, vw0, vw1); FENCE();
      hk_write_edge(hr, lds + L_CONV, tid4); FENCE();
      WAIT_VM(8); barrier_raw();
      p1_dma_stage(rx, rw, lds, 6, hw, vx, vw0, vw1); FENCE();
      tag = lds_tag(lds); FENCE();
      WAIT_VM(8); barrier_raw();
      p1_dma_stage(rx, rw, lds, 7, hw, vx, vw0, vw1); FENCE();
      WAIT_VM(8); barrier_raw();
      STAMP2(12, 4);
      p1_dma_stage(rx, rw, lds, 8, hw, vx, vw0, vw1); FENCE();
      STAMP2(13, 4);
      const int kvidx = F_KV + (cch >> 6) * 32 + (lane & 31);
      const u64 kvf = ld_flag(flagp(p, kvidx));
      FENCE();
      WAIT_VM(9); STAMP2(14, 4); barrier_raw();
      const bool ok = __builtin_amdgcn_ballot_w64(kvf != tag) == 0;
      STAMPW(15, 4);
      FENCE();
      if (ok) { ur_dma(p, lds + L_CONV, cch, hw, lane); FENCE(); WAIT_VM(2); } else { WAIT_VM(0); }
      WAIT_LGKM0();
      barrier_raw();
      barrier_raw();
      barrier_raw();
      const __amdgpu_buffer_rsrc_t rq = __builtin_amdgcn_make_buffer_rsrc((void*)(p.ws + WS_QKV), 0, 1728 * S * 2, 0x00020000);
      const bool cvalid = c * 128 + hw * 32 < 3 * D;
#pragma unroll
      for (int u = 0; u < 8; u++) {
        const int n = hw * 32 + u * 4 + (lane >> 4);
        u32x4 v = *(const LAS u32x4*)(lds + L_RING + n * 256 + (((lane & 15) ^ (n & 15)) << 4));
        __builtin_amdgcn_raw_buffer_store_b128(v, rq, cvalid ? (unsigned)((c * 128 + n) * (S * 2) + rb * 256 + (lane & 15) * 16) : 0x7ffffff0u, 0, AUX_SC1);
      }
      FENCE();
      if (ok) { WAIT_VM(8); }
      else { poll_block(p, kvidx, true, tag); ur_dma(p, lds + L_CONV, cch, hw, lane); FENCE(); WAIT_VM(0); }
      barrier_raw();
      WAIT_VM(0);
      if (cvalid && lane == 0) st_flag(flagp(p, F_Q + rb * 64 + 4 * c + hw), tag);
      STAMPW(7, 4);
    } else {
      const int wm = wave & 1, wn = wave >> 1, g = lane >> 4;
      u64 gen = 0;
      if (wave == 0) gen = ld_flag(flagp(p, F_GEN));
      FENCE();
      {
        B1Regs br;
        b1_load(p, br, tile0, tid4);
        if (wave == 0 && lane < 10) *(LAS unsigned*)(lds + L_MISC + lane * 4) = 0u;
        b1_compute(br, lds + L_XCH, tile0, tid4);
      }
      FENCE();
      f32x4 acc[4][4];
#pragma unroll
      for (int i = 0; i < 4; i++)
#pragma unroll
        for (int j = 0; j < 4; j++) acc[i][j] = (f32x4){0.f, 0.f, 0.f, 0.f};
      const unsigned xa0 = L_RING + (64 * wm + (lane & 15)) * 128 + ((g ^ (lane & 7)) << 4);
      const int q = (lane >> 2) & 3, pp = lane & 3, f = q | ((g & 1) << 2);
      unsigned wb[4];
#pragma unroll
      for (int nt = 0; nt < 4; nt++) wb[nt] = L_RING + 16384 + (8 * g + q) * 256 + (((4 * wn + nt) ^ f) << 5) + pp * 8;
      bf16x8 a[2][4], bq[2][4];
      auto load_frags = [&](int buf, int s, int ks) {
        LAS char* base = lds + (s % 3) * STG;
#pragma unroll
        for (int mt = 0; mt < 4; mt++) a[buf][mt] = *(const LAS bf16x8*)(base + ((xa0 ^ (ks << 6)) + mt * 2048));
#pragma unroll
        for (int nt = 0; nt < 4; nt++) {
          s16x4 lo = trr(base + wb[nt] + ks * 8192), hi = trr(base + wb[nt] + ks * 8192 + 1024);
          bq[buf][nt] = cat44(lo, hi);
        }
      };
      auto mm = [&](int buf) {
#pragma unroll
        for (int mt = 0; mt < 4; mt++)
#pragma unroll
          for (int nt = 0; nt < 4; nt++) acc[mt][nt] = __builtin_amdgcn_mfma_f32_16x16x32_bf16(a[buf][mt], bq[buf][nt], acc[mt][nt], 0, 0, 0);
      };
      WAIT_LGKM0();
      barrier_raw();
      STAMPW(1, 0);
      load_frags(0, 0, 0);
      b1_store(p, lds + L_XCH, tile0, w4, lane);
#pragma unroll
      for (int s = 0; s < 9; s++) {
        SCHED_FENCE();
        load_frags(1, s, 1);
        mm(0);
        INTERLEAVE12();
        SCHED_FENCE();
        if (s == 3) {
          WAIT_VM(0);
          if (wave == 0) {
            asm volatile("" : "+v"(gen));
            tag = mk_tag(((u64)(unsigned)__builtin_amdgcn_readfirstlane((unsigned)(gen >> 32)) << 32) | (unsigned)__builtin_amdgcn_readfirstlane((unsigned)gen));
            if (lane == 0) *(LAS u32x2*)(lds + L_TAG) = (u32x2){(unsigned)tag, (unsigned)(tag >> 32)};
          }
        }
        WAIT_LGKM0();
        if (s == 6) STAMP2(15, 0);
        if (s < 8) {
          barrier_raw();
          STAMP2(s, 0);
          if (s == 3 && wave == 0 && lane < 2) st_flag(flagp(p, lane ? F_SEEN + b : F_KV + (tile0 % 9) * 32 + tile0 / 9), tag);
          SCHED_FENCE(); load_frags(0, s + 1, 0); mm(1); INTERLEAVE12();
        } else mm(1);
      }
      SCHED_FENCE();
      STAMPW(2, 0);
      barrier_raw();
      const float qs = 0.23570226039551584f * 1.4426950408889634f;
#pragma unroll
      for (int nt = 0; nt < 4; nt++) {
        const int n = 64 * wn + 16 * nt + (lane & 15);
        const float sc = (c * 128 + n < D) ? qs : 1.0f;
#pragma unroll
        for (int mt = 0; mt < 4; mt++) {
          const int t8 = 8 * wm + 2 * mt + (g >> 1);
          u32x2 v = {pk_bf16(acc[mt][nt][0] * sc, acc[mt][nt][1] * sc), pk_bf16(acc[mt][nt][2] * sc, acc[mt][nt][3] * sc)};
          *(LAS u32x2*)(lds + L_RING + n * 256 + ((t8 ^ (lane & 15)) << 4) + (g & 1) * 8) = v;
        }
      }
      WAIT_LGKM0();
      barrier_raw();
      STAMPW(3, 0);
      tag = lds_tag(lds);
      barrier_raw();
      STAMPW(4, 0);
      conv_run(p, lds + L_CONV, lds + L_XCH, lds + L_MISC, cch, w4, lane);
      STAMPW(6, 0);
      zidx = F_Z + (w4 & 1) * D + cch + (w4 >> 1);
    }
  } else {
    const int grp = wave >> 2;
    LAS char* cv = lds + (grp ? L_CONV2 : L_CONV);
    LAS char* xc = lds + (grp ? L_XCH2 : L_XCH);
    const int tile = tile0 + grp, ch = cch + 2 * grp;
    u64 gen = 0;
    if (wave == 0) gen = ld_flag(flagp(p, F_GEN));
    FENCE();
    B1Regs br; HkRegs hr;
    b1_load(p, br, tile, tid4);
    hk_load(p, hr, ch, tid4);
    if (wave == 0 && lane < 10) *(LAS unsigned*)(lds + L_MISC + lane * 4) = 0u;
    FENCE();
    b1_compute(br, xc, tile, tid4);
    hk_write_main(hr, cv, tid4, 0);
    hk_write_main(hr, cv, tid4, 1);
    hk_write_edge(hr, cv, tid4);
    if (wave == 0) {
      asm volatile("" : "+v"(gen));
      tag = mk_tag(((u64)(unsigned)__builtin_amdgcn_readfirstlane((unsigned)(gen >> 32)) << 32) | (unsigned)__builtin_amdgcn_readfirstlane((unsigned)gen));
      if (lane == 0) *(LAS u32x2*)(lds + L_TAG) = (u32x2){(unsigned)tag, (unsigned)(tag >> 32)};
    }
    WAIT_LGKM0(); barrier_raw();
    tag = lds_tag(lds);
    b1_store(p, xc, tile, w4, lane);
    WAIT_VM(0); barrier_raw();
    if (wave == 0) {
      if (lane < 3) st_flag(flagp(p, lane == 2 ? F_SEEN + b : F_KV + ((tile0 + lane) % 9) * 32 + (tile0 + lane) / 9), tag);
      poll_block<8>(p, F_KV + (cch >> 6) * 32 + (lane & 31), true, tag);
    }
    if (b == 255 && wave == 7) {
#pragma unroll 1
      for (int i = 0; i < 4; i++) poll_block<8>(p, F_SEEN + 64 * i + lane, true, tag);
      if (lane == 0) st_flag(flagp(p, F_GEN), tag);
    }
    barrier_raw();
    ur_dma(p, cv, ch, w4, lane);
    WAIT_VM(0); barrier_raw();
    STAMPW(4, 0);
    conv_run(p, cv, xc, lds + L_MISC + 16 * grp, ch, w4, lane);
    STAMPW(6, 0);
    const int zi = F_Z + (w4 & 1) * D + ch + (w4 >> 1);
    WAIT_LGKM0(); barrier_raw();
    if (isH) { WAIT_VM(0); if (lane == 0) st_flag(flagp(p, zi), tag); }
    else zidx = zi;
  }
  if (isH) {
    AttOff ao;
    att_off(ao, arb, aj, lane);
    if (tid4 < 48) *(LAS u32x4*)(att + A_ONES + tid4 * 16) = (u32x4){0x3f803f80u, 0x3f803f80u, 0x3f803f80u, 0x3f803f80u};
    FENCE();
    if (w4 == 0) {
      const int l = lane < 18 ? lane : 0, r = l / 6, kind = (l - 6 * r) >> 1, e = l & 1;
      const int row = arb - 2 + r, col = kind * 576 + 36 * aj + 35 * e;
      const bool act = lane < 18 && row >= 0 && !(kind == 0 && r < 2);
      poll_block<2>(p, F_Q + (row < 0 ? 0 : row) * 64 + (col >> 5), act, tag);
      if (lane == 0) *(volatile LAS unsigned*)(lds + L_QRDY) = 1u;
    } else lds_wait(lds + L_QRDY);
    STAMPW(8, 4);
    att_dma(p, att, ao, w4);
    __builtin_amdgcn_s_waitcnt(0x0F70);
    asm volatile("" ::: "memory");
  }
  WAIT_LGKM0();
  barrier_raw();
  STAMPW(9, 0);
  const int qb = 4 * xcd + (loc & 3), ns = loc >> 2;
  att_wave(p, att, lds + L_WO, ns, arb, aj, wave, lane, zidx, tag);
  FENCE();
  WAIT_VM(2);
  barrier_raw();
  STAMPW(10, 0);
  WAIT_VM(0);
  if (lane == 0) st_flag(flagp(p, F_O + (arb * 4 + w4) * 32 + 2 * aj + (wave >> 2)), tag);
  if (isH) {
    b3_wave(p, lds + L_Z + w4 * 2048, tile0, w4, lane, tag);
    if (!heavy) b3_wave(p, lds + L_Z + 8192 + w4 * 2048, tile0 + 1, w4, lane, tag);
    STAMPW(14, 4);
  } else {
    p3_wave(p, lds + L_WO, qb, ns, w4, lane, tag);
    STAMPW(13, 0);
  }
}

extern "C" void kernel_launch(void* const* d_in, const int* in_sizes, int n_in, void* d_out, int out_size, void* d_ws, size_t ws_size, hipStream_t stream) {
  Params p;
  p.x = (const u16*)d_in[0]; p.wqkv = (const u16*)d_in[1]; p.wo = (const u16*)d_in[2]; p.q = (const u16*)d_in[3]; p.k = (const u16*)d_in[4]; p.v = (const u16*)d_in[5];
  p.sk = (const u16*)d_in[6]; p.hk = (const u16*)d_in[7]; p.Dd = (const u16*)d_in[8]; p.out = (u16*)d_out; p.ws = (char*)d_ws;
  constexpr int LDS_BYTES = 160 * 1024;
  (void)hipFuncSetAttribute((const void*)fused, hipFuncAttributeMaxDynamicSharedMemorySize, LDS_BYTES);
  fused<<<256, 512, LDS_BYTES, stream>>>(p);
}
```

```cpp
#include <hip/hip_runtime.h>
typedef unsigned short u16;
typedef unsigned long long u64;
typedef __bf16 bf16x8 __attribute__((ext_vector_type(8)));
typedef __bf16 bf16x2 __attribute__((ext_vector_type(2)));
typedef short s16x4 __attribute__((ext_vector_type(4)));
typedef float f32x2 __attribute__((ext_vector_type(2)));
typedef float f32x4 __attribute__((ext_vector_type(4)));
typedef float f32x16 __attribute__((ext_vector_type(16)));
typedef unsigned u32x2 __attribute__((ext_vector_type(2)));
typedef unsigned u32x4 __attribute__((ext_vector_type(4)));
#define GAS __attribute__((address_space(1)))
#define LAS __attribute__((address_space(3)))
#define DEV __device__ __forceinline__

constexpr int S = 2048, D = 576;
constexpr size_t WS_QKV = 0;
constexpr size_t WS_KVT = 8u << 20;
constexpr size_t WS_ZT = 11u << 20;
constexpr size_t WS_OM = 14u << 20;
constexpr size_t WS_OT = 17u << 20;
constexpr size_t WS_FLAG = 18u << 20;
constexpr int F_GEN = 0, F_SEEN = 128, F_KV = 512, F_Q = 1024, F_Z = 2048, F_O = 4096;
constexpr size_t WS_STAMP = 32u << 20;
#define STAMPW(i, w)
#define STAMP2(i, w)

DEV unsigned pk_bf16(float a, float b) { f32x2 v = {a, b}; bf16x2 r = __builtin_convertvector(v, bf16x2); return __builtin_bit_cast(unsigned, r); }
DEV f32x2 unpk(unsigned w) { f32x2 r; r.x = __uint_as_float(w << 16); r.y = __uint_as_float(w & 0xffff0000u); return r; }
DEV void barrier_raw() { __builtin_amdgcn_s_barrier(); }
#define WAIT_VM(n) asm volatile("s_waitcnt vmcnt(" #n ")" ::: "memory")
#define WAIT_LGKM0() asm volatile("s_waitcnt lgkmcnt(0)" ::: "memory")
#define SCHED_FENCE() __builtin_amdgcn_sched_barrier(0)
#define FENCE() do { asm volatile("" ::: "memory"); __builtin_amdgcn_sched_barrier(0); } while (0)
#define INTERLEAVE12() do { _Pragma("unroll") for (int i_ = 0; i_ < 12; i_++) { __builtin_amdgcn_sched_group_barrier(0x008, 1, 0); __builtin_amdgcn_sched_group_barrier(0x100, 1, 0); } __builtin_amdgcn_sched_group_barrier(0x008, 4, 0); } while (0)
constexpr int AUX_SC1 = 16;
constexpr int SPIN_MAX = 20000;

struct Params { const u16 *x, *wqkv, *wo, *q, *k, *v, *sk, *hk, *Dd; u16* out; char* ws; };

constexpr int STG = 32768;
constexpr int CV_HP = 4160, CV_UR = 4 * CV_HP, CV_SZ = CV_UR + 4576;
constexpr int L_CONV = 0;
constexpr int L_XCH = 2 * CV_SZ;
constexpr int L_MISC = 58880;
constexpr int L_QRDY = L_MISC + 32, L_TAG = L_MISC + 40;
constexpr int L_RING = 65536;
constexpr int L_CONV2 = 65536, L_XCH2 = L_CONV2 + 2 * CV_SZ;
constexpr int L_ATT = 98304;
constexpr int A_K = 0, A_V = 36 * 768, A_ONES = 72 * 768, A_Q = 73 * 768;
constexpr int WO_RS = 160, L_WO = 0;
constexpr int L_Z = L_ATT;

DEV GAS u64* flagp(const Params& p, int idx) { return (GAS u64*)(p.ws + WS_FLAG) + idx * 16; }
DEV u64 ld_flag(GAS u64* a) { return __hip_atomic_load(a, __ATOMIC_RELAXED, __HIP_MEMORY_SCOPE_AGENT); }
DEV void st_flag(GAS u64* a, u64 v) { __hip_atomic_store(a, v, __ATOMIC_RELAXED, __HIP_MEMORY_SCOPE_AGENT); }
DEV u64 mk_tag(u64 gen) { return gen * 6364136223846793005ull + 1442695040888963407ull; }
template <int SLEEP = 4>
DEV void poll_block(const Params& p, int idx, bool active, u64 tag) {
  GAS u64* a = flagp(p, idx);
#pragma unroll 1
  for (int it = 0; it < SPIN_MAX; it++) {
    u64 v = tag;
    if (active) v = ld_flag(a);
    if (__builtin_amdgcn_ballot_w64(v != tag) == 0) break;
    __builtin_amdgcn_s_sleep(SLEEP);
  }
  asm volatile("" ::: "memory");
}
DEV u64 lds_tag(LAS char* lds) {
  const u32x2 t = *(const LAS u32x2*)(lds + L_TAG);
  return ((u64)(unsigned)__builtin_amdgcn_readfirstlane(t[1]) << 32) | (unsigned)__builtin_amdgcn_readfirstlane(t[0]);
}
DEV void lds_wait(LAS char* w) {
#pragma unroll 1
  for (int it = 0; it < (1 << 24); it++) {
    if (*(volatile LAS unsigned*)w == 1u) break;
    __builtin_amdgcn_s_sleep(1);
  }
  asm volatile("" ::: "memory");
}

DEV void p1_dma_stage(const __amdgpu_buffer_rsrc_t& rx, const __amdgpu_buffer_rsrc_t& rw, LAS char* lds, int s, int hw, unsigned vx, unsigned vw0, unsigned vw1) {
  LAS char* xs = lds + L_RING + (s % 3) * STG; LAS char* wsl = xs + 16384;
#pragma unroll
  for (int u = 0; u < 4; u++)
    __builtin_amdgcn_raw_ptr_buffer_load_lds(rx, xs + (4 * hw + u) * 1024, 16, vx, (4 * hw + u) * 8 * 1152 + s * 128, 0, 0);
#pragma unroll
  for (int u = 0; u < 4; u++)
    __builtin_amdgcn_raw_ptr_buffer_load_lds(rw, wsl + (4 * hw + u) * 1024, 16, (u & 2) ? vw1 : vw0, (64 * s + (4 * hw + u) * 4) * 3456, 0, 0);
}

struct B1Regs { u32x4 kr[5], vr[2], sw[4]; };
DEV void b1_load(const Params& p, B1Regs& r, int tile, int tid4) {
  const int tb = tile / 9, cb = tile - tb * 9, tp = tid4 >> 3, c8 = tid4 & 7;
  const GAS u32x4* kp = (const GAS u32x4*)p.k; const GAS u32x4* vp = (const GAS u32x4*)p.v; const GAS u32x4* skp = (const GAS u32x4*)p.sk;
  const int t0 = 64 * tb + 2 * tp, ch = 8 * cb + c8;
#pragma unroll
  for (int i = 0; i < 5; i++) { const int t = t0 - 3 + i; r.kr[i] = kp[(t < 0 ? 0 : t) * 72 + ch]; }
#pragma unroll
  for (int i = 0; i < 2; i++) r.vr[i] = vp[(t0 + i) * 72 + ch];
#pragma unroll
  for (int i = 0; i < 4; i++) r.sw[i] = skp[ch * 4 + i];
}
DEV void b1_compute(B1Regs& r, LAS char* stg, int tile, int tid4) {
  const int tb = tile / 9, tp = tid4 >> 3, c8 = tid4 & 7;
  const int t0 = 64 * tb + 2 * tp;
#pragma unroll
  for (int i = 0; i < 3; i++) if (t0 - 3 + i < 0) r.kr[i] = (u32x4){0, 0, 0, 0};
  LAS char* o = stg + c8 * 1024 + ((((31 - tp) >> 2) ^ c8) << 4) + ((31 - tp) & 3) * 4;
#pragma unroll
  for (int pr = 0; pr < 4; pr++) {
    const f32x2 a0 = unpk(r.sw[pr][0]), a1 = unpk(r.sw[pr][1]), b0 = unpk(r.sw[pr][2]), b1 = unpk(r.sw[pr][3]);
    const f32x2 w0 = {a0.x, b0.x}, w1 = {a0.y, b0.y}, w2 = {a1.x, b1.x}, w3 = {a1.y, b1.y};
    f32x2 kf[5];
#pragma unroll
    for (int i = 0; i < 5; i++) kf[i] = unpk(r.kr[i][pr]);
    f32x2 s0 = kf[0] * w3 + kf[1] * w2; s0 += kf[2] * w1; s0 += kf[3] * w0; s0 = s0 * unpk(r.vr[0][pr]);
    f32x2 s1 = kf[1] * w3 + kf[2] * w2; s1 += kf[3] * w1; s1 += kf[4] * w0; s1 = s1 * unpk(r.vr[1][pr]);
    *(LAS unsigned*)(o + (2 * pr) * 128) = pk_bf16(s1.x, s0.x);
    *(LAS unsigned*)(o + (2 * pr + 1) * 128) = pk_bf16(s1.y, s0.y);
  }
}
DEV void b1_store(const Params& p, LAS char* stg, int tile, int w4, int lane) {
  const int tb = tile / 9, cb = tile - tb * 9;
  const __amdgpu_buffer_rsrc_t rk = __builtin_amdgcn_make_buffer_rsrc((void*)(p.ws + WS_KVT), 0, D * S * 2, 0x00020000);
#pragma unroll
  for (int u = 0; u < 2; u++) {
    const int ii = w4 * 2 + u, ch = 8 * ii + (lane >> 3);
    u32x4 v = *(const LAS u32x4*)(stg + ch * 128 + (lane & 7) * 16);
    __builtin_amdgcn_raw_buffer_store_b128(v, rk, (unsigned)((64 * cb + ch) * 4096 + (1984 - 64 * tb) * 2 + (((lane & 7) ^ ii) << 4)), 0, AUX_SC1);
  }
}

struct HkRegs { u32x4 c0[3]; u32x2 c1[3]; };
DEV void hk_load(const Params& p, HkRegs& r, int ch0, int tid4) {
  const GAS u32x4* hk = (const GAS u32x4*)p.hk;
#pragma unroll
  for (int cs = 0; cs < 2; cs++) {
    const int i0 = tid4 - 2, i1 = tid4 - 1;
    r.c0[cs] = hk[(ch0 + cs) * 256 + (i0 < 0 ? 0 : i0)];
    r.c1[cs] = *(const GAS u32x2*)(hk + (ch0 + cs) * 256 + (i1 < 0 ? 0 : i1));
  }
  const int cs = (tid4 >> 1) & 1, i0 = 254 + (tid4 & 1);
  r.c0[2] = hk[(ch0 + cs) * 256 + i0];
  r.c1[2] = *(const GAS u32x2*)(hk + (ch0 + cs) * 256 + 255);
}
DEV void hk_put(LAS char* b, u32x4 c0, u32x2 c1) {
  const unsigned a0 = __builtin_amdgcn_alignbit(c0[1], c0[0], 16), a1 = __builtin_amdgcn_alignbit(c0[2], c0[1], 16), a2 = __builtin_amdgcn_alignbit(c0[3], c0[2], 16),
                 a3 = __builtin_amdgcn_alignbit(c1[0], c0[3], 16), a4 = __builtin_amdgcn_alignbit(c1[1], c1[0], 16);
  *(LAS u32x4*)(b) = (u32x4){a0, a1, a2, a3};
  *(LAS u32x4*)(b + CV_HP) = (u32x4){c0[1], c0[2], c0[3], c1[0]};
  *(LAS u32x4*)(b + 2 * CV_HP) = (u32x4){a1, a2, a3, a4};
  *(LAS u32x4*)(b + 3 * CV_HP) = (u32x4){c0[2], c0[3], c1[0], c1[1]};
}
DEV void hk_write_main(HkRegs& r, LAS char* cv, int tid4, int cs) {
  const u32x4 z4 = {0, 0, 0, 0};
  const u32x2 z2 = {0, 0};
  u32x4 c0 = r.c0[cs]; u32x2 c1 = r.c1[cs];
  if (tid4 < 2) c0 = z4;
  if (tid4 < 1) c1 = z2;
  hk_put(cv + cs * CV_SZ + tid4 * 16, c0, c1);
}
DEV void hk_write_edge(HkRegs& r, LAS char* cv, int tid4) {
  const u32x4 z4 = {0, 0, 0, 0};
  const u32x2 z2 = {0, 0};
  if (tid4 < 4) hk_put(cv + ((tid4 >> 1) & 1) * CV_SZ + (256 + (tid4 & 1)) * 16, r.c0[2], (tid4 & 1) ? z2 : r.c1[2]);
  if (tid4 < 60) { const int cs = tid4 >= 30; *(LAS u32x4*)(cv + cs * CV_SZ + CV_UR + (256 + tid4 - 30 * cs) * 16) = z4; }
}
DEV void ur_dma(const Params& p, LAS char* cv, int ch0, int w4, int lane) {
  const __amdgpu_buffer_rsrc_t rk = __builtin_amdgcn_make_buffer_rsrc((void*)(p.ws + WS_KVT), 0, D * S * 2, 0x00020000);
  const int cs = w4 >> 1;
#pragma unroll
  for (int u = 0; u < 2; u++) {
    const int pc = (w4 & 1) * 2 + u;
    __builtin_amdgcn_raw_ptr_buffer_load_lds(rk, cv + cs * CV_SZ + CV_UR + pc * 1024, 16, (unsigned)(lane * 16), (ch0 + cs) * 4096 + pc * 1024, 0, AUX_SC1);
  }
}
DEV void conv_run(const Params& p, LAS char* cv, LAS char* xch, LAS char* xfl, int ch0, int w4, int lane) {
  const int cs = w4 >> 1, half = w4 & 1;
  const int r = lane & 15, g = lane >> 4;
  unsigned dbits = p.Dd[ch0 + cs];
  FENCE();
  f32x4 acc[8];
  {
    LAS char* hp = cv + cs * CV_SZ + (r & 3) * CV_HP + ((r & ~3) + 8 * g + half * 128) * 2;
    LAS char* ur = cv + cs * CV_SZ + CV_UR + (2032 - 16 * r + 8 * g + half * 128) * 2;
#pragma unroll
    for (int i = 0; i < 8; i++) acc[i] = (f32x4){0.f, 0.f, 0.f, 0.f};
#pragma unroll
    for (int ph = 0; ph < 4; ph++) {
      bf16x8 A[8], F[8];
#pragma unroll
      for (int a = 0; a < 8; a++) {
        u32x2 lo = *(const LAS u32x2*)(hp + (256 * a + 32 * ph) * 2), hi = *(const LAS u32x2*)(hp + (256 * a + 32 * ph) * 2 + 8);
        A[a] = __builtin_bit_cast(bf16x8, (u32x4){lo[0], lo[1], hi[0], hi[1]});
        F[a] = *(const LAS bf16x8*)(ur + (32 * ph - 256 * a) * 2);
      }
#pragma unroll
      for (int a = 0; a < 8; a++)
#pragma unroll
        for (int n = 0; n + a < 8; n++) acc[a + n] = __builtin_amdgcn_mfma_f32_16x16x32_bf16(A[a], F[n], acc[a + n], 0, 0, 0);
    }
  }
  STAMPW(5, 0);
  {
    LAS char* x = xch + w4 * 4096 + lane * 16;
#pragma unroll
    for (int i = 0; i < 4; i++) {
      f32x4 a = acc[i], b = acc[4 + i];
      asm volatile("" : "+v"(a), "+v"(b));
      *(LAS f32x4*)(x + i * 1024) = half ? a : b;
    }
    WAIT_LGKM0();
    if (lane == 0) *(volatile LAS unsigned*)(xfl + w4 * 4) = 1u;
    lds_wait(xfl + (w4 ^ 1) * 4);
  }
  LAS char* x = xch + (w4 ^ 1) * 4096 + lane * 16;
  const float Dv = __uint_as_float(dbits << 16);
  LAS char* zst = cv + cs * CV_SZ + half * 2048;
  LAS char* ur = cv + cs * CV_SZ + CV_UR;
#pragma unroll
  for (int i = 0; i < 4; i++) {
    f32x4 ya = acc[i], yb = acc[4 + i];
    asm volatile("" : "+v"(ya), "+v"(yb));
    f32x4 y = (half ? yb : ya) + *(const LAS f32x4*)(x + i * 1024);
    const int t = 256 * (4 * half + i) + 16 * r + 4 * g;
    u32x2 u = *(const LAS u32x2*)(ur + (2044 - t) * 2);
    y[3] += __uint_as_float(u[0] << 16) * Dv; y[2] += __uint_as_float(u[0] & 0xffff0000u) * Dv;
    y[1] += __uint_as_float(u[1] << 16) * Dv; y[0] += __uint_as_float(u[1] & 0xffff0000u) * Dv;
    *(LAS u32x2*)(zst + (256 * i + 16 * r + 4 * g) * 2) = (u32x2){pk_bf16(y[0], y[1]), pk_bf16(y[2], y[3])};
  }
  WAIT_LGKM0();
  const __amdgpu_buffer_rsrc_t rz = __builtin_amdgcn_make_buffer_rsrc((void*)(p.ws + WS_ZT), 0, D * S * 2, 0x00020000);
#pragma unroll
  for (int i = 0; i < 2; i++) {
    u32x4 v = *(const LAS u32x4*)(zst + i * 1024 + lane * 16);
    __builtin_amdgcn_raw_buffer_store_b128(v, rz, (unsigned)((ch0 + cs) * 4096 + half * 2048 + i * 1024 + lane * 16), 0, AUX_SC1);
  }
}

struct AttOff { unsigned k[3], v[3], q; };
constexpr unsigned OOB = 0x7ffffff0u;
DEV void att_off(AttOff& o, int rb, int j, int lane) {
  const int tok2 = 256 * (rb - 2);
#pragma unroll
  for (int t = 0; t < 3; t++) {
    const int lin = 64 * t + lane, row = lin / 48, sl = lin - row * 48;
    const int ck = tok2 + (((((sl >> 2) ^ row) << 2) | (sl & 3)) << 4), cv = tok2 + ((sl ^ row) << 4);
    o.k[t] = ck < 0 ? OOB : (unsigned)((576 + 36 * j + row) * 4096 + ck);
    o.v[t] = cv < 0 ? OOB : (unsigned)((1152 + 36 * j + row) * 4096 + cv);
  }
  const int row = lane >> 4, sl = lane & 15;
  o.q = (unsigned)((36 * j + row) * 4096 + rb * 256 + (((((sl >> 2) ^ row) << 2) | (sl & 3)) << 4));
}
DEV void att_dma(const Params& p, LAS char* att, const AttOff& o, int w) {
  const __amdgpu_buffer_rsrc_t rq = __builtin_amdgcn_make_buffer_rsrc((void*)(p.ws + WS_QKV), 0, 1728 * S * 2, 0x00020000);
#pragma unroll
  for (int u = 0; u < 3; u++) {
    const int a = w + 4 * u;
    if (u < 2 || w == 0) {
#pragma unroll
      for (int t = 0; t < 3; t++) __builtin_amdgcn_raw_ptr_buffer_load_lds(rq, att + A_K + (3 * a + t) * 1024, 16, o.k[t], a * 16384, 0, AUX_SC1);
    }
    const int av = u < 2 ? a : 8;
    if (u < 2 || w == 1) {
#pragma unroll
      for (int t = 0; t < 3; t++) __builtin_amdgcn_raw_ptr_buffer_load_lds(rq, att + A_V + (3 * av + t) * 1024, 16, o.v[t] ^ ((av & 3) << 6), av * 16384, 0, AUX_SC1);
    }
  }
  const int q0 = w == 0 ? 0 : (w == 1 ? 1 : (w == 2 ? 2 : 6)), qn = w < 2 ? 1 : (w == 2 ? 4 : 3);
#pragma unroll
  for (int u = 0; u < 4; u++)
    if (u < qn) __builtin_amdgcn_raw_ptr_buffer_load_lds(rq, att + A_Q + (q0 + u) * 1024, 16, o.q, (q0 + u) * 16384, 0, AUX_SC1);
}
struct WoDma { u32x4 rs; unsigned off; int ch; unsigned l0; };
DEV void wo_init(WoDma& d, const Params& p, int ns, int w, int lane) {
  const u64 a = (u64)p.wo;
  d.rs = (u32x4){(unsigned)a, (unsigned)(a >> 32) & 0xffffu, (unsigned)(D * D * 2), 0x00020000u};
  const int lin = 64 * w + lane, row = lin / 10;
  d.ch = lin - row * 10;
  d.off = (unsigned)(row * 1152 + ns * 144 + d.ch * 16);
  d.l0 = (unsigned)(L_WO + w * 1024);
}
DEV void wo_piece(WoDma& d, int u) {
  unsigned keep;
  asm volatile("s_mov_b32 %0, m0\n\ts_mov_b32 m0, %1\n\ts_nop 0\n\tbuffer_load_dwordx4 %2, %3, 0 offen lds\n\ts_mov_b32 m0, %0" : "=&s"(keep) : "s"(d.l0 + u * 8192), "v"(d.off), "s"(d.rs) : "memory");
  d.ch += 2; d.off += 51 * 1152 + 32;
  if (d.ch >= 10) { d.ch -= 10; d.off += 1152 - 160; }
}
DEV bf16x8 cat44(s16x4 lo, s16x4 hi) { return __builtin_bit_cast(bf16x8, __builtin_shufflevector(lo, hi, 0, 1, 2, 3, 4, 5, 6, 7)); }
DEV s16x4 trr(LAS char* a) { return __builtin_amdgcn_ds_read_tr16_b64_v4i16((LAS s16x4*)a); }
#define MFMA32(a, b, c) __builtin_amdgcn_mfma_f32_32x32x16_bf16((a), (b), (c), 0, 0, 0)
#define MFMA32T(a, b, c) __builtin_amdgcn_mfma_f32_32x32x8bf16_1k((a), (b), (c), 0, 0, 0)
DEV float ex2(float x) { return __builtin_amdgcn_exp2f(x); }
DEV float swap_lo(float x) { auto r = __builtin_amdgcn_permlane32_swap(__float_as_uint(x), __float_as_uint(x), false, false); return __uint_as_float(r[0]); }

struct KFrag { s16x4 lo, hi, t; };
struct VFrag { bf16x8 v0, v1; };
struct AttAddr { LAS char *ak[4], *at[4], *av[4], *aw[4], *aq, *aqt; WoDma wd; };
DEV void att_setup(AttAddr& A, const Params& p, LAS char* att, int ns, int wave, int lane) {
  const int hh = wave >> 2, qt = wave & 3;
  const int c = lane & 31, h = lane >> 5;
  const int kh = (lane >> 4) & 1, q4 = (lane >> 2) & 3, p4 = lane & 3, pi = ((p4 & 1) << 1) | (p4 >> 1);
  const int row = 18 * hh + 8 * h + q4, rowt = 18 * hh + 16 + (q4 & 1);
  const int vrow = c < 18 ? 18 * hh + c : ((c >= 20 && c < 28) ? 18 * hh : 36);
  const int vkey = vrow == 36 ? (hh ? 0 : 2) : (vrow & 15);
  A.aq = att + A_Q + row * 256 + ((qt ^ (row & 3)) << 6) + kh * 32 + p4 * 8;
  A.aqt = att + A_Q + rowt * 256 + ((qt ^ (rowt & 3)) << 6) + kh * 32 + p4 * 8;
#pragma unroll
  for (int mm = 0; mm < 4; mm++) {
    const int kt = qt + mm;
    A.ak[mm] = att + A_K + row * 768 + ((kt >> 2) << 8) + (((kt & 3) ^ (row & 3)) << 6) + kh * 32 + pi * 8;
    A.at[mm] = att + A_K + rowt * 768 + ((kt >> 2) << 8) + (((kt & 3) ^ (rowt & 3)) << 6) + kh * 32 + pi * 8;
    A.av[mm] = att + A_V + vrow * 768 + ((kt >> 2) << 8) + (((4 * (kt & 3) + h) ^ vkey) << 4);
    A.aw[mm] = (LAS char*)((unsigned)(size_t)A.av[mm] ^ 32u);
    asm volatile("" : "+v"(A.ak[mm]), "+v"(A.at[mm]), "+v"(A.av[mm]), "+v"(A.aw[mm]));
  }
  wo_init(A.wd, p, ns, wave, lane);
  asm volatile("" : "+v"(A.aq), "+v"(A.aqt), "+v"(A.wd.off), "+v"(A.wd.ch));
}
DEV void att_wave(const Params& p, LAS char* att, AttAddr& A, int rb, int j, int wave, int lane, int zidx, u64 tag) {
  const int hh = wave >> 2, qt = wave & 3;
  const int c = lane & 31, h = lane >> 5;
  bf16x8 q0; s16x4 q1;
  q0 = cat44(trr(A.aq), trr(A.aq + 1024));
  { const u32x2 tt = __builtin_bit_cast(u32x2, trr(A.aqt)); q1 = __builtin_bit_cast(s16x4, (u32x2){h ? 0u : tt[0], 0u}); }
  LAS char **ak = A.ak, **at = A.at, **av = A.av, **aw = A.aw;
  WoDma& wd = A.wd;
  auto ldk = [&](int m) { KFrag k; LAS char* a = ak[m & 3] + (m >> 2) * 256; k.lo = trr(a); k.hi = trr(a + 4 * 768); k.t = trr(at[m & 3] + (m >> 2) * 256); return k; };
  auto ldv = [&](int m) { VFrag v; v.v0 = *(const LAS bf16x8*)(av[m & 3] + (m >> 2) * 256); v.v1 = *(const LAS bf16x8*)(aw[m & 3] + (m >> 2) * 256); return v; };
  f32x16 zero;
#pragma unroll
  for (int i = 0; i < 16; i++) zero[i] = 0.f;
  const int th = c - 8 * h;
  f32x16 o, sa, sb;
  {
    KFrag k0 = ldk(0), k8 = ldk(8), kn = ldk(1);
    VFrag v8 = ldv(8), v0 = ldv(0);
    SCHED_FENCE();
    sa = MFMA32(cat44(k0.lo, k0.hi), q0, zero);
    sb = MFMA32(cat44(k8.lo, k8.hi), q0, zero);
    sa = MFMA32T(k0.t, q1, sa);
    sb = MFMA32T(k8.t, q1, sb);
    f32x16 s1 = MFMA32(cat44(kn.lo, kn.hi), q0, zero);
    s1 = MFMA32T(kn.t, q1, s1);
    SCHED_FENCE();
    u32x4 pf, pd;
#pragma unroll
    for (int g = 0; g < 2; g++) {
#pragma unroll
      for (int k = 0; k < 4; k++) {
        int tv = th - 16 * g - 2 * k;
        asm volatile("" : "+v"(tv));
        const bool c0 = tv < 0, c1 = tv < 1;
        const float a0 = c0 ? sa[8 * g + 2 * k] : sb[8 * g + 2 * k], a1 = c1 ? sa[8 * g + 2 * k + 1] : sb[8 * g + 2 * k + 1];
        const unsigned mk = (c0 ? 0xffffu : 0u) | (c1 ? 0xffff0000u : 0u);
        const unsigned pp = pk_bf16(ex2(a0), ex2(a1));
        pf[k] = pp & mk; pd[k] = pp & ~mk;
        SCHED_FENCE();
      }
      o = MFMA32(g ? v8.v1 : v8.v0, __builtin_bit_cast(bf16x8, pd), g ? o : zero);
      o = MFMA32(g ? v0.v1 : v0.v0, __builtin_bit_cast(bf16x8, pf), o);
    }
    sa = s1;
    SCHED_FENCE();
  }
  KFrag kc = ldk(2);
  VFrag vc = ldv(1);
#pragma unroll
  for (int m = 1; m < 8; m++) {
    KFrag kx; VFrag vx;
    SCHED_FENCE();
    if (m < 7) { wo_piece(wd, 2 * m - 2); wo_piece(wd, 2 * m - 1); }
    if (m < 6) kx = ldk(m + 2);
    if (m < 7) vx = ldv(m + 1);
    SCHED_FENCE();
    if (m < 7) sb = MFMA32(cat44(kc.lo, kc.hi), q0, zero);
    SCHED_FENCE();
    float e[16];
#pragma unroll
    for (int i = 0; i < 8; i++) e[i] = ex2(sa[i]);
    SCHED_FENCE();
    if (m < 7) sb = MFMA32T(kc.t, q1, sb);
    SCHED_FENCE();
    u32x4 p0, p1;
#pragma unroll
    for (int k = 0; k < 4; k++) p0[k] = pk_bf16(e[2 * k], e[2 * k + 1]);
#pragma unroll
    for (int i = 8; i < 16; i++) e[i] = ex2(sa[i]);
    SCHED_FENCE();
    o = MFMA32(vc.v0, __builtin_bit_cast(bf16x8, p0), o);
    SCHED_FENCE();
#pragma unroll
    for (int k = 0; k < 4; k++) p1[k] = pk_bf16(e[8 + 2 * k], e[8 + 2 * k + 1]);
    SCHED_FENCE();
    o = MFMA32(vc.v1, __builtin_bit_cast(bf16x8, p1), o);
    if (m == 4 && zidx >= 0) {
      WAIT_VM(8);
      if (lane == 0) st_flag(flagp(p, zidx), tag);
    }
    sa = sb; kc = kx; vc = vx;
  }
  SCHED_FENCE();
  float l = swap_lo(o[10]);
  {
    float t = __builtin_fabsf(o[0]);
#pragma unroll
    for (int i = 1; i < 10; i++) t += __builtin_fabsf(o[i]);
    const bool good = l >= 0x1p-60f && l <= 0x1p60f && t <= 0x1p126f;
    if (__builtin_amdgcn_ballot_w64(!good) != 0) {
      const int mmin = rb >= 2 ? 0 : (4 * (2 - rb) - qt);
      const int kh = (lane >> 4) & 1, q4 = (lane >> 2) & 3, p4 = lane & 3, pi = ((p4 & 1) << 1) | (p4 >> 1);
      const int row = 18 * hh + 8 * h + q4, rowt = 18 * hh + 16 + (q4 & 1);
      const int vrow = c < 18 ? 18 * hh + c : ((c >= 20 && c < 28) ? 18 * hh : 36);
      const int vkey = vrow == 36 ? (hh ? 0 : 2) : (vrow & 15);
      auto scores = [&](int m) -> f32x16 {
        const int kt = qt + m;
        LAS char* a = att + A_K + row * 768 + ((kt >> 2) << 8) + (((kt & 3) ^ (row & 3)) << 6) + kh * 32 + pi * 8;
        const s16x4 lo = trr(a), hi = trr(a + 4 * 768), tl = trr(att + A_K + rowt * 768 + ((kt >> 2) << 8) + (((kt & 3) ^ (rowt & 3)) << 6) + kh * 32 + pi * 8);
        f32x16 s = MFMA32(cat44(lo, hi), q0, zero);
        s = MFMA32T(tl, q1, s);
        const int lo8 = m == 0 ? th + 1 : -64, hi8 = m == 8 ? th : 64;
#pragma unroll
        for (int i = 0; i < 16; i++) {
          const int idx = 16 * (i >> 3) + (i & 7);
          if (idx < lo8 || idx > hi8) s[i] = -__builtin_inff();
        }
        return s;
      };
      float mx = -__builtin_inff();
#pragma unroll 1
      for (int m = mmin; m <= 8; m++) {
        const f32x16 s = scores(m);
#pragma unroll
        for (int i = 0; i < 16; i++) mx = fmaxf(mx, s[i]);
      }
      { auto r = __builtin_amdgcn_permlane32_swap(__float_as_uint(mx), __float_as_uint(mx), false, false); mx = fmaxf(__uint_as_float(r[0]), __uint_as_float(r[1])); }
      o = zero;
#pragma unroll 1
      for (int m = mmin; m <= 8; m++) {
        const f32x16 s = scores(m);
        u32x4 p0, p1;
#pragma unroll
        for (int k = 0; k < 4; k++) { p0[k] = pk_bf16(ex2(s[2 * k] - mx), ex2(s[2 * k + 1] - mx)); p1[k] = pk_bf16(ex2(s[8 + 2 * k] - mx), ex2(s[8 + 2 * k + 1] - mx)); }
        const int kt = qt + m;
        LAS char* a = att + A_V + vrow * 768 + ((kt >> 2) << 8) + (((4 * (kt & 3) + h) ^ vkey) << 4);
        o = MFMA32(*(const LAS bf16x8*)a, __builtin_bit_cast(bf16x8, p0), o);
        o = MFMA32(*(const LAS bf16x8*)((LAS char*)((unsigned)(size_t)a ^ 32u)), __builtin_bit_cast(bf16x8, p1), o);
      }
      l = swap_lo(o[10]);
    }
  }
  const float inv = __builtin_amdgcn_rcpf(l);
  u32x4 om = {pk_bf16(o[0] * inv, o[1] * inv), pk_bf16(o[2] * inv, o[3] * inv), pk_bf16(o[4] * inv, o[5] * inv), pk_bf16(o[6] * inv, o[7] * inv)};
  const __amdgpu_buffer_rsrc_t ro = __builtin_amdgcn_make_buffer_rsrc((void*)(p.ws + WS_OM), 0, 128 * 16 * 1024, 0x00020000);
  const __amdgpu_buffer_rsrc_t rt = __builtin_amdgcn_make_buffer_rsrc((void*)(p.ws + WS_OT), 0, 32 * S * 4, 0x00020000);
  const int qt16 = 8 * rb + 2 * qt + (c >> 4);
  __builtin_amdgcn_raw_buffer_store_b128(om, ro, (unsigned)(((qt16 * 16 + j) * 64 + (c & 15) + 16 * (2 * hh + h)) * 16), 0, AUX_SC1);
  __builtin_amdgcn_raw_buffer_store_b32(pk_bf16(o[8] * inv, o[9] * inv), rt, h ? OOB : (unsigned)(((2 * j + hh) * S + 128 * rb + 32 * qt + c) * 4), 0, AUX_SC1);
}

DEV void p3_wave(const Params& p, LAS char* wl, int qb, int ns, int w, int lane, u64 tag) {
  const int r = lane & 15, g = lane >> 4, q4 = (lane >> 2) & 3, p4 = lane & 3;
  const int qt16 = 4 * qb + w;
  poll_block(p, F_O + (qt16 >> 1) * 32 + (lane & 31), true, tag);
  STAMPW(11, 0);
  const __amdgpu_buffer_rsrc_t ro = __builtin_amdgcn_make_buffer_rsrc((void*)(p.ws + WS_OM), 0, 128 * 16 * 1024, 0x00020000);
  const __amdgpu_buffer_rsrc_t rt = __builtin_amdgcn_make_buffer_rsrc((void*)(p.ws + WS_OT), 0, 32 * S * 4, 0x00020000);
  u32x4 of[18];
#pragma unroll
  for (int s = 0; s < 16; s++) of[s] = __builtin_amdgcn_raw_buffer_load_b128(ro, (unsigned)(lane * 16), (unsigned)((qt16 * 16 + s) * 1024), AUX_SC1);
#pragma unroll
  for (int s = 0; s < 2; s++)
#pragma unroll
    for (int w4 = 0; w4 < 4; w4++) of[16 + s][w4] = __builtin_amdgcn_raw_buffer_load_b32(rt, (unsigned)(((4 * g + w4) * S + 16 * qt16 + r) * 4), (unsigned)(16 * s * S * 4), AUX_SC1);
  FENCE();
  f32x4 acc[5];
#pragma unroll
  for (int i = 0; i < 5; i++) acc[i] = (f32x4){0.f, 0.f, 0.f, 0.f};
  LAS char* am = wl + (18 * (g >> 1) + 4 * (g & 1) + q4) * WO_RS + p4 * 8;
  LAS char* atl = wl + (18 * (4 * g + (q4 >> 1)) + 16 + (q4 & 1)) * WO_RS + p4 * 8;
#pragma unroll
  for (int s = 0; s < 18; s++) {
#pragma unroll
    for (int nt = 0; nt < 5; nt++) {
      LAS char* a = (s < 16 ? am + 36 * s * WO_RS : atl + 18 * 16 * (s - 16) * WO_RS) + nt * 32;
      const int tstep = s < 16 ? 8 * WO_RS : 36 * WO_RS;
      bf16x8 A = cat44(trr(a), trr(a + tstep));
      acc[nt] = __builtin_amdgcn_mfma_f32_16x16x32_bf16(A, __builtin_bit_cast(bf16x8, of[s]), acc[nt], 0, 0, 0);
    }
  }
  STAMPW(12, 0);
  GAS u32x2* out = (GAS u32x2*)(p.out + (size_t)(16 * qt16 + r) * 1152 + ns * 72 + 4 * g);
#pragma unroll
  for (int nt = 0; nt < 5; nt++)
    if (nt < 4 || g < 2) out[nt * 4] = (u32x2){pk_bf16(acc[nt][0], acc[nt][1]), pk_bf16(acc[nt][2], acc[nt][3])};
}
DEV void b3_wave(const Params& p, LAS char* zl, int tile, int w4, int lane, u64 tag) {
  const int tb = tile / 9, cb = tile - tb * 9;
  const int ch0 = 64 * cb + 16 * w4, c = lane & 1, r32 = lane >> 1;
  const GAS u32x4* qp = (const GAS u32x4*)p.q;
  u32x4 qv[2];
#pragma unroll
  for (int u = 0; u < 2; u++) qv[u] = qp[(64 * tb + 32 * u + r32) * 72 + (ch0 >> 3) + c];
  poll_block(p, F_Z + (tb >> 4) * D + ch0 + (lane & 15), true, tag);
  const __amdgpu_buffer_rsrc_t rz = __builtin_amdgcn_make_buffer_rsrc((void*)(p.ws + WS_ZT), 0, D * S * 2, 0x00020000);
#pragma unroll
  for (int u = 0; u < 2; u++)
    __builtin_amdgcn_raw_ptr_buffer_load_lds(rz, zl + u * 1024, 16, (unsigned)((ch0 + 8 * u + (lane >> 3)) * 4096 + tb * 128 + (((lane & 7) ^ (4 * u)) << 4)), 0, 0, AUX_SC1);
  WAIT_VM(0);
#pragma unroll
  for (int u = 0; u < 2; u++) {
    const int tl = 32 * u + r32;
    LAS char* zr = zl + c * 1024 + (((tl >> 3) ^ (4 * c)) << 4) + (tl & 7) * 2;
    u32x4 o;
#pragma unroll
    for (int e = 0; e < 4; e++) {
      const unsigned z0 = *(const LAS u16*)(zr + (2 * e) * 128), z1 = *(const LAS u16*)(zr + (2 * e + 1) * 128);
      const f32x2 qq = unpk(qv[u][e]);
      o[e] = pk_bf16(qq.x * __uint_as_float(z0 << 16), qq.y * __uint_as_float(z1 << 16));
    }
    *(GAS u32x4*)(p.out + (size_t)(64 * tb + tl) * 1152 + 576 + ch0 + 8 * c) = o;
  }
}

__global__ void __launch_bounds__(512, 2) fused(Params p) {
  extern __shared__ __attribute__((aligned(16))) char smem[];
  LAS char* lds = (LAS char*)smem;
  const int wave = __builtin_amdgcn_readfirstlane(threadIdx.x >> 6), lane = threadIdx.x & 63;
  const int b = blockIdx.x, xcd = b & 7, loc = b >> 3;
  const bool heavy = loc < 28;
  const int w4 = wave & 3, tid4 = w4 * 64 + lane;
  const bool isH = wave >= 4;
  STAMPW(0, 0);
  u64 tag;
  const int arb = heavy ? 2 + (loc >> 1) : ((loc - 28) >> 1), aj = 2 * xcd + (loc & 1);
  const int li = xcd * 4 + (loc - 28);
  const int tile0 = heavy ? xcd * 28 + loc : 224 + 2 * li;
  const int cch = heavy ? 2 * (xcd * 28 + loc) : 448 + 4 * li;
  int zidx = -1;
  LAS char* att = lds + L_ATT;

  if (heavy) {
    const int rb = (xcd >> 1) * 4 + (loc & 3), c = (xcd & 1) * 7 + (loc >> 2);
    if (isH) {
      const int hw = w4;
      const __amdgpu_buffer_rsrc_t rx = __builtin_amdgcn_make_buffer_rsrc((void*)(p.x + (size_t)rb * 128 * D), 0, 128 * D * 2, 0x00020000);
      const __amdgpu_buffer_rsrc_t rw = __builtin_amdgcn_make_buffer_rsrc((void*)p.wqkv, 0, D * 3 * D * 2, 0x00020000);
      const unsigned vx = (lane >> 3) * 1152 + (((lane & 7) ^ (lane >> 3)) << 4);
      const int cm = (c == 13) ? 7 : 15;
      const unsigned vw0 = (lane >> 4) * 3456 + c * 256 + ((((lane & 15) ^ ((lane >> 4) << 1)) & cm) << 4);
      const unsigned vw1 = (lane >> 4) * 3456 + c * 256 + ((((lane & 15) ^ (((lane >> 4) | 4) << 1)) & cm) << 4);
      FENCE();
      p1_dma_stage(rx, rw, lds, 0, hw, vx, vw0, vw1);
      p1_dma_stage(rx, rw, lds, 1, hw, vx, vw0, vw1);
      FENCE();
      HkRegs hr;
      hk_load(p, hr, cch, tid4);
      FENCE();
      WAIT_VM(14); barrier_raw();
      p1_dma_stage(rx, rw, lds, 2, hw, vx, vw0, vw1); FENCE();
      WAIT_VM(14); barrier_raw();
      p1_dma_stage(rx, rw, lds, 3, hw, vx, vw0, vw1); FENCE();
      hk_write_main(hr, lds + L_CONV, tid4, 0); FENCE();
      WAIT_VM(8); barrier_raw();
      p1_dma_stage(rx, rw, lds, 4, hw, vx, vw0, vw1); FENCE();
      hk_write_main(hr, lds + L_CONV, tid4, 1); FENCE();
      WAIT_VM(8); barrier_raw();
      p1_dma_stage(rx, rw, lds, 5, hw, vx, vw0, vw1); FENCE();
      hk_write_edge(hr, lds + L_CONV, tid4); FENCE();
      WAIT_VM(8); barrier_raw();
      p1_dma_stage(rx, rw, lds, 6, hw, vx, vw0, vw1); FENCE();
      tag = lds_tag(lds); FENCE();
      WAIT_VM(8); barrier_raw();
      p1_dma_stage(rx, rw, lds, 7, hw, vx, vw0, vw1); FENCE();
      WAIT_VM(8); barrier_raw();
      STAMP2(12, 4);
      p1_dma_stage(rx, rw, lds, 8, hw, vx, vw0, vw1); FENCE();
      STAMP2(13, 4);
      const int kvidx = F_KV + (cch >> 6) * 32 + (lane & 31);
      const u64 kvf = ld_flag(flagp(p, kvidx));
      FENCE();
      WAIT_VM(9); STAMP2(14, 4); barrier_raw();
      const bool ok = __builtin_amdgcn_ballot_w64(kvf != tag) == 0;
      STAMPW(15, 4);
      FENCE();
      if (ok) { ur_dma(p, lds + L_CONV, cch, hw, lane); FENCE(); WAIT_VM(2); } else { WAIT_VM(0); }
      WAIT_LGKM0();
      barrier_raw();
      barrier_raw();
      barrier_raw();
      const __amdgpu_buffer_rsrc_t rq = __builtin_amdgcn_make_buffer_rsrc((void*)(p.ws + WS_QKV), 0, 1728 * S * 2, 0x00020000);
      const bool cvalid = c * 128 + hw * 32 < 3 * D;
#pragma unroll
      for (int u = 0; u < 8; u++) {
        const int n = hw * 32 + u * 4 + (lane >> 4);
        u32x4 v = *(const LAS u32x4*)(lds + L_RING + n * 256 + (((lane & 15) ^ (n & 15)) << 4));
        __builtin_amdgcn_raw_buffer_store_b128(v, rq, cvalid ? (unsigned)((c * 128 + n) * (S * 2) + rb * 256 + (lane & 15) * 16) : 0x7ffffff0u, 0, AUX_SC1);
      }
      FENCE();
      if (ok) { WAIT_VM(8); }
      else { poll_block(p, kvidx, true, tag); ur_dma(p, lds + L_CONV, cch, hw, lane); FENCE(); WAIT_VM(0); }
      barrier_raw();
      __builtin_amdgcn_s_waitcnt(0x0F70);
      asm volatile("" ::: "memory");
      if (cvalid && lane == 0) st_flag(flagp(p, F_Q + rb * 64 + 4 * c + hw), tag);
      STAMPW(7, 4);
    } else {
      const int wm = wave & 1, wn = wave >> 1, g = lane >> 4;
      u64 gen = 0;
      if (wave == 0) gen = ld_flag(flagp(p, F_GEN));
      FENCE();
      {
        B1Regs br;
        b1_load(p, br, tile0, tid4);
        if (wave == 0 && lane < 10) *(LAS unsigned*)(lds + L_MISC + lane * 4) = 0u;
        b1_compute(br, lds + L_XCH, tile0, tid4);
      }
      FENCE();
      f32x4 acc[4][4];
#pragma unroll
      for (int i = 0; i < 4; i++)
#pragma unroll
        for (int j = 0; j < 4; j++) acc[i][j] = (f32x4){0.f, 0.f, 0.f, 0.f};
      const unsigned xa0 = L_RING + (64 * wm + (lane & 15)) * 128 + ((g ^ (lane & 7)) << 4);
      const int q = (lane >> 2) & 3, pp = lane & 3, f = q | ((g & 1) << 2);
      unsigned wb[4];
#pragma unroll
      for (int nt = 0; nt < 4; nt++) wb[nt] = L_RING + 16384 + (8 * g + q) * 256 + (((4 * wn + nt) ^ f) << 5) + pp * 8;
      bf16x8 a[2][4], bq[2][4];
      auto load_frags = [&](int buf, int s, int ks) {
        LAS char* base = lds + (s % 3) * STG;
#pragma unroll
        for (int mt = 0; mt < 4; mt++) a[buf][mt] = *(const LAS bf16x8*)(base + ((xa0 ^ (ks << 6)) + mt * 2048));
#pragma unroll
        for (int nt = 0; nt < 4; nt++) {
          s16x4 lo = trr(base + wb[nt] + ks * 8192), hi = trr(base + wb[nt] + ks * 8192 + 1024);
          bq[buf][nt] = cat44(lo, hi);
        }
      };
      auto mm = [&](int buf) {
#pragma unroll
        for (int mt = 0; mt < 4; mt++)
#pragma unroll
          for (int nt = 0; nt < 4; nt++) acc[mt][nt] = __builtin_amdgcn_mfma_f32_16x16x32_bf16(a[buf][mt], bq[buf][nt], acc[mt][nt], 0, 0, 0);
      };
      WAIT_LGKM0();
      barrier_raw();
      STAMPW(1, 0);
      load_frags(0, 0, 0);
      b1_store(p, lds + L_XCH, tile0, w4, lane);
#pragma unroll
      for (int s = 0; s < 9; s++) {
        SCHED_FENCE();
        load_frags(1, s, 1);
        mm(0);
        INTERLEAVE12();
        SCHED_FENCE();
        if (s == 3) {
          WAIT_VM(0);
          if (wave == 0) {
            asm volatile("" : "+v"(gen));
            tag = mk_tag(((u64)(unsigned)__builtin_amdgcn_readfirstlane((unsigned)(gen >> 32)) << 32) | (unsigned)__builtin_amdgcn_readfirstlane((unsigned)gen));
            if (lane == 0) *(LAS u32x2*)(lds + L_TAG) = (u32x2){(unsigned)tag, (unsigned)(tag >> 32)};
          }
        }
        WAIT_LGKM0();
        if (s == 6) STAMP2(15, 0);
        if (s < 8) {
          barrier_raw();
          STAMP2(s, 0);
          if (s == 3 && wave == 0 && lane < 2) st_flag(flagp(p, lane ? F_SEEN + b : F_KV + (tile0 % 9) * 32 + tile0 / 9), tag);
          SCHED_FENCE(); load_frags(0, s + 1, 0); mm(1); INTERLEAVE12();
        } else mm(1);
      }
      SCHED_FENCE();
      STAMPW(2, 0);
      barrier_raw();
      const float qs = 0.23570226039551584f * 1.4426950408889634f;
#pragma unroll
      for (int nt = 0; nt < 4; nt++) {
        const int n = 64 * wn + 16 * nt + (lane & 15);
        const float sc = (c * 128 + n < D) ? qs : 1.0f;
#pragma unroll
        for (int mt = 0; mt < 4; mt++) {
          const int t8 = 8 * wm + 2 * mt + (g >> 1);
          u32x2 v = {pk_bf16(acc[mt][nt][0] * sc, acc[mt][nt][1] * sc), pk_bf16(acc[mt][nt][2] * sc, acc[mt][nt][3] * sc)};
          *(LAS u32x2*)(lds + L_RING + n * 256 + ((t8 ^ (lane & 15)) << 4) + (g & 1) * 8) = v;
        }
      }
      WAIT_LGKM0();
      barrier_raw();
      STAMPW(3, 0);
      tag = lds_tag(lds);
      barrier_raw();
      STAMPW(4, 0);
      conv_run(p, lds + L_CONV, lds + L_XCH, lds + L_MISC, cch, w4, lane);
      STAMPW(6, 0);
      zidx = F_Z + (w4 & 1) * D + cch + (w4 >> 1);
    }
  } else {
    const int grp = wave >> 2;
    LAS char* cv = lds + (grp ? L_CONV2 : L_CONV);
    LAS char* xc = lds + (grp ? L_XCH2 : L_XCH);
    const int tile = tile0 + grp, ch = cch + 2 * grp;
    u64 gen = 0;
    if (wave == 0) gen = ld_flag(flagp(p, F_GEN));
    FENCE();
    B1Regs br; HkRegs hr;
    b1_load(p, br, tile, tid4);
    hk_load(p, hr, ch, tid4);
    if (wave == 0 && lane < 10) *(LAS unsigned*)(lds + L_MISC + lane * 4) = 0u;
    FENCE();
    b1_compute(br, xc, tile, tid4);
    hk_write_main(hr, cv, tid4, 0);
    hk_write_main(hr, cv, tid4, 1);
    hk_write_edge(hr, cv, tid4);
    if (wave == 0) {
      asm volatile("" : "+v"(gen));
      tag = mk_tag(((u64)(unsigned)__builtin_amdgcn_readfirstlane((unsigned)(gen >> 32)) << 32) | (unsigned)__builtin_amdgcn_readfirstlane((unsigned)gen));
      if (lane == 0) *(LAS u32x2*)(lds + L_TAG) = (u32x2){(unsigned)tag, (unsigned)(tag >> 32)};
    }
    WAIT_LGKM0(); barrier_raw();
    tag = lds_tag(lds);
    b1_store(p, xc, tile, w4, lane);
    WAIT_VM(0); barrier_raw();
    if (wave == 0) {
      if (lane < 3) st_flag(flagp(p, lane == 2 ? F_SEEN + b : F_KV + ((tile0 + lane) % 9) * 32 + (tile0 + lane) / 9), tag);
      poll_block<8>(p, F_KV + (cch >> 6) * 32 + (lane & 31), true, tag);
    }
    if (b == 255 && wave == 7) {
#pragma unroll 1
      for (int i = 0; i < 4; i++) poll_block<8>(p, F_SEEN + 64 * i + lane, true, tag);
      if (lane == 0) st_flag(flagp(p, F_GEN), tag);
    }
    barrier_raw();
    ur_dma(p, cv, ch, w4, lane);
    __builtin_amdgcn_s_waitcnt(0x0F70);
    asm volatile("" ::: "memory");
    barrier_raw();
    STAMPW(4, 0);
    conv_run(p, cv, xc, lds + L_MISC + 16 * grp, ch, w4, lane);
    STAMPW(6, 0);
    const int zi = F_Z + (w4 & 1) * D + ch + (w4 >> 1);
    WAIT_LGKM0(); barrier_raw();
    if (isH) { WAIT_VM(0); if (lane == 0) st_flag(flagp(p, zi), tag); }
    else zidx = zi;
  }
  if (isH) {
    AttOff ao;
    att_off(ao, arb, aj, lane);
    if (tid4 < 48) { const unsigned one = (8 * tid4 + 128 * (arb - 2) >= 0) ? 0x3f803f80u : 0u; *(LAS u32x4*)(att + A_ONES + tid4 * 16) = (u32x4){one, one, one, one}; }
    FENCE();
    if (w4 == 0) {
      const int l = lane < 18 ? lane : 0, r = l / 6, kind = (l - 6 * r) >> 1, e = l & 1;
      const int row = arb - 2 + r, col = kind * 576 + 36 * aj + 35 * e;
      const bool act = lane < 18 && row >= 0 && !(kind == 0 && r < 2);
      poll_block<2>(p, F_Q + (row < 0 ? 0 : row) * 64 + (col >> 5), act, tag);
      if (lane == 0) *(volatile LAS unsigned*)(lds + L_QRDY) = 1u;
    } else lds_wait(lds + L_QRDY);
    STAMPW(8, 4);
    att_dma(p, att, ao, w4);
    __builtin_amdgcn_s_waitcnt(0x0F70);
    asm volatile("" ::: "memory");
  }
  const int qb = 4 * xcd + (loc & 3), ns = loc >> 2;
  AttAddr aa;
  att_setup(aa, p, att, ns, wave, lane);
  WAIT_LGKM0();
  barrier_raw();
  STAMPW(9, 0);
  att_wave(p, att, aa, arb, aj, wave, lane, zidx, tag);
  FENCE();
  STAMPW(10, 0);
  WAIT_VM(0);
  if (lane == 0) st_flag(flagp(p, F_O + (arb * 4 + w4) * 32 + 2 * aj + (wave >> 2)), tag);
  WAIT_LGKM0();
  barrier_raw();
  if (isH) {
    b3_wave(p, lds + L_Z + w4 * 2048, tile0, w4, lane, tag);
    if (!heavy) b3_wave(p, lds + L_Z + 8192 + w4 * 2048, tile0 + 1, w4, lane, tag);
    STAMPW(14, 4);
  } else {
    p3_wave(p, lds + L_WO, qb, ns, w4, lane, tag);
    STAMPW(13, 0);
  }
}

extern "C" void kernel_launch(void* const* d_in, const int* in_sizes, int n_in, void* d_out, int out_size, void* d_ws, size_t ws_size, hipStream_t stream) {
  Params p;
  p.x = (const u16*)d_in[0]; p.wqkv = (const u16*)d_in[1]; p.wo = (const u16*)d_in[2]; p.q = (const u16*)d_in[3]; p.k = (const u16*)d_in[4]; p.v = (const u16*)d_in[5];
  p.sk = (const u16*)d_in[6]; p.hk = (const u16*)d_in[7]; p.Dd = (const u16*)d_in[8]; p.out = (u16*)d_out; p.ws = (char*)d_ws;
  constexpr int LDS_BYTES = 160 * 1024;
  (void)hipFuncSetAttribute((const void*)fused, hipFuncAttributeMaxDynamicSharedMemorySize, LDS_BYTES);
  fused<<<256, 512, LDS_BYTES, stream>>>(p);
}
```

```cpp
#include <hip/hip_runtime.h>
typedef unsigned short u16;
typedef unsigned long long u64;
typedef __bf16 bf16x8 __attribute__((ext_vector_type(8)));
typedef __bf16 bf16x2 __attribute__((ext_vector_type(2)));
typedef short s16x4 __attribute__((ext_vector_type(4)));
typedef float f32x2 __attribute__((ext_vector_type(2)));
typedef float f32x4 __attribute__((ext_vector_type(4)));
typedef float f32x16 __attribute__((ext_vector_type(16)));
typedef unsigned u32x2 __attribute__((ext_vector_type(2)));
typedef unsigned u32x4 __attribute__((ext_vector_type(4)));
#define GAS __attribute__((address_space(1)))
#define LAS __attribute__((address_space(3)))
#define DEV __device__ __forceinline__

constexpr int S = 2048, D = 576;
constexpr size_t WS_QKV = 0;
constexpr size_t WS_KVT = 8u << 20;
constexpr size_t WS_ZT = 11u << 20;
constexpr size_t WS_OM = 14u << 20;
constexpr size_t WS_OT = 17u << 20;
constexpr size_t WS_FLAG = 18u << 20;
constexpr int F_GEN = 0, F_SEEN = 128, F_KV = 512, F_Q = 1024, F_Z = 2048, F_O = 4096;

DEV unsigned pk_bf16(float a, float b) { f32x2 v = {a, b}; bf16x2 r = __builtin_convertvector(v, bf16x2); return __builtin_bit_cast(unsigned, r); }
DEV f32x2 unpk(unsigned w) { f32x2 r; r.x = __uint_as_float(w << 16); r.y = __uint_as_float(w & 0xffff0000u); return r; }
DEV void barrier_raw() { __builtin_amdgcn_s_barrier(); }
#define WAIT_VM(n) asm volatile("s_waitcnt vmcnt(" #n ")" ::: "memory")
#define WAIT_LGKM0() asm volatile("s_waitcnt lgkmcnt(0)" ::: "memory")
#define SCHED_FENCE() __builtin_amdgcn_sched_barrier(0)
#define FENCE() do { asm volatile("" ::: "memory"); __builtin_amdgcn_sched_barrier(0); } while (0)
#define INTERLEAVE12() do { _Pragma("unroll") for (int i_ = 0; i_ < 12; i_++) { __builtin_amdgcn_sched_group_barrier(0x008, 1, 0); __builtin_amdgcn_sched_group_barrier(0x100, 1, 0); } __builtin_amdgcn_sched_group_barrier(0x008, 4, 0); } while (0)
constexpr int AUX_SC1 = 16;
constexpr int SPIN_MAX = 20000;

struct Params { const u16 *x, *wqkv, *wo, *q, *k, *v, *sk, *hk, *Dd; u16* out; char* ws; };

constexpr int STG = 32768;
constexpr int CV_HP = 4160, CV_UR = 4 * CV_HP, CV_SZ = CV_UR + 4576;
constexpr int L_CONV = 0;
constexpr int L_XCH = 2 * CV_SZ;
constexpr int L_MISC = 58880;
constexpr int L_QRDY = L_MISC + 32, L_TAG = L_MISC + 40;
constexpr int L_RING = 65536;
constexpr int L_CONV2 = 65536, L_XCH2 = L_CONV2 + 2 * CV_SZ;
constexpr int L_ATT = 98304;
constexpr int A_K = 0, A_V = 36 * 768, A_ONES = 72 * 768, A_Q = 73 * 768;
constexpr int WO_RS = 160, L_WO = 0;
constexpr int L_Z = L_ATT;

DEV GAS u64* flagp(const Params& p, int idx) { return (GAS u64*)(p.ws + WS_FLAG) + idx * 16; }
DEV u64 ld_flag(GAS u64* a) { return __hip_atomic_load(a, __ATOMIC_RELAXED, __HIP_MEMORY_SCOPE_AGENT); }
DEV void st_flag(GAS u64* a, u64 v) { __hip_atomic_store(a, v, __ATOMIC_RELAXED, __HIP_MEMORY_SCOPE_AGENT); }
DEV u64 mk_tag(u64 gen) { return gen * 6364136223846793005ull + 1442695040888963407ull; }
template <int SLEEP = 4>
DEV void poll_block(const Params& p, int idx, bool active, u64 tag) {
  GAS u64* a = flagp(p, idx);
#pragma unroll 1
  for (int it = 0; it < SPIN_MAX; it++) {
    u64 v = tag;
    if (active) v = ld_flag(a);
    if (__builtin_amdgcn_ballot_w64(v != tag) == 0) break;
    __builtin_amdgcn_s_sleep(SLEEP);
  }
  asm volatile("" ::: "memory");
}
DEV u64 lds_tag(LAS char* lds) {
  const u32x2 t = *(const LAS u32x2*)(lds + L_TAG);
  return ((u64)(unsigned)__builtin_amdgcn_readfirstlane(t[1]) << 32) | (unsigned)__builtin_amdgcn_readfirstlane(t[0]);
}
DEV void lds_wait(LAS char* w) {
#pragma unroll 1
  for (int it = 0; it < (1 << 24); it++) {
    if (*(volatile LAS unsigned*)w == 1u) break;
    __builtin_amdgcn_s_sleep(1);
  }
  asm volatile("" ::: "memory");
}

DEV void p1_dma_stage(const __amdgpu_buffer_rsrc_t& rx, const __amdgpu_buffer_rsrc_t& rw, LAS char* lds, int s, int hw, unsigned vx, unsigned vw0, unsigned vw1) {
  LAS char* xs = lds + L_RING + (s % 3) * STG; LAS char* wsl = xs + 16384;
#pragma unroll
  for (int u = 0; u < 4; u++)
    __builtin_amdgcn_raw_ptr_buffer_load_lds(rx, xs + (4 * hw + u) * 1024, 16, vx, (4 * hw + u) * 8 * 1152 + s * 128, 0, 0);
#pragma unroll
  for (int u = 0; u < 4; u++)
    __builtin_amdgcn_raw_ptr_buffer_load_lds(rw, wsl + (4 * hw + u) * 1024, 16, (u & 2) ? vw1 : vw0, (64 * s + (4 * hw + u) * 4) * 3456, 0, 0);
}

struct B1Regs { u32x4 kr[5], vr[2], sw[4]; };
DEV void b1_load(const Params& p, B1Regs& r, int tile, int tid4) {
  const int tb = tile / 9, cb = tile - tb * 9, tp = tid4 >> 3, c8 = tid4 & 7;
  const GAS u32x4* kp = (const GAS u32x4*)p.k; const GAS u32x4* vp = (const GAS u32x4*)p.v; const GAS u32x4* skp = (const GAS u32x4*)p.sk;
  const int t0 = 64 * tb + 2 * tp, ch = 8 * cb + c8;
#pragma unroll
  for (int i = 0; i < 5; i++) { const int t = t0 - 3 + i; r.kr[i] = kp[(t < 0 ? 0 : t) * 72 + ch]; }
#pragma unroll
  for (int i = 0; i < 2; i++) r.vr[i] = vp[(t0 + i) * 72 + ch];
#pragma unroll
  for (int i = 0; i < 4; i++) r.sw[i] = skp[ch * 4 + i];
}
DEV void b1_compute(B1Regs& r, LAS char* stg, int tile, int tid4) {
  const int tb = tile / 9, tp = tid4 >> 3, c8 = tid4 & 7;
  const int t0 = 64 * tb + 2 * tp;
#pragma unroll
  for (int i = 0; i < 3; i++) if (t0 - 3 + i < 0) r.kr[i] = (u32x4){0, 0, 0, 0};
  LAS char* o = stg + c8 * 1024 + ((((31 - tp) >> 2) ^ c8) << 4) + ((31 - tp) & 3) * 4;
#pragma unroll
  for (int pr = 0; pr < 4; pr++) {
    const f32x2 a0 = unpk(r.sw[pr][0]), a1 = unpk(r.sw[pr][1]), b0 = unpk(r.sw[pr][2]), b1 = unpk(r.sw[pr][3]);
    const f32x2 w0 = {a0.x, b0.x}, w1 = {a0.y, b0.y}, w2 = {a1.x, b1.x}, w3 = {a1.y, b1.y};
    f32x2 kf[5];
#pragma unroll
    for (int i = 0; i < 5; i++) kf[i] = unpk(r.kr[i][pr]);
    f32x2 s0 = kf[0] * w3 + kf[1] * w2; s0 += kf[2] * w1; s0 += kf[3] * w0; s0 = s0 * unpk(r.vr[0][pr]);
    f32x2 s1 = kf[1] * w3 + kf[2] * w2; s1 += kf[3] * w1; s1 += kf[4] * w0; s1 = s1 * unpk(r.vr[1][pr]);
    *(LAS unsigned*)(o + (2 * pr) * 128) = pk_bf16(s1.x, s0.x);
    *(LAS unsigned*)(o + (2 * pr + 1) * 128) = pk_bf16(s1.y, s0.y);
  }
}
DEV void b1_store(const Params& p, LAS char* stg, int tile, int w4, int lane) {
  const int tb = tile / 9, cb = tile - tb * 9;
  const __amdgpu_buffer_rsrc_t rk = __builtin_amdgcn_make_buffer_rsrc((void*)(p.ws + WS_KVT), 0, D * S * 2, 0x00020000);
#pragma unroll
  for (int u = 0; u < 2; u++) {
    const int ii = w4 * 2 + u, ch = 8 * ii + (lane >> 3);
    u32x4 v = *(const LAS u32x4*)(stg + ch * 128 + (lane & 7) * 16);
    __builtin_amdgcn_raw_buffer_store_b128(v, rk, (unsigned)((64 * cb + ch) * 4096 + (1984 - 64 * tb) * 2 + (((lane & 7) ^ ii) << 4)), 0, AUX_SC1);
  }
}

struct HkRegs { u32x4 c0[3]; u32x2 c1[3]; };
DEV void hk_load(const Params& p, HkRegs& r, int ch0, int tid4) {
  const GAS u32x4* hk = (const GAS u32x4*)p.hk;
#pragma unroll
  for (int cs = 0; cs < 2; cs++) {
    const int i0 = tid4 - 2, i1 = tid4 - 1;
    r.c0[cs] = hk[(ch0 + cs) * 256 + (i0 < 0 ? 0 : i0)];
    r.c1[cs] = *(const GAS u32x2*)(hk + (ch0 + cs) * 256 + (i1 < 0 ? 0 : i1));
  }
  const int cs = (tid4 >> 1) & 1, i0 = 254 + (tid4 & 1);
  r.c0[2] = hk[(ch0 + cs) * 256 + i0];
  r.c1[2] = *(const GAS u32x2*)(hk + (ch0 + cs) * 256 + 255);
}
DEV void hk_put(LAS char* b, u32x4 c0, u32x2 c1) {
  const unsigned a0 = __builtin_amdgcn_alignbit(c0[1], c0[0], 16), a1 = __builtin_amdgcn_alignbit(c0[2], c0[1], 16), a2 = __builtin_amdgcn_alignbit(c0[3], c0[2], 16),
                 a3 = __builtin_amdgcn_alignbit(c1[0], c0[3], 16), a4 = __builtin_amdgcn_alignbit(c1[1], c1[0], 16);
  *(LAS u32x4*)(b) = (u32x4){a0, a1, a2, a3};
  *(LAS u32x4*)(b + CV_HP) = (u32x4){c0[1], c0[2], c0[3], c1[0]};
  *(LAS u32x4*)(b + 2 * CV_HP) = (u32x4){a1, a2, a3, a4};
  *(LAS u32x4*)(b + 3 * CV_HP) = (u32x4){c0[2], c0[3], c1[0], c1[1]};
}
DEV void hk_write_main(HkRegs& r, LAS char* cv, int tid4, int cs) {
  const u32x4 z4 = {0, 0, 0, 0};
  const u32x2 z2 = {0, 0};
  u32x4 c0 = r.c0[cs]; u32x2 c1 = r.c1[cs];
  if (tid4 < 2) c0 = z4;
  if (tid4 < 1) c1 = z2;
  hk_put(cv + cs * CV_SZ + tid4 * 16, c0, c1);
}
DEV void hk_write_edge(HkRegs& r, LAS char* cv, int tid4) {
  const u32x4 z4 = {0, 0, 0, 0};
  const u32x2 z2 = {0, 0};
  if (tid4 < 4) hk_put(cv + ((tid4 >> 1) & 1) * CV_SZ + (256 + (tid4 & 1)) * 16, r.c0[2], (tid4 & 1) ? z2 : r.c1[2]);
  if (tid4 < 60) { const int cs = tid4 >= 30; *(LAS u32x4*)(cv + cs * CV_SZ + CV_UR + (256 + tid4 - 30 * cs) * 16) = z4; }
}
DEV void ur_dma(const Params& p, LAS char* cv, int ch0, int w4, int lane) {
  const __amdgpu_buffer_rsrc_t rk = __builtin_amdgcn_make_buffer_rsrc((void*)(p.ws + WS_KVT), 0, D * S * 2, 0x00020000);
  const int cs = w4 >> 1;
#pragma unroll
  for (int u = 0; u < 2; u++) {
    const int pc = (w4 & 1) * 2 + u;
    __builtin_amdgcn_raw_ptr_buffer_load_lds(rk, cv + cs * CV_SZ + CV_UR + pc * 1024, 16, (unsigned)(lane * 16), (ch0 + cs) * 4096 + pc * 1024, 0, AUX_SC1);
  }
}
DEV void conv_run(const Params& p, LAS char* cv, LAS char* xch, LAS char* xfl, int ch0, int w4, int lane) {
  const int cs = w4 >> 1, half = w4 & 1;
  const int r = lane & 15, g = lane >> 4;
  unsigned dbits = p.Dd[ch0 + cs];
  FENCE();
  f32x4 acc[8];
  {
    LAS char* hp = cv + cs * CV_SZ + (r & 3) * CV_HP + ((r & ~3) + 8 * g + half * 128) * 2;
    LAS char* ur = cv + cs * CV_SZ + CV_UR + (2032 - 16 * r + 8 * g + half * 128) * 2;
#pragma unroll
    for (int i = 0; i < 8; i++) acc[i] = (f32x4){0.f, 0.f, 0.f, 0.f};
#pragma unroll
    for (int ph = 0; ph < 4; ph++) {
      bf16x8 A[8], F[8];
#pragma unroll
      for (int a = 0; a < 8; a++) {
        u32x2 lo = *(const LAS u32x2*)(hp + (256 * a + 32 * ph) * 2), hi = *(const LAS u32x2*)(hp + (256 * a + 32 * ph) * 2 + 8);
        A[a] = __builtin_bit_cast(bf16x8, (u32x4){lo[0], lo[1], hi[0], hi[1]});
        F[a] = *(const LAS bf16x8*)(ur + (32 * ph - 256 * a) * 2);
      }
#pragma unroll
      for (int a = 0; a < 8; a++)
#pragma unroll
        for (int n = 0; n + a < 8; n++) acc[a + n] = __builtin_amdgcn_mfma_f32_16x16x32_bf16(A[a], F[n], acc[a + n], 0, 0, 0);
    }
  }

  {
    LAS char* x = xch + w4 * 4096 + lane * 16;
#pragma unroll
    for (int i = 0; i < 4; i++) {
      f32x4 a = acc[i], b = acc[4 + i];
      asm volatile("" : "+v"(a), "+v"(b));
      *(LAS f32x4*)(x + i * 1024) = half ? a : b;
    }
    WAIT_LGKM0();
    if (lane == 0) *(volatile LAS unsigned*)(xfl + w4 * 4) = 1u;
    lds_wait(xfl + (w4 ^ 1) * 4);
  }
  LAS char* x = xch + (w4 ^ 1) * 4096 + lane * 16;
  const float Dv = __uint_as_float(dbits << 16);
  LAS char* zst = cv + cs * CV_SZ + half * 2048;
  LAS char* ur = cv + cs * CV_SZ + CV_UR;
#pragma unroll
  for (int i = 0; i < 4; i++) {
    f32x4 ya = acc[i], yb = acc[4 + i];
    asm volatile("" : "+v"(ya), "+v"(yb));
    f32x4 y = (half ? yb : ya) + *(const LAS f32x4*)(x + i * 1024);
    const int t = 256 * (4 * half + i) + 16 * r + 4 * g;
    u32x2 u = *(const LAS u32x2*)(ur + (2044 - t) * 2);
    y[3] += __uint_as_float(u[0] << 16) * Dv; y[2] += __uint_as_float(u[0] & 0xffff0000u) * Dv;
    y[1] += __uint_as_float(u[1] << 16) * Dv; y[0] += __uint_as_float(u[1] & 0xffff0000u) * Dv;
    *(LAS u32x2*)(zst + (256 * i + 16 * r + 4 * g) * 2) = (u32x2){pk_bf16(y[0], y[1]), pk_bf16(y[2], y[3])};
  }
  WAIT_LGKM0();
  const __amdgpu_buffer_rsrc_t rz = __builtin_amdgcn_make_buffer_rsrc((void*)(p.ws + WS_ZT), 0, D * S * 2, 0x00020000);
#pragma unroll
  for (int i = 0; i < 2; i++) {
    u32x4 v = *(const LAS u32x4*)(zst + i * 1024 + lane * 16);
    __builtin_amdgcn_raw_buffer_store_b128(v, rz, (unsigned)((ch0 + cs) * 4096 + half * 2048 + i * 1024 + lane * 16), 0, AUX_SC1);
  }
}

struct AttOff { unsigned k[3], v[3], q; };
constexpr unsigned OOB = 0x7ffffff0u;
DEV void att_off(AttOff& o, int rb, int j, int lane) {
  const int tok2 = 256 * (rb - 2);
#pragma unroll
  for (int t = 0; t < 3; t++) {
    const int lin = 64 * t + lane, row = lin / 48, sl = lin - row * 48;
    const int ck = tok2 + (((((sl >> 2) ^ row) << 2) | (sl & 3)) << 4), cv = tok2 + ((sl ^ row) << 4);
    o.k[t] = ck < 0 ? OOB : (unsigned)((576 + 36 * j + row) * 4096 + ck);
    o.v[t] = cv < 0 ? OOB : (unsigned)((1152 + 36 * j + row) * 4096 + cv);
  }
  const int row = lane >> 4, sl = lane & 15;
  o.q = (unsigned)((36 * j + row) * 4096 + rb * 256 + (((((sl >> 2) ^ row) << 2) | (sl & 3)) << 4));
}
DEV void att_dma(const Params& p, LAS char* att, const AttOff& o, int w) {
  const __amdgpu_buffer_rsrc_t rq = __builtin_amdgcn_make_buffer_rsrc((void*)(p.ws + WS_QKV), 0, 1728 * S * 2, 0x00020000);
#pragma unroll
  for (int u = 0; u < 3; u++) {
    const int a = w + 4 * u;
    if (u < 2 || w == 0) {
#pragma unroll
      for (int t = 0; t < 3; t++) __builtin_amdgcn_raw_ptr_buffer_load_lds(rq, att + A_K + (3 * a + t) * 1024, 16, o.k[t], a * 16384, 0, AUX_SC1);
    }
    const int av = u < 2 ? a : 8;
    if (u < 2 || w == 1) {
#pragma unroll
      for (int t = 0; t < 3; t++) __builtin_amdgcn_raw_ptr_buffer_load_lds(rq, att + A_V + (3 * av + t) * 1024, 16, o.v[t] ^ ((av & 3) << 6), av * 16384, 0, AUX_SC1);
    }
  }
  const int q0 = w == 0 ? 0 : (w == 1 ? 1 : (w == 2 ? 2 : 6)), qn = w < 2 ? 1 : (w == 2 ? 4 : 3);
#pragma unroll
  for (int u = 0; u < 4; u++)
    if (u < qn) __builtin_amdgcn_raw_ptr_buffer_load_lds(rq, att + A_Q + (q0 + u) * 1024, 16, o.q, (q0 + u) * 16384, 0, AUX_SC1);
}
struct WoDma { u32x4 rs; unsigned off; int ch; unsigned l0; };
DEV void wo_init(WoDma& d, const Params& p, int ns, int w, int lane) {
  const u64 a = (u64)p.wo;
  d.rs = (u32x4){(unsigned)a, (unsigned)(a >> 32) & 0xffffu, (unsigned)(D * D * 2), 0x00020000u};
  const int lin = 64 * w + lane, row = lin / 10;
  d.ch = lin - row * 10;
  d.off = (unsigned)(row * 1152 + ns * 144 + d.ch * 16);
  d.l0 = (unsigned)(L_WO + w * 1024);
}
DEV void wo_piece(WoDma& d, int u) {
  unsigned keep;
  asm volatile("s_mov_b32 %0, m0\n\ts_mov_b32 m0, %1\n\ts_nop 0\n\tbuffer_load_dwordx4 %2, %3, 0 offen lds\n\ts_mov_b32 m0, %0" : "=&s"(keep) : "s"(d.l0 + u * 8192), "v"(d.off), "s"(d.rs) : "memory");
  d.ch += 2; d.off += 51 * 1152 + 32;
  if (d.ch >= 10) { d.ch -= 10; d.off += 1152 - 160; }
}
DEV bf16x8 cat44(s16x4 lo, s16x4 hi) { return __builtin_bit_cast(bf16x8, __builtin_shufflevector(lo, hi, 0, 1, 2, 3, 4, 5, 6, 7)); }
DEV s16x4 trr(LAS char* a) { return __builtin_amdgcn_ds_read_tr16_b64_v4i16((LAS s16x4*)a); }
#define MFMA32(a, b, c) __builtin_amdgcn_mfma_f32_32x32x16_bf16((a), (b), (c), 0, 0, 0)
#define MFMA32T(a, b, c) __builtin_amdgcn_mfma_f32_32x32x8bf16_1k((a), (b), (c), 0, 0, 0)
DEV float ex2(float x) { return __builtin_amdgcn_exp2f(x); }
DEV float swap_lo(float x) { auto r = __builtin_amdgcn_permlane32_swap(__float_as_uint(x), __float_as_uint(x), false, false); return __uint_as_float(r[0]); }

struct KFrag { s16x4 lo, hi, t; };
struct VFrag { bf16x8 v0, v1; };
struct AttAddr { LAS char *ak[4], *at[4], *av[4], *aw[4], *aq, *aqt; WoDma wd; };
DEV void att_setup(AttAddr& A, const Params& p, LAS char* att, int ns, int wave, int lane) {
  const int hh = wave >> 2, qt = wave & 3;
  const int c = lane & 31, h = lane >> 5;
  const int kh = (lane >> 4) & 1, q4 = (lane >> 2) & 3, p4 = lane & 3, pi = ((p4 & 1) << 1) | (p4 >> 1);
  const int row = 18 * hh + 8 * h + q4, rowt = 18 * hh + 16 + (q4 & 1);
  const int vrow = c < 18 ? 18 * hh + c : ((c >= 20 && c < 28) ? 18 * hh : 36);
  const int vkey = vrow == 36 ? (hh ? 0 : 2) : (vrow & 15);
  A.aq = att + A_Q + row * 256 + ((qt ^ (row & 3)) << 6) + kh * 32 + p4 * 8;
  A.aqt = att + A_Q + rowt * 256 + ((qt ^ (rowt & 3)) << 6) + kh * 32 + p4 * 8;
#pragma unroll
  for (int mm = 0; mm < 4; mm++) {
    const int kt = qt + mm;
    A.ak[mm] = att + A_K + row * 768 + ((kt >> 2) << 8) + (((kt & 3) ^ (row & 3)) << 6) + kh * 32 + pi * 8;
    A.at[mm] = att + A_K + rowt * 768 + ((kt >> 2) << 8) + (((kt & 3) ^ (rowt & 3)) << 6) + kh * 32 + pi * 8;
    A.av[mm] = att + A_V + vrow * 768 + ((kt >> 2) << 8) + (((4 * (kt & 3) + h) ^ vkey) << 4);
    A.aw[mm] = (LAS char*)((unsigned)(size_t)A.av[mm] ^ 32u);
    asm volatile("" : "+v"(A.ak[mm]), "+v"(A.at[mm]), "+v"(A.av[mm]), "+v"(A.aw[mm]));
  }
  wo_init(A.wd, p, ns, wave, lane);
  asm volatile("" : "+v"(A.aq), "+v"(A.aqt), "+v"(A.wd.off), "+v"(A.wd.ch));
}
DEV void att_wave(const Params& p, LAS char* att, AttAddr& A, int rb, int j, int wave, int lane, int zidx, u64 tag) {
  const int hh = wave >> 2, qt = wave & 3;
  const int c = lane & 31, h = lane >> 5;
  bf16x8 q0; s16x4 q1;
  q0 = cat44(trr(A.aq), trr(A.aq + 1024));
  { const u32x2 tt = __builtin_bit_cast(u32x2, trr(A.aqt)); q1 = __builtin_bit_cast(s16x4, (u32x2){h ? 0u : tt[0], 0u}); }
  LAS char **ak = A.ak, **at = A.at, **av = A.av, **aw = A.aw;
  WoDma& wd = A.wd;
  auto ldk = [&](int m) { KFrag k; LAS char* a = ak[m & 3] + (m >> 2) * 256; k.lo = trr(a); k.hi = trr(a + 4 * 768); k.t = trr(at[m & 3] + (m >> 2) * 256); return k; };
  auto ldv = [&](int m) { VFrag v; v.v0 = *(const LAS bf16x8*)(av[m & 3] + (m >> 2) * 256); v.v1 = *(const LAS bf16x8*)(aw[m & 3] + (m >> 2) * 256); return v; };
  f32x16 zero;
#pragma unroll
  for (int i = 0; i < 16; i++) zero[i] = 0.f;
  const int th = c - 8 * h;
  f32x16 o, sa, sb;
  {
    KFrag k0 = ldk(0), k8 = ldk(8), kn = ldk(1);
    VFrag v8 = ldv(8), v0 = ldv(0);
    SCHED_FENCE();
    sa = MFMA32(cat44(k0.lo, k0.hi), q0, zero);
    sb = MFMA32(cat44(k8.lo, k8.hi), q0, zero);
    sa = MFMA32T(k0.t, q1, sa);
    sb = MFMA32T(k8.t, q1, sb);
    f32x16 s1 = MFMA32(cat44(kn.lo, kn.hi), q0, zero);
    s1 = MFMA32T(kn.t, q1, s1);
    SCHED_FENCE();
    u32x4 pf, pd;
#pragma unroll
    for (int g = 0; g < 2; g++) {
#pragma unroll
      for (int k = 0; k < 4; k++) {
        int tv = th - 16 * g - 2 * k;
        asm volatile("" : "+v"(tv));
        const bool c0 = tv < 0, c1 = tv < 1;
        const float a0 = c0 ? sa[8 * g + 2 * k] : sb[8 * g + 2 * k], a1 = c1 ? sa[8 * g + 2 * k + 1] : sb[8 * g + 2 * k + 1];
        const unsigned mk = (c0 ? 0xffffu : 0u) | (c1 ? 0xffff0000u : 0u);
        const unsigned pp = pk_bf16(ex2(a0), ex2(a1));
        pf[k] = pp & mk; pd[k] = pp & ~mk;
        SCHED_FENCE();
      }
      o = MFMA32(g ? v8.v1 : v8.v0, __builtin_bit_cast(bf16x8, pd), g ? o : zero);
      o = MFMA32(g ? v0.v1 : v0.v0, __builtin_bit_cast(bf16x8, pf), o);
    }
    sa = s1;
    SCHED_FENCE();
  }
  KFrag kc = ldk(2);
  VFrag vc = ldv(1);
#pragma unroll
  for (int m = 1; m < 8; m++) {
    KFrag kx; VFrag vx;
    SCHED_FENCE();
    if (m < 7) { wo_piece(wd, 2 * m - 2); wo_piece(wd, 2 * m - 1); }
    if (m < 6) kx = ldk(m + 2);
    if (m < 7) vx = ldv(m + 1);
    SCHED_FENCE();
    if (m < 7) sb = MFMA32(cat44(kc.lo, kc.hi), q0, zero);
    SCHED_FENCE();
    float e[16];
#pragma unroll
    for (int i = 0; i < 8; i++) e[i] = ex2(sa[i]);
    SCHED_FENCE();
    if (m < 7) sb = MFMA32T(kc.t, q1, sb);
    SCHED_FENCE();
    u32x4 p0, p1;
#pragma unroll
    for (int k = 0; k < 4; k++) p0[k] = pk_bf16(e[2 * k], e[2 * k + 1]);
#pragma unroll
    for (int i = 8; i < 16; i++) e[i] = ex2(sa[i]);
    SCHED_FENCE();
    o = MFMA32(vc.v0, __builtin_bit_cast(bf16x8, p0), o);
    SCHED_FENCE();
#pragma unroll
    for (int k = 0; k < 4; k++) p1[k] = pk_bf16(e[8 + 2 * k], e[8 + 2 * k + 1]);
    SCHED_FENCE();
    o = MFMA32(vc.v1, __builtin_bit_cast(bf16x8, p1), o);
    if (m == 4 && zidx >= 0) {
      WAIT_VM(8);
      if (lane == 0) st_flag(flagp(p, zidx), tag);
    }
    sa = sb; kc = kx; vc = vx;
  }
  SCHED_FENCE();
  float l = swap_lo(o[10]);
  {
    float t = __builtin_fabsf(o[0]);
#pragma unroll
    for (int i = 1; i < 10; i++) t += __builtin_fabsf(o[i]);
    const bool good = l >= 0x1p-60f && l <= 0x1p60f && t <= 0x1p126f;
    if (__builtin_amdgcn_ballot_w64(!good) != 0) {
      const int mmin = rb >= 2 ? 0 : (4 * (2 - rb) - qt);
      const int kh = (lane >> 4) & 1, q4 = (lane >> 2) & 3, p4 = lane & 3, pi = ((p4 & 1) << 1) | (p4 >> 1);
      const int row = 18 * hh + 8 * h + q4, rowt = 18 * hh + 16 + (q4 & 1);
      const int vrow = c < 18 ? 18 * hh + c : ((c >= 20 && c < 28) ? 18 * hh : 36);
      const int vkey = vrow == 36 ? (hh ? 0 : 2) : (vrow & 15);
      auto scores = [&](int m) -> f32x16 {
        const int kt = qt + m;
        LAS char* a = att + A_K + row * 768 + ((kt >> 2) << 8) + (((kt & 3) ^ (row & 3)) << 6) + kh * 32 + pi * 8;
        const s16x4 lo = trr(a), hi = trr(a + 4 * 768), tl = trr(att + A_K + rowt * 768 + ((kt >> 2) << 8) + (((kt & 3) ^ (rowt & 3)) << 6) + kh * 32 + pi * 8);
        f32x16 s = MFMA32(cat44(lo, hi), q0, zero);
        s = MFMA32T(tl, q1, s);
        const int lo8 = m == 0 ? th + 1 : -64, hi8 = m == 8 ? th : 64;
#pragma unroll
        for (int i = 0; i < 16; i++) {
          const int idx = 16 * (i >> 3) + (i & 7);
          if (idx < lo8 || idx > hi8) s[i] = -__builtin_inff();
        }
        return s;
      };
      float mx = -__builtin_inff();
#pragma unroll 1
      for (int m = mmin; m <= 8; m++) {
        const f32x16 s = scores(m);
#pragma unroll
        for (int i = 0; i < 16; i++) mx = fmaxf(mx, s[i]);
      }
      { auto r = __builtin_amdgcn_permlane32_swap(__float_as_uint(mx), __float_as_uint(mx), false, false); mx = fmaxf(__uint_as_float(r[0]), __uint_as_float(r[1])); }
      o = zero;
#pragma unroll 1
      for (int m = mmin; m <= 8; m++) {
        const f32x16 s = scores(m);
        u32x4 p0, p1;
#pragma unroll
        for (int k = 0; k < 4; k++) { p0[k] = pk_bf16(ex2(s[2 * k] - mx), ex2(s[2 * k + 1] - mx)); p1[k] = pk_bf16(ex2(s[8 + 2 * k] - mx), ex2(s[8 + 2 * k + 1] - mx)); }
        const int kt = qt + m;
        LAS char* a = att + A_V + vrow * 768 + ((kt >> 2) << 8) + (((4 * (kt & 3) + h) ^ vkey) << 4);
        o = MFMA32(*(const LAS bf16x8*)a, __builtin_bit_cast(bf16x8, p0), o);
        o = MFMA32(*(const LAS bf16x8*)((LAS char*)((unsigned)(size_t)a ^ 32u)), __builtin_bit_cast(bf16x8, p1), o);
      }
      l = swap_lo(o[10]);
    }
  }
  const float inv = __builtin_amdgcn_rcpf(l);
  u32x4 om = {pk_bf16(o[0] * inv, o[1] * inv), pk_bf16(o[2] * inv, o[3] * inv), pk_bf16(o[4] * inv, o[5] * inv), pk_bf16(o[6] * inv, o[7] * inv)};
  const __amdgpu_buffer_rsrc_t ro = __builtin_amdgcn_make_buffer_rsrc((void*)(p.ws + WS_OM), 0, 128 * 16 * 1024, 0x00020000);
  const __amdgpu_buffer_rsrc_t rt = __builtin_amdgcn_make_buffer_rsrc((void*)(p.ws + WS_OT), 0, 32 * S * 4, 0x00020000);
  const int qt16 = 8 * rb + 2 * qt + (c >> 4);
  __builtin_amdgcn_raw_buffer_store_b128(om, ro, (unsigned)(((qt16 * 16 + j) * 64 + (c & 15) + 16 * (2 * hh + h)) * 16), 0, AUX_SC1);
  __builtin_amdgcn_raw_buffer_store_b32(pk_bf16(o[8] * inv, o[9] * inv), rt, h ? OOB : (unsigned)(((2 * j + hh) * S + 128 * rb + 32 * qt + c) * 4), 0, AUX_SC1);
}

DEV void p3_wave(const Params& p, LAS char* wl, int qb, int ns, int w, int lane, u64 tag) {
  const int r = lane & 15, g = lane >> 4, q4 = (lane >> 2) & 3, p4 = lane & 3;
  const int qt16 = 4 * qb + w;
  poll_block(p, F_O + (qt16 >> 1) * 32 + (lane & 31), true, tag);

  const __amdgpu_buffer_rsrc_t ro = __builtin_amdgcn_make_buffer_rsrc((void*)(p.ws + WS_OM), 0, 128 * 16 * 1024, 0x00020000);
  const __amdgpu_buffer_rsrc_t rt = __builtin_amdgcn_make_buffer_rsrc((void*)(p.ws + WS_OT), 0, 32 * S * 4, 0x00020000);
  u32x4 of[18];
#pragma unroll
  for (int s = 0; s < 16; s++) of[s] = __builtin_amdgcn_raw_buffer_load_b128(ro, (unsigned)(lane * 16), (unsigned)((qt16 * 16 + s) * 1024), AUX_SC1);
#pragma unroll
  for (int s = 0; s < 2; s++)
#pragma unroll
    for (int w4 = 0; w4 < 4; w4++) of[16 + s][w4] = __builtin_amdgcn_raw_buffer_load_b32(rt, (unsigned)(((4 * g + w4) * S + 16 * qt16 + r) * 4), (unsigned)(16 * s * S * 4), AUX_SC1);
  FENCE();
  f32x4 acc[5];
#pragma unroll
  for (int i = 0; i < 5; i++) acc[i] = (f32x4){0.f, 0.f, 0.f, 0.f};
  LAS char* am = wl + (18 * (g >> 1) + 4 * (g & 1) + q4) * WO_RS + p4 * 8;
  LAS char* atl = wl + (18 * (4 * g + (q4 >> 1)) + 16 + (q4 & 1)) * WO_RS + p4 * 8;
#pragma unroll
  for (int s = 0; s < 18; s++) {
#pragma unroll
    for (int nt = 0; nt < 5; nt++) {
      LAS char* a = (s < 16 ? am + 36 * s * WO_RS : atl + 18 * 16 * (s - 16) * WO_RS) + nt * 32;
      const int tstep = s < 16 ? 8 * WO_RS : 36 * WO_RS;
      bf16x8 A = cat44(trr(a), trr(a + tstep));
      acc[nt] = __builtin_amdgcn_mfma_f32_16x16x32_bf16(A, __builtin_bit_cast(bf16x8, of[s]), acc[nt], 0, 0, 0);
    }
  }

  GAS u32x2* out = (GAS u32x2*)(p.out + (size_t)(16 * qt16 + r) * 1152 + ns * 72 + 4 * g);
#pragma unroll
  for (int nt = 0; nt < 5; nt++)
    if (nt < 4 || g < 2) out[nt * 4] = (u32x2){pk_bf16(acc[nt][0], acc[nt][1]), pk_bf16(acc[nt][2], acc[nt][3])};
}
DEV void b3_wave(const Params& p, LAS char* zl, int tile, int w4, int lane, u64 tag) {
  const int tb = tile / 9, cb = tile - tb * 9;
  const int ch0 = 64 * cb + 16 * w4, c = lane & 1, r32 = lane >> 1;
  const GAS u32x4* qp = (const GAS u32x4*)p.q;
  u32x4 qv[2];
#pragma unroll
  for (int u = 0; u < 2; u++) qv[u] = qp[(64 * tb + 32 * u + r32) * 72 + (ch0 >> 3) + c];
  poll_block(p, F_Z + (tb >> 4) * D + ch0 + (lane & 15), true, tag);
  const __amdgpu_buffer_rsrc_t rz = __builtin_amdgcn_make_buffer_rsrc((void*)(p.ws + WS_ZT), 0, D * S * 2, 0x00020000);
#pragma unroll
  for (int u = 0; u < 2; u++)
    __builtin_amdgcn_raw_ptr_buffer_load_lds(rz, zl + u * 1024, 16, (unsigned)((ch0 + 8 * u + (lane >> 3)) * 4096 + tb * 128 + (((lane & 7) ^ (4 * u)) << 4)), 0, 0, AUX_SC1);
  WAIT_VM(0);
#pragma unroll
  for (int u = 0; u < 2; u++) {
    const int tl = 32 * u + r32;
    LAS char* zr = zl + c * 1024 + (((tl >> 3) ^ (4 * c)) << 4) + (tl & 7) * 2;
    u32x4 o;
#pragma unroll
    for (int e = 0; e < 4; e++) {
      const unsigned z0 = *(const LAS u16*)(zr + (2 * e) * 128), z1 = *(const LAS u16*)(zr + (2 * e + 1) * 128);
      const f32x2 qq = unpk(qv[u][e]);
      o[e] = pk_bf16(qq.x * __uint_as_float(z0 << 16), qq.y * __uint_as_float(z1 << 16));
    }
    *(GAS u32x4*)(p.out + (size_t)(64 * tb + tl) * 1152 + 576 + ch0 + 8 * c) = o;
  }
}

__global__ void __launch_bounds__(512, 2) fused(Params p) {
  extern __shared__ __attribute__((aligned(16))) char smem[];
  LAS char* lds = (LAS char*)smem;
  const int wave = __builtin_amdgcn_readfirstlane(threadIdx.x >> 6), lane = threadIdx.x & 63;
  const int b = blockIdx.x, xcd = b & 7, loc = b >> 3;
  const bool heavy = loc < 28;
  const int w4 = wave & 3, tid4 = w4 * 64 + lane;
  const bool isH = wave >= 4;

  u64 tag;
  const u64 t_begin = __builtin_amdgcn_s_memrealtime();
  const int arb = heavy ? 2 + (loc >> 1) : ((loc - 28) >> 1), aj = 2 * xcd + (loc & 1);
  const int li = xcd * 4 + (loc - 28);
  const int tile0 = heavy ? xcd * 28 + loc : 224 + 2 * li;
  const int cch = heavy ? 2 * (xcd * 28 + loc) : 448 + 4 * li;
  int zidx = -1;
  LAS char* att = lds + L_ATT;

  if (heavy) {
    const int rb = (xcd >> 1) * 4 + (loc & 3), c = (xcd & 1) * 7 + (loc >> 2);
    if (isH) {
      const int hw = w4;
      const __amdgpu_buffer_rsrc_t rx = __builtin_amdgcn_make_buffer_rsrc((void*)(p.x + (size_t)rb * 128 * D), 0, 128 * D * 2, 0x00020000);
      const __amdgpu_buffer_rsrc_t rw = __builtin_amdgcn_make_buffer_rsrc((void*)p.wqkv, 0, D * 3 * D * 2, 0x00020000);
      const unsigned vx = (lane >> 3) * 1152 + (((lane & 7) ^ (lane >> 3)) << 4);
      const int cm = (c == 13) ? 7 : 15;
      const unsigned vw0 = (lane >> 4) * 3456 + c * 256 + ((((lane & 15) ^ ((lane >> 4) << 1)) & cm) << 4);
      const unsigned vw1 = (lane >> 4) * 3456 + c * 256 + ((((lane & 15) ^ (((lane >> 4) | 4) << 1)) & cm) << 4);
      FENCE();
      p1_dma_stage(rx, rw, lds, 0, hw, vx, vw0, vw1);
      p1_dma_stage(rx, rw, lds, 1, hw, vx, vw0, vw1);
      FENCE();
      HkRegs hr;
      hk_load(p, hr, cch, tid4);
      FENCE();
      WAIT_VM(14); barrier_raw();
      p1_dma_stage(rx, rw, lds, 2, hw, vx, vw0, vw1); FENCE();
      WAIT_VM(14); barrier_raw();
      p1_dma_stage(rx, rw, lds, 3, hw, vx, vw0, vw1); FENCE();
      hk_write_main(hr, lds + L_CONV, tid4, 0); FENCE();
      WAIT_VM(8); barrier_raw();
      p1_dma_stage(rx, rw, lds, 4, hw, vx, vw0, vw1); FENCE();
      hk_write_main(hr, lds + L_CONV, tid4, 1); FENCE();
      WAIT_VM(8); barrier_raw();
      p1_dma_stage(rx, rw, lds, 5, hw, vx, vw0, vw1); FENCE();
      hk_write_edge(hr, lds + L_CONV, tid4); FENCE();
      WAIT_VM(8); barrier_raw();
      p1_dma_stage(rx, rw, lds, 6, hw, vx, vw0, vw1); FENCE();
      tag = lds_tag(lds); FENCE();
      WAIT_VM(8); barrier_raw();
      p1_dma_stage(rx, rw, lds, 7, hw, vx, vw0, vw1); FENCE();
      WAIT_VM(8); barrier_raw();

      p1_dma_stage(rx, rw, lds, 8, hw, vx, vw0, vw1); FENCE();

      const int kvidx = F_KV + (cch >> 6) * 32 + (lane & 31);
      const u64 kvf = ld_flag(flagp(p, kvidx));
      FENCE();
      WAIT_VM(9); barrier_raw();
      const bool ok = __builtin_amdgcn_ballot_w64(kvf != tag) == 0;

      FENCE();
      if (ok) { ur_dma(p, lds + L_CONV, cch, hw, lane); FENCE(); WAIT_VM(2); } else { WAIT_VM(0); }
      WAIT_LGKM0();
      barrier_raw();
      barrier_raw();
      barrier_raw();
      const __amdgpu_buffer_rsrc_t rq = __builtin_amdgcn_make_buffer_rsrc((void*)(p.ws + WS_QKV), 0, 1728 * S * 2, 0x00020000);
      const bool cvalid = c * 128 + hw * 32 < 3 * D;
#pragma unroll
      for (int u = 0; u < 8; u++) {
        const int n = hw * 32 + u * 4 + (lane >> 4);
        u32x4 v = *(const LAS u32x4*)(lds + L_RING + n * 256 + (((lane & 15) ^ (n & 15)) << 4));
        __builtin_amdgcn_raw_buffer_store_b128(v, rq, cvalid ? (unsigned)((c * 128 + n) * (S * 2) + rb * 256 + (lane & 15) * 16) : 0x7ffffff0u, 0, AUX_SC1);
      }
      FENCE();
      if (ok) { WAIT_VM(8); }
      else { poll_block(p, kvidx, true, tag); ur_dma(p, lds + L_CONV, cch, hw, lane); FENCE(); WAIT_VM(0); }
      barrier_raw();
      __builtin_amdgcn_s_waitcnt(0x0F70);
      asm volatile("" ::: "memory");
      if (cvalid && lane == 0) st_flag(flagp(p, F_Q + rb * 64 + 4 * c + hw), tag);

    } else {
      const int wm = wave & 1, wn = wave >> 1, g = lane >> 4;
      u64 gen = 0;
      if (wave == 0) gen = ld_flag(flagp(p, F_GEN));
      FENCE();
      {
        B1Regs br;
        b1_load(p, br, tile0, tid4);
        if (wave == 0 && lane < 10) *(LAS unsigned*)(lds + L_MISC + lane * 4) = 0u;
        b1_compute(br, lds + L_XCH, tile0, tid4);
      }
      FENCE();
      f32x4 acc[4][4];
#pragma unroll
      for (int i = 0; i < 4; i++)
#pragma unroll
        for (int j = 0; j < 4; j++) acc[i][j] = (f32x4){0.f, 0.f, 0.f, 0.f};
      const unsigned xa0 = L_RING + (64 * wm + (lane & 15)) * 128 + ((g ^ (lane & 7)) << 4);
      const int q = (lane >> 2) & 3, pp = lane & 3, f = q | ((g & 1) << 2);
      unsigned wb[4];
#pragma unroll
      for (int nt = 0; nt < 4; nt++) wb[nt] = L_RING + 16384 + (8 * g + q) * 256 + (((4 * wn + nt) ^ f) << 5) + pp * 8;
      bf16x8 a[2][4], bq[2][4];
      auto load_frags = [&](int buf, int s, int ks) {
        LAS char* base = lds + (s % 3) * STG;
#pragma unroll
        for (int mt = 0; mt < 4; mt++) a[buf][mt] = *(const LAS bf16x8*)(base + ((xa0 ^ (ks << 6)) + mt * 2048));
#pragma unroll
        for (int nt = 0; nt < 4; nt++) {
          s16x4 lo = trr(base + wb[nt] + ks * 8192), hi = trr(base + wb[nt] + ks * 8192 + 1024);
          bq[buf][nt] = cat44(lo, hi);
        }
      };
      auto mm = [&](int buf) {
#pragma unroll
        for (int mt = 0; mt < 4; mt++)
#pragma unroll
          for (int nt = 0; nt < 4; nt++) acc[mt][nt] = __builtin_amdgcn_mfma_f32_16x16x32_bf16(a[buf][mt], bq[buf][nt], acc[mt][nt], 0, 0, 0);
      };
      WAIT_LGKM0();
      barrier_raw();

      load_frags(0, 0, 0);
      b1_store(p, lds + L_XCH, tile0, w4, lane);
#pragma unroll
      for (int s = 0; s < 9; s++) {
        SCHED_FENCE();
        load_frags(1, s, 1);
        mm(0);
        INTERLEAVE12();
        SCHED_FENCE();
        if (s == 3) {
          WAIT_VM(0);
          if (wave == 0) {
            asm volatile("" : "+v"(gen));
            tag = mk_tag(((u64)(unsigned)__builtin_amdgcn_readfirstlane((unsigned)(gen >> 32)) << 32) | (unsigned)__builtin_amdgcn_readfirstlane((unsigned)gen));
            if (lane == 0) *(LAS u32x2*)(lds + L_TAG) = (u32x2){(unsigned)tag, (unsigned)(tag >> 32)};
          }
        }
        WAIT_LGKM0();

        if (s < 8) {
          barrier_raw();

          if (s == 3 && wave == 0 && lane < 2) st_flag(flagp(p, lane ? F_SEEN + b : F_KV + (tile0 % 9) * 32 + tile0 / 9), tag);
          SCHED_FENCE(); load_frags(0, s + 1, 0); mm(1); INTERLEAVE12();
        } else mm(1);
      }
      SCHED_FENCE();

      barrier_raw();
      const float qs = 0.23570226039551584f * 1.4426950408889634f;
#pragma unroll
      for (int nt = 0; nt < 4; nt++) {
        const int n = 64 * wn + 16 * nt + (lane & 15);
        const float sc = (c * 128 + n < D) ? qs : 1.0f;
#pragma unroll
        for (int mt = 0; mt < 4; mt++) {
          const int t8 = 8 * wm + 2 * mt + (g >> 1);
          u32x2 v = {pk_bf16(acc[mt][nt][0] * sc, acc[mt][nt][1] * sc), pk_bf16(acc[mt][nt][2] * sc, acc[mt][nt][3] * sc)};
          *(LAS u32x2*)(lds + L_RING + n * 256 + ((t8 ^ (lane & 15)) << 4) + (g & 1) * 8) = v;
        }
      }
      WAIT_LGKM0();
      barrier_raw();

      tag = lds_tag(lds);
      barrier_raw();

      conv_run(p, lds + L_CONV, lds + L_XCH, lds + L_MISC, cch, w4, lane);

      zidx = F_Z + (w4 & 1) * D + cch + (w4 >> 1);
    }
  } else {
    const int grp = wave >> 2;
    LAS char* cv = lds + (grp ? L_CONV2 : L_CONV);
    LAS char* xc = lds + (grp ? L_XCH2 : L_XCH);
    const int tile = tile0 + grp, ch = cch + 2 * grp;
    u64 gen = 0;
    if (wave == 0) gen = ld_flag(flagp(p, F_GEN));
    FENCE();
    B1Regs br; HkRegs hr;
    b1_load(p, br, tile, tid4);
    hk_load(p, hr, ch, tid4);
    if (wave == 0 && lane < 10) *(LAS unsigned*)(lds + L_MISC + lane * 4) = 0u;
    FENCE();
    b1_compute(br, xc, tile, tid4);
    hk_write_main(hr, cv, tid4, 0);
    hk_write_main(hr, cv, tid4, 1);
    hk_write_edge(hr, cv, tid4);
    if (wave == 0) {
      asm volatile("" : "+v"(gen));
      tag = mk_tag(((u64)(unsigned)__builtin_amdgcn_readfirstlane((unsigned)(gen >> 32)) << 32) | (unsigned)__builtin_amdgcn_readfirstlane((unsigned)gen));
      if (lane == 0) *(LAS u32x2*)(lds + L_TAG) = (u32x2){(unsigned)tag, (unsigned)(tag >> 32)};
    }
    WAIT_LGKM0(); barrier_raw();
    tag = lds_tag(lds);
    b1_store(p, xc, tile, w4, lane);
    WAIT_VM(0); barrier_raw();
    if (wave == 0) {
      if (lane < 3) st_flag(flagp(p, lane == 2 ? F_SEEN + b : F_KV + ((tile0 + lane) % 9) * 32 + (tile0 + lane) / 9), tag);
      poll_block<8>(p, F_KV + (cch >> 6) * 32 + (lane & 31), true, tag);
    }
    if (b == 255 && wave == 7) {
#pragma unroll 1
      for (int i = 0; i < 4; i++) poll_block<8>(p, F_SEEN + 64 * i + lane, true, tag);
      if (lane == 0) st_flag(flagp(p, F_GEN), tag);
    }
    barrier_raw();
    ur_dma(p, cv, ch, w4, lane);
    __builtin_amdgcn_s_waitcnt(0x0F70);
    asm volatile("" ::: "memory");
    barrier_raw();

    conv_run(p, cv, xc, lds + L_MISC + 16 * grp, ch, w4, lane);

    const int zi = F_Z + (w4 & 1) * D + ch + (w4 >> 1);
    WAIT_LGKM0(); barrier_raw();
    if (isH) { WAIT_VM(0); if (lane == 0) st_flag(flagp(p, zi), tag); }
    else zidx = zi;
  }
  if (isH) {
    AttOff ao;
    att_off(ao, arb, aj, lane);
    if (tid4 < 48) { const unsigned one = (8 * tid4 + 128 * (arb - 2) >= 0) ? 0x3f803f80u : 0u; *(LAS u32x4*)(att + A_ONES + tid4 * 16) = (u32x4){one, one, one, one}; }
    FENCE();
    if (w4 == 0) {
      const int l = lane < 18 ? lane : 0, r = l / 6, kind = (l - 6 * r) >> 1, e = l & 1;
      const int row = arb - 2 + r, col = kind * 576 + 36 * aj + 35 * e;
      const bool act = lane < 18 && row >= 0 && !(kind == 0 && r < 2);
      poll_block<2>(p, F_Q + (row < 0 ? 0 : row) * 64 + (col >> 5), act, tag);
      if (lane == 0) *(volatile LAS unsigned*)(lds + L_QRDY) = 1u;
    } else lds_wait(lds + L_QRDY);

    att_dma(p, att, ao, w4);
    __builtin_amdgcn_s_waitcnt(0x0F70);
    asm volatile("" ::: "memory");
  }
  const int qb = 4 * xcd + (loc & 3), ns = loc >> 2;
  AttAddr aa;
  att_setup(aa, p, att, ns, wave, lane);
  WAIT_LGKM0();
  barrier_raw();

  att_wave(p, att, aa, arb, aj, wave, lane, zidx, tag);
  FENCE();

  WAIT_VM(0);
  if (lane == 0) st_flag(flagp(p, F_O + (arb * 4 + w4) * 32 + 2 * aj + (wave >> 2)), tag);
  WAIT_LGKM0();
  barrier_raw();
  if (isH) {
    b3_wave(p, lds + L_Z + w4 * 2048, tile0, w4, lane, tag);
    if (!heavy) b3_wave(p, lds + L_Z + 8192 + w4 * 2048, tile0 + 1, w4, lane, tag);

  } else {
    p3_wave(p, lds + L_WO, qb, ns, w4, lane, tag);
  }
  for (int it = 0; it < 100000; it++) { if (__builtin_amdgcn_s_memrealtime() - t_begin >= 2000) break; __builtin_amdgcn_s_sleep(2); }
}

extern "C" void kernel_launch(void* const* d_in, const int* in_sizes, int n_in, void* d_out, int out_size, void* d_ws, size_t ws_size, hipStream_t stream) {
  Params p;
  p.x = (const u16*)d_in[0]; p.wqkv = (const u16*)d_in[1]; p.wo = (const u16*)d_in[2]; p.q = (const u16*)d_in[3]; p.k = (const u16*)d_in[4]; p.v = (const u16*)d_in[5];
  p.sk = (const u16*)d_in[6]; p.hk = (const u16*)d_in[7]; p.Dd = (const u16*)d_in[8]; p.out = (u16*)d_out; p.ws = (char*)d_ws;
  constexpr int LDS_BYTES = 160 * 1024;
  (void)hipFuncSetAttribute((const void*)fused, hipFuncAttributeMaxDynamicSharedMemorySize, LDS_BYTES);
  fused<<<256, 512, LDS_BYTES, stream>>>(p);
}
```

```cpp
#include <hip/hip_runtime.h>
typedef unsigned short u16;
typedef unsigned long long u64;
typedef __bf16 bf16x8 __attribute__((ext_vector_type(8)));
typedef __bf16 bf16x2 __attribute__((ext_vector_type(2)));
typedef short s16x4 __attribute__((ext_vector_type(4)));
typedef float f32x2 __attribute__((ext_vector_type(2)));
typedef float f32x4 __attribute__((ext_vector_type(4)));
typedef float f32x16 __attribute__((ext_vector_type(16)));
typedef unsigned u32x2 __attribute__((ext_vector_type(2)));
typedef unsigned u32x4 __attribute__((ext_vector_type(4)));
#define GAS __attribute__((address_space(1)))
#define LAS __attribute__((address_space(3)))
#define DEV __device__ __forceinline__

constexpr int S = 2048, D = 576;
constexpr size_t WS_QKV = 0;
constexpr size_t WS_KVT = 8u << 20;
constexpr size_t WS_ZT = 11u << 20;
constexpr size_t WS_OM = 14u << 20;
constexpr size_t WS_OT = 17u << 20;
constexpr size_t WS_FLAG = 18u << 20;
constexpr int F_GEN = 0, F_SEEN = 128, F_Q = 1024, F_Z = 2048, F_O = 4096, F_KV = 6144;
constexpr size_t WS_STAMP = 32u << 20;
#define STAMPW(i, w)
#define STAMP2(i, w)
#define STAMP3(i)
#define STAMP_INIT()
#define STAMP_FLUSH()

DEV unsigned pk_bf16(float a, float b) { f32x2 v = {a, b}; bf16x2 r = __builtin_convertvector(v, bf16x2); return __builtin_bit_cast(unsigned, r); }
DEV f32x2 unpk(unsigned w) { f32x2 r; r.x = __uint_as_float(w << 16); r.y = __uint_as_float(w & 0xffff0000u); return r; }
DEV void barrier_raw() { __builtin_amdgcn_s_barrier(); }
#define WAIT_VM(n) asm volatile("s_waitcnt vmcnt(" #n ")" ::: "memory")
#define WAIT_LGKM0() asm volatile("s_waitcnt lgkmcnt(0)" ::: "memory")
#define SCHED_FENCE() __builtin_amdgcn_sched_barrier(0)
#define FENCE() do { asm volatile("" ::: "memory"); __builtin_amdgcn_sched_barrier(0); } while (0)
#define INTERLEAVE12() do { _Pragma("unroll") for (int i_ = 0; i_ < 12; i_++) { __builtin_amdgcn_sched_group_barrier(0x008, 1, 0); __builtin_amdgcn_sched_group_barrier(0x100, 1, 0); } __builtin_amdgcn_sched_group_barrier(0x008, 4, 0); } while (0)
constexpr int AUX_SC1 = 16;
constexpr int SPIN_MAX = 20000;
#define RAWIT 2
#define PFKV 1
#define ROT 1
#define KVPRE 30
#define KVSL 8
#define QSL 8
#define PFWO 1

struct Params { const u16 *x, *wqkv, *wo, *q, *k, *v, *sk, *hk, *Dd; u16* out; char* ws; };

constexpr int STG = 32768;
constexpr int CV_HP = 4160, CV_HP0 = 0, CV_UR0 = 16640, CV_URS = 4576, CV_HP1 = 28608;
constexpr int L_CONV = 0;
constexpr int L_MISC = 25856;
constexpr int L_XF0 = L_MISC, L_QRDY = L_MISC + 32, L_KVRDY = L_MISC + 36, L_TAG = L_MISC + 40, L_RDY = L_MISC + 48;
constexpr int L_RK = 0, L_RV = 9216, L_RS = 26112, L_B1 = 49152, L_XCH = 45312;
constexpr int L_DUMMY = 27648;
constexpr int L_CIMG = 65536;
constexpr int L_CONV2 = 45312;
constexpr int L_ATT = 98304;
constexpr int A_K = 0, A_V = 36 * 768, A_ONES = 72 * 768, A_Q = 73 * 768;
constexpr int WO_RS = 160, L_WO = 0;
constexpr int L_Z = L_ATT;
DEV constexpr int ring_slot(int s) { return (s & 3) == 0 ? 65536 : ((s & 3) == 1 ? 32768 : ((s & 3) == 2 ? 98304 : 131072)); }

DEV GAS u64* flagp(const Params& p, int idx) { return (GAS u64*)(p.ws + WS_FLAG) + idx * 16; }
DEV u64 ld_flag(GAS u64* a) { return __hip_atomic_load(a, __ATOMIC_RELAXED, __HIP_MEMORY_SCOPE_AGENT); }
DEV void st_flag(GAS u64* a, u64 v) { __hip_atomic_store(a, v, __ATOMIC_RELAXED, __HIP_MEMORY_SCOPE_AGENT); }
DEV u64 mk_tag(u64 gen) { return gen * 6364136223846793005ull + 1442695040888963407ull; }
template <int SLEEP = 4>
DEV void poll_block(const Params& p, int idx, bool active, u64 tag) {
  GAS u64* a = flagp(p, idx);
#pragma unroll 1
  for (int it = 0; it < SPIN_MAX; it++) {
    u64 v = tag;
    if (active) v = ld_flag(a);
    if (__builtin_amdgcn_ballot_w64(v != tag) == 0) break;
    __builtin_amdgcn_s_sleep(SLEEP);
  }
  asm volatile("" ::: "memory");
}
DEV u64 lds_tag(LAS char* lds) {
  const u32x2 t = *(const LAS u32x2*)(lds + L_TAG);
  return ((u64)(unsigned)__builtin_amdgcn_readfirstlane(t[1]) << 32) | (unsigned)__builtin_amdgcn_readfirstlane(t[0]);
}
DEV void lds_wait(LAS char* w) {
#pragma unroll 1
  for (int it = 0; it < (1 << 24); it++) {
    if (*(volatile LAS unsigned*)w == 1u) break;
    __builtin_amdgcn_s_sleep(1);
  }
  asm volatile("" ::: "memory");
}

DEV void lds_wait_rdy(LAS char* rdy, int partner) {
#pragma unroll 1
  for (int it = 0; it < (1 << 24); it++) {
    const unsigned u = *(volatile LAS unsigned*)(rdy + partner * 4);
    const u32x4 h = *(volatile LAS u32x4*)(rdy + 16);
    if ((u & h[0] & h[1] & h[2] & h[3]) == 1u) break;
    __builtin_amdgcn_s_sleep(1);
  }
  asm volatile("" ::: "memory");
}

DEV u32x4 mk_rs(const void* base, unsigned bytes) { const u64 a = (u64)base; return (u32x4){(unsigned)a, (unsigned)(a >> 32) & 0xffffu, bytes, 0x00020000u}; }
DEV void dma16(u32x4 rs, unsigned ldsa, unsigned voff, unsigned soff) {
  unsigned keep;
  asm volatile("s_mov_b32 %0, m0\n\ts_mov_b32 m0, %1\n\ts_nop 0\n\tbuffer_load_dwordx4 %2, %3, %4 offen lds\n\ts_mov_b32 m0, %0" : "=&s"(keep) : "s"(ldsa), "v"(voff), "s"(rs), "s"(soff) : "memory");
}
DEV void dma16c(u32x4 rs, unsigned ldsa, unsigned voff, unsigned soff) {
  unsigned keep;
  asm volatile("s_mov_b32 %0, m0\n\ts_mov_b32 m0, %1\n\ts_nop 0\n\tbuffer_load_dwordx4 %2, %3, %4 offen sc1 lds\n\ts_mov_b32 m0, %0" : "=&s"(keep) : "s"(ldsa), "v"(voff), "s"(rs), "s"(soff) : "memory");
}
DEV void dma4(u32x4 rs, unsigned ldsa, unsigned voff) {
  unsigned keep;
  asm volatile("s_mov_b32 %0, m0\n\ts_mov_b32 m0, %1\n\ts_nop 0\n\tbuffer_load_dword %2, %3, 0 offen lds\n\ts_mov_b32 m0, %0" : "=&s"(keep) : "s"(ldsa), "v"(voff), "s"(rs) : "memory");
}
constexpr unsigned OOB = 0x7ffffff0u;

DEV void p1_dma_stage(u32x4 rx, u32x4 rw, int s, int s0, int hw, unsigned vx, unsigned vw0, unsigned vw1) {
  const unsigned xs = (unsigned)ring_slot(s), wsl = xs + 16384;
  int ks = s + s0; ks = ks >= 9 ? ks - 9 : ks;
#pragma unroll
  for (int u = 0; u < 4; u++) dma16(rx, xs + (4 * hw + u) * 1024, vx, (unsigned)((4 * hw + u) * 8 * 1152 + ks * 128));
#pragma unroll
  for (int u = 0; u < 4; u++) dma16(rw, wsl + (4 * hw + u) * 1024, (u & 2) ? vw1 : vw0, (unsigned)((64 * ks + (4 * hw + u) * 4) * 3456));
}

struct B1Regs { u32x4 kr[5], vr[2], sw[4]; };
DEV void b1_load(const Params& p, B1Regs& r, int tb, int cb, int tid4) {
  const int tp = tid4 >> 3, c8 = tid4 & 7;
  const GAS u32x4* kp = (const GAS u32x4*)p.k; const GAS u32x4* vp = (const GAS u32x4*)p.v; const GAS u32x4* skp = (const GAS u32x4*)p.sk;
  const int t0 = 64 * tb + 2 * tp, ch = 8 * cb + c8;
#pragma unroll
  for (int i = 0; i < 5; i++) { const int t = t0 - 3 + i; r.kr[i] = kp[(t < 0 ? 0 : t) * 72 + ch]; }
#pragma unroll
  for (int i = 0; i < 2; i++) r.vr[i] = vp[(t0 + i) * 72 + ch];
#pragma unroll
  for (int i = 0; i < 4; i++) r.sw[i] = skp[ch * 4 + i];
}
struct RawOff { unsigned vo, ve; };
DEV void b1_raw_off(RawOff& o, int tb, int cb, int w, int lane) {
  o.vo = (unsigned)((64 * tb + (lane >> 3)) * 1152 + cb * 128 + (lane & 7) * 16);
  o.ve = w == 0 ? (tb ? o.vo - 9216u : OOB) : (lane < 32 ? (unsigned)(cb * 512 + lane * 16) : OOB);
  asm volatile("" : "+v"(o.vo), "+v"(o.ve));
}
DEV void b1_raw_dma(const Params& p, const RawOff& o, int w) {
  const u32x4 rk = mk_rs(p.k, S * D * 2), rv = mk_rs(p.v, S * D * 2), rs = mk_rs(p.sk, D * 4 * 2);
  dma16(rk, (unsigned)(L_RK + (1 + w) * 1024), o.vo, (unsigned)(w * 9216));
  dma16(rk, (unsigned)(L_RK + (5 + w) * 1024), o.vo, (unsigned)((4 + w) * 9216));
  dma16(rv, (unsigned)(L_RV + w * 1024), o.vo, (unsigned)(w * 9216));
  dma16(rv, (unsigned)(L_RV + (4 + w) * 1024), o.vo, (unsigned)((4 + w) * 9216));
  if (w == 0) dma16(rk, (unsigned)L_RK, o.ve, 0u);
  if (w == 1) dma16(rs, (unsigned)L_RS, o.ve, 0u);
}
DEV void b1_read_raw(B1Regs& r, LAS char* lds, int tid4) {
  const int tp = tid4 >> 3, c8 = tid4 & 7;
#pragma unroll
  for (int i = 0; i < 4; i++) r.sw[i] = *(const LAS u32x4*)(lds + L_RS + c8 * 64 + i * 16);
#pragma unroll
  for (int i = 0; i < 5; i++) r.kr[i] = *(const LAS u32x4*)(lds + L_RK + (2 * tp + 5 + i) * 128 + c8 * 16);
#pragma unroll
  for (int i = 0; i < 2; i++) r.vr[i] = *(const LAS u32x4*)(lds + L_RV + (2 * tp + i) * 128 + c8 * 16);
}
DEV void b1_pin(B1Regs& r) {
#pragma unroll
  for (int i = 0; i < 4; i++) asm volatile("" : "+v"(r.sw[i]));
#pragma unroll
  for (int i = 0; i < 5; i++) asm volatile("" : "+v"(r.kr[i]));
#pragma unroll
  for (int i = 0; i < 2; i++) asm volatile("" : "+v"(r.vr[i]));
}
DEV void b1_zero_halo(B1Regs& r, int tb, int tid4) {
  const int t0 = 64 * tb + 2 * (tid4 >> 3);
#pragma unroll
  for (int i = 0; i < 3; i++) if (t0 - 3 + i < 0) r.kr[i] = (u32x4){0, 0, 0, 0};
}
template <int P0 = 0, int P1 = 4>
DEV void b1_compute(B1Regs& r, LAS char* stg, int tid4) {
  const int tp = tid4 >> 3, c8 = tid4 & 7;
  LAS char* o = stg + c8 * 1024 + ((((31 - tp) >> 2) ^ c8) << 4) + ((31 - tp) & 3) * 4;
#pragma unroll
  for (int pr = P0; pr < P1; pr++) {
    const f32x2 a0 = unpk(r.sw[pr][0]), a1 = unpk(r.sw[pr][1]), b0 = unpk(r.sw[pr][2]), b1 = unpk(r.sw[pr][3]);
    const f32x2 w0 = {a0.x, b0.x}, w1 = {a0.y, b0.y}, w2 = {a1.x, b1.x}, w3 = {a1.y, b1.y};
    f32x2 kf[5];
#pragma unroll
    for (int i = 0; i < 5; i++) kf[i] = unpk(r.kr[i][pr]);
    f32x2 s0 = kf[0] * w3 + kf[1] * w2; s0 += kf[2] * w1; s0 += kf[3] * w0; s0 = s0 * unpk(r.vr[0][pr]);
    f32x2 s1 = kf[1] * w3 + kf[2] * w2; s1 += kf[3] * w1; s1 += kf[4] * w0; s1 = s1 * unpk(r.vr[1][pr]);
    *(LAS unsigned*)(o + (2 * pr) * 128) = pk_bf16(s1.x, s0.x);
    *(LAS unsigned*)(o + (2 * pr + 1) * 128) = pk_bf16(s1.y, s0.y);
  }
}
DEV void b1_store(const Params& p, LAS char* stg, int tb, int cb, int w4, int lane) {
  const __amdgpu_buffer_rsrc_t rk = __builtin_amdgcn_make_buffer_rsrc((void*)(p.ws + WS_KVT), 0, D * S * 2, 0x00020000);
#pragma unroll
  for (int u = 0; u < 2; u++) {
    const int ii = w4 * 2 + u, ch = 8 * ii + (lane >> 3);
    u32x4 v = *(const LAS u32x4*)(stg + ch * 128 + (lane & 7) * 16);
    __builtin_amdgcn_raw_buffer_store_b128(v, rk, (unsigned)((64 * cb + ch) * 4096 + (1984 - 64 * tb) * 2 + (((lane & 7) ^ ii) << 4)), 0, AUX_SC1);
  }
}

struct HkRegs { u32x4 c0[3]; u32x2 c1[3]; };
DEV void hk_load(const Params& p, HkRegs& r, int ch0, int tid4) {
  const GAS u32x4* hk = (const GAS u32x4*)p.hk;
#pragma unroll
  for (int cs = 0; cs < 2; cs++) {
    const int i0 = tid4 - 2, i1 = tid4 - 1;
    r.c0[cs] = hk[(ch0 + cs) * 256 + (i0 < 0 ? 0 : i0)];
    r.c1[cs] = *(const GAS u32x2*)(hk + (ch0 + cs) * 256 + (i1 < 0 ? 0 : i1));
  }
  const int cs = (tid4 >> 1) & 1, i0 = 254 + (tid4 & 1);
  r.c0[2] = hk[(ch0 + cs) * 256 + i0];
  r.c1[2] = *(const GAS u32x2*)(hk + (ch0 + cs) * 256 + 255);
}
DEV void hk_put(LAS char* b, u32x4 c0, u32x2 c1) {
  const unsigned a0 = __builtin_amdgcn_alignbit(c0[1], c0[0], 16), a1 = __builtin_amdgcn_alignbit(c0[2], c0[1], 16), a2 = __builtin_amdgcn_alignbit(c0[3], c0[2], 16),
                 a3 = __builtin_amdgcn_alignbit(c1[0], c0[3], 16), a4 = __builtin_amdgcn_alignbit(c1[1], c1[0], 16);
  *(LAS u32x4*)(b) = (u32x4){a0, a1, a2, a3};
  *(LAS u32x4*)(b + CV_HP) = (u32x4){c0[1], c0[2], c0[3], c1[0]};
  *(LAS u32x4*)(b + 2 * CV_HP) = (u32x4){a1, a2, a3, a4};
  *(LAS u32x4*)(b + 3 * CV_HP) = (u32x4){c0[2], c0[3], c1[0], c1[1]};
}
DEV void hk_write_main(HkRegs& r, LAS char* cv, int tid4, int cs) {
  const u32x4 z4 = {0, 0, 0, 0};
  const u32x2 z2 = {0, 0};
  u32x4 c0 = r.c0[cs]; u32x2 c1 = r.c1[cs];
  if (tid4 < 2) c0 = z4;
  if (tid4 < 1) c1 = z2;
  hk_put(cv + (cs ? CV_HP1 : CV_HP0) + tid4 * 16, c0, c1);
}
DEV void hk_write_edge(HkRegs& r, LAS char* cv, int tid4) {
  const u32x4 z4 = {0, 0, 0, 0};
  const u32x2 z2 = {0, 0};
  if (tid4 < 4) hk_put(cv + (((tid4 >> 1) & 1) ? CV_HP1 : CV_HP0) + (256 + (tid4 & 1)) * 16, r.c0[2], (tid4 & 1) ? z2 : r.c1[2]);
  if (tid4 < 60) { const int cs = tid4 >= 30; *(LAS u32x4*)(cv + CV_UR0 + cs * CV_URS + (256 + tid4 - 30 * cs) * 16) = z4; }
}
DEV void ur_dma(const Params& p, LAS char* cv, int ch0, int w4, int lane) {
  const __amdgpu_buffer_rsrc_t rk = __builtin_amdgcn_make_buffer_rsrc((void*)(p.ws + WS_KVT), 0, D * S * 2, 0x00020000);
  const int cs = w4 >> 1;
#pragma unroll
  for (int u = 0; u < 2; u++) {
    const int pc = (w4 & 1) * 2 + u;
    __builtin_amdgcn_raw_ptr_buffer_load_lds(rk, cv + CV_UR0 + cs * CV_URS + pc * 1024, 16, (unsigned)(lane * 16), (ch0 + cs) * 4096 + pc * 1024, 0, AUX_SC1);
  }
}
DEV void conv_mfma(const Params& p, LAS char* cv, int ch0, int w4, int lane, f32x4 (&acc)[8], unsigned& dbits) {
  const int cs = w4 >> 1, half = w4 & 1;
  const int r = lane & 15, g = lane >> 4;
  dbits = p.Dd[ch0 + cs];
  FENCE();
  {
    LAS char* hp = cv + (cs ? CV_HP1 : CV_HP0) + (r & 3) * CV_HP + ((r & ~3) + 8 * g + half * 128) * 2;
    LAS char* ur = cv + CV_UR0 + cs * CV_URS + (2032 - 16 * r + 8 * g + half * 128) * 2;
#pragma unroll
    for (int i = 0; i < 8; i++) acc[i] = (f32x4){0.f, 0.f, 0.f, 0.f};
#pragma unroll
    for (int ph = 0; ph < 4; ph++) {
      bf16x8 A[8], F[8];
#pragma unroll
      for (int a = 0; a < 8; a++) {
        u32x2 lo = *(const LAS u32x2*)(hp + (256 * a + 32 * ph) * 2), hi = *(const LAS u32x2*)(hp + (256 * a + 32 * ph) * 2 + 8);
        A[a] = __builtin_bit_cast(bf16x8, (u32x4){lo[0], lo[1], hi[0], hi[1]});
        F[a] = *(const LAS bf16x8*)(ur + (32 * ph - 256 * a) * 2);
      }
#pragma unroll
      for (int a = 0; a < 8; a++)
#pragma unroll
        for (int n = 0; n + a < 8; n++) acc[a + n] = __builtin_amdgcn_mfma_f32_16x16x32_bf16(A[a], F[n], acc[a + n], 0, 0, 0);
    }
  }
  asm volatile("" : "+v"(dbits));
  STAMPW(5, 0);
}
DEV void conv_finish(const Params& p, LAS char* cv, LAS char* xch, LAS char* xfl, int ch0, int w4, int lane, f32x4 (&acc)[8], unsigned dbits) {
  const int cs = w4 >> 1, half = w4 & 1;
  const int r = lane & 15, g = lane >> 4;
  LAS char* hp0 = cv + (cs ? CV_HP1 : CV_HP0);
  {
    LAS char* x = xch + w4 * 4096 + lane * 16;
#pragma unroll
    for (int i = 0; i < 4; i++) {
      f32x4 a = acc[i], b = acc[4 + i];
      asm volatile("" : "+v"(a), "+v"(b));
      *(LAS f32x4*)(x + i * 1024) = half ? a : b;
    }
    WAIT_LGKM0();
    if (lane == 0) *(volatile LAS unsigned*)(xfl + w4 * 4) = 1u;
    lds_wait(xfl + (w4 ^ 1) * 4);
  }
  LAS char* x = xch + (w4 ^ 1) * 4096 + lane * 16;
  const float Dv = __uint_as_float(dbits << 16);
  LAS char* zst = hp0 + half * 2048;
  LAS char* ur = cv + CV_UR0 + cs * CV_URS;
#pragma unroll
  for (int i = 0; i < 4; i++) {
    f32x4 ya = acc[i], yb = acc[4 + i];
    asm volatile("" : "+v"(ya), "+v"(yb));
    f32x4 y = (half ? yb : ya) + *(const LAS f32x4*)(x + i * 1024);
    const int t = 256 * (4 * half + i) + 16 * r + 4 * g;
    u32x2 u = *(const LAS u32x2*)(ur + (2044 - t) * 2);
    y[3] += __uint_as_float(u[0] << 16) * Dv; y[2] += __uint_as_float(u[0] & 0xffff0000u) * Dv;
    y[1] += __uint_as_float(u[1] << 16) * Dv; y[0] += __uint_as_float(u[1] & 0xffff0000u) * Dv;
    *(LAS u32x2*)(zst + (256 * i + 16 * r + 4 * g) * 2) = (u32x2){pk_bf16(y[0], y[1]), pk_bf16(y[2], y[3])};
  }
  WAIT_LGKM0();
  const __amdgpu_buffer_rsrc_t rz = __builtin_amdgcn_make_buffer_rsrc((void*)(p.ws + WS_ZT), 0, D * S * 2, 0x00020000);
#pragma unroll
  for (int i = 0; i < 2; i++) {
    u32x4 v = *(const LAS u32x4*)(zst + i * 1024 + lane * 16);
    __builtin_amdgcn_raw_buffer_store_b128(v, rz, (unsigned)((ch0 + cs) * 4096 + half * 2048 + i * 1024 + lane * 16), 0, AUX_SC1);
  }
}

struct AttOff { unsigned k[3], v[3], q; };
DEV void att_off(AttOff& o, int rb, int j, int lane) {
  const int tok2 = 256 * (rb - 2);
#pragma unroll
  for (int t = 0; t < 3; t++) {
    const int lin = 64 * t + lane, row = lin / 48, sl = lin - row * 48;
    const int ck = tok2 + (((((sl >> 2) ^ row) << 2) | (sl & 3)) << 4), cv = tok2 + ((sl ^ row) << 4);
    o.k[t] = ck < 0 ? OOB : (unsigned)((576 + 36 * j + row) * 4096 + ck);
    o.v[t] = cv < 0 ? OOB : (unsigned)((1152 + 36 * j + row) * 4096 + cv);
  }
  const int row = lane >> 4, sl = lane & 15;
  o.q = (unsigned)((36 * j + row) * 4096 + rb * 256 + (((((sl >> 2) ^ row) << 2) | (sl & 3)) << 4));
}
DEV void att_dma(const Params& p, const AttOff& o, int w) {
  const u32x4 rq = mk_rs(p.ws + WS_QKV, 1728 * S * 2);
#pragma unroll
  for (int u = 0; u < 3; u++) {
    const int a = w + 4 * u;
    if (u < 2 || w == 0) {
#pragma unroll
      for (int t = 0; t < 3; t++) dma16c(rq, (unsigned)(L_ATT + A_K + (3 * a + t) * 1024), o.k[t], (unsigned)(a * 16384));
    }
    const int av = u < 2 ? a : 8;
    if (u < 2 || w == 1) {
#pragma unroll
      for (int t = 0; t < 3; t++) dma16c(rq, (unsigned)(L_ATT + A_V + (3 * av + t) * 1024), o.v[t] ^ ((av & 3) << 6), (unsigned)(av * 16384));
    }
  }
  const int q0 = w == 0 ? 0 : (w == 1 ? 1 : (w == 2 ? 2 : 6)), qn = w < 2 ? 1 : (w == 2 ? 4 : 3);
#pragma unroll
  for (int u = 0; u < 4; u++)
    if (u < qn) dma16c(rq, (unsigned)(L_ATT + A_Q + (q0 + u) * 1024), o.q, (unsigned)((q0 + u) * 16384));
}
DEV void att_fetch(const Params& p, LAS char* lds, int arb, int aj, int w4, int lane, u64 tag) {
  const int tid4 = w4 * 64 + lane;

  AttOff ao;
  att_off(ao, arb, aj, lane);
  if (tid4 < 48) { const unsigned one = (8 * tid4 + 128 * (arb - 2) >= 0) ? 0x3f803f80u : 0u; *(LAS u32x4*)(lds + L_ATT + A_ONES + tid4 * 16) = (u32x4){one, one, one, one}; }
  FENCE();
  if (w4 == 0) {
    const int l = lane < 18 ? lane : 0, r = l / 6, kind = (l - 6 * r) >> 1, e = l & 1;
    const int row = arb - 2 + r, col = kind * 576 + 36 * aj + 35 * e;
    const bool act = lane < 18 && row >= 0 && !(kind == 0 && r < 2);
    poll_block<QSL>(p, F_Q + (row < 0 ? 0 : row) * 64 + (col >> 5), act, tag);
    if (lane == 0) *(volatile LAS unsigned*)(lds + L_QRDY) = 1u;
  } else lds_wait(lds + L_QRDY);
  STAMPW(8, 4);
  att_dma(p, ao, w4);
}
struct WoDma { u32x4 rs; unsigned off; int ch; unsigned l0; };
DEV void wo_init(WoDma& d, const Params& p, int ns, int w, int lane) {
  const u64 a = (u64)p.wo;
  d.rs = (u32x4){(unsigned)a, (unsigned)(a >> 32) & 0xffffu, (unsigned)(D * D * 2), 0x00020000u};
  const int lin = 64 * w + lane, row = lin / 10;
  d.ch = lin - row * 10;
  d.off = (unsigned)(row * 1152 + ns * 144 + d.ch * 16);
  d.l0 = (unsigned)(L_WO + w * 1024);
}
DEV void wo_piece(WoDma& d, int u) {
  unsigned keep;
  asm volatile("s_mov_b32 %0, m0\n\ts_mov_b32 m0, %1\n\ts_nop 0\n\tbuffer_load_dwordx4 %2, %3, 0 offen lds\n\ts_mov_b32 m0, %0" : "=&s"(keep) : "s"(d.l0 + u * 8192), "v"(d.off), "s"(d.rs) : "memory");
  d.ch += 2; d.off += 51 * 1152 + 32;
  if (d.ch >= 10) { d.ch -= 10; d.off += 1152 - 160; }
}
DEV bf16x8 cat44(s16x4 lo, s16x4 hi) { return __builtin_bit_cast(bf16x8, __builtin_shufflevector(lo, hi, 0, 1, 2, 3, 4, 5, 6, 7)); }
DEV s16x4 trr(LAS char* a) { return __builtin_amdgcn_ds_read_tr16_b64_v4i16((LAS s16x4*)a); }
#define MFMA32(a, b, c) __builtin_amdgcn_mfma_f32_32x32x16_bf16((a), (b), (c), 0, 0, 0)
#define MFMA32T(a, b, c) __builtin_amdgcn_mfma_f32_32x32x8bf16_1k((a), (b), (c), 0, 0, 0)
DEV float ex2(float x) { return __builtin_amdgcn_exp2f(x); }
DEV float swap_lo(float x) { auto r = __builtin_amdgcn_permlane32_swap(__float_as_uint(x), __float_as_uint(x), false, false); return __uint_as_float(r[0]); }

struct KFrag { s16x4 lo, hi, t; };
struct VFrag { bf16x8 v0, v1; };
struct AttAddr { LAS char *ak[4], *at[4], *av[4], *aw[4], *aq, *aqt; WoDma wd; };
DEV void att_setup(AttAddr& A, const Params& p, LAS char* att, int ns, int wave, int lane) {
  const int hh = wave >> 2, qt = wave & 3;
  const int c = lane & 31, h = lane >> 5;
  const int kh = (lane >> 4) & 1, q4 = (lane >> 2) & 3, p4 = lane & 3, pi = ((p4 & 1) << 1) | (p4 >> 1);
  const int row = 18 * hh + 8 * h + q4, rowt = 18 * hh + 16 + (q4 & 1);
  const int vrow = c < 18 ? 18 * hh + c : ((c >= 20 && c < 28) ? 18 * hh : 36);
  const int vkey = vrow == 36 ? (hh ? 0 : 2) : (vrow & 15);
  A.aq = att + A_Q + row * 256 + ((qt ^ (row & 3)) << 6) + kh * 32 + p4 * 8;
  A.aqt = att + A_Q + rowt * 256 + ((qt ^ (rowt & 3)) << 6) + kh * 32 + p4 * 8;
#pragma unroll
  for (int mm = 0; mm < 4; mm++) {
    const int kt = qt + mm;
    A.ak[mm] = att + A_K + row * 768 + ((kt >> 2) << 8) + (((kt & 3) ^ (row & 3)) << 6) + kh * 32 + pi * 8;
    A.at[mm] = att + A_K + rowt * 768 + ((kt >> 2) << 8) + (((kt & 3) ^ (rowt & 3)) << 6) + kh * 32 + pi * 8;
    A.av[mm] = att + A_V + vrow * 768 + ((kt >> 2) << 8) + (((4 * (kt & 3) + h) ^ vkey) << 4);
    A.aw[mm] = (LAS char*)((unsigned)(size_t)A.av[mm] ^ 32u);
    asm volatile("" : "+v"(A.ak[mm]), "+v"(A.at[mm]), "+v"(A.av[mm]), "+v"(A.aw[mm]));
  }
  wo_init(A.wd, p, ns, wave, lane);
  asm volatile("" : "+v"(A.aq), "+v"(A.aqt), "+v"(A.wd.off), "+v"(A.wd.ch));
}
DEV void att_wave(const Params& p, LAS char* att, AttAddr& A, int rb, int j, int wave, int lane, int zidx, u64 tag) {
  const int hh = wave >> 2, qt = wave & 3;
  const int c = lane & 31, h = lane >> 5;
  bf16x8 q0; s16x4 q1;
  q0 = cat44(trr(A.aq), trr(A.aq + 1024));
  { const u32x2 tt = __builtin_bit_cast(u32x2, trr(A.aqt)); q1 = __builtin_bit_cast(s16x4, (u32x2){h ? 0u : tt[0], 0u}); }
  LAS char **ak = A.ak, **at = A.at, **av = A.av, **aw = A.aw;
  WoDma& wd = A.wd;
  auto ldk = [&](int m) { KFrag k; LAS char* a = ak[m & 3] + (m >> 2) * 256; k.lo = trr(a); k.hi = trr(a + 4 * 768); k.t = trr(at[m & 3] + (m >> 2) * 256); return k; };
  auto ldv = [&](int m) { VFrag v; v.v0 = *(const LAS bf16x8*)(av[m & 3] + (m >> 2) * 256); v.v1 = *(const LAS bf16x8*)(aw[m & 3] + (m >> 2) * 256); return v; };
  f32x16 zero;
#pragma unroll
  for (int i = 0; i < 16; i++) zero[i] = 0.f;
  const int th = c - 8 * h;
  f32x16 o, sa, sb;
  {
    KFrag k0 = ldk(0), k8 = ldk(8), kn = ldk(1);
    VFrag v8 = ldv(8), v0 = ldv(0);
    SCHED_FENCE();
    sa = MFMA32(cat44(k0.lo, k0.hi), q0, zero);
    sb = MFMA32(cat44(k8.lo, k8.hi), q0, zero);
    sa = MFMA32T(k0.t, q1, sa);
    sb = MFMA32T(k8.t, q1, sb);
    f32x16 s1 = MFMA32(cat44(kn.lo, kn.hi), q0, zero);
    s1 = MFMA32T(kn.t, q1, s1);
    SCHED_FENCE();
    u32x4 pf, pd;
#pragma unroll
    for (int g = 0; g < 2; g++) {
#pragma unroll
      for (int k = 0; k < 4; k++) {
        int tv = th - 16 * g - 2 * k;
        asm volatile("" : "+v"(tv));
        const bool c0 = tv < 0, c1 = tv < 1;
        const float a0 = c0 ? sa[8 * g + 2 * k] : sb[8 * g + 2 * k], a1 = c1 ? sa[8 * g + 2 * k + 1] : sb[8 * g + 2 * k + 1];
        const unsigned mk = (c0 ? 0xffffu : 0u) | (c1 ? 0xffff0000u : 0u);
        const unsigned pp = pk_bf16(ex2(a0), ex2(a1));
        pf[k] = pp & mk; pd[k] = pp & ~mk;
        SCHED_FENCE();
      }
      o = MFMA32(g ? v8.v1 : v8.v0, __builtin_bit_cast(bf16x8, pd), g ? o : zero);
      o = MFMA32(g ? v0.v1 : v0.v0, __builtin_bit_cast(bf16x8, pf), o);
    }
    sa = s1;
    SCHED_FENCE();
  }
  KFrag kc = ldk(2);
  VFrag vc = ldv(1);
#pragma unroll
  for (int m = 1; m < 8; m++) {
    KFrag kx; VFrag vx;
    SCHED_FENCE();
    if (m < 7) { wo_piece(wd, 2 * m - 2); wo_piece(wd, 2 * m - 1); }
    if (m < 6) kx = ldk(m + 2);
    if (m < 7) vx = ldv(m + 1);
    SCHED_FENCE();
    if (m < 7) sb = MFMA32(cat44(kc.lo, kc.hi), q0, zero);
    SCHED_FENCE();
    float e[16];
#pragma unroll
    for (int i = 0; i < 8; i++) e[i] = ex2(sa[i]);
    SCHED_FENCE();
    if (m < 7) sb = MFMA32T(kc.t, q1, sb);
    SCHED_FENCE();
    u32x4 p0, p1;
#pragma unroll
    for (int k = 0; k < 4; k++) p0[k] = pk_bf16(e[2 * k], e[2 * k + 1]);
#pragma unroll
    for (int i = 8; i < 16; i++) e[i] = ex2(sa[i]);
    SCHED_FENCE();
    o = MFMA32(vc.v0, __builtin_bit_cast(bf16x8, p0), o);
    SCHED_FENCE();
#pragma unroll
    for (int k = 0; k < 4; k++) p1[k] = pk_bf16(e[8 + 2 * k], e[8 + 2 * k + 1]);
    SCHED_FENCE();
    o = MFMA32(vc.v1, __builtin_bit_cast(bf16x8, p1), o);
    if (m == 4 && zidx >= 0) {
      WAIT_VM(8);
      if (lane == 0) st_flag(flagp(p, zidx), tag);
    }
    sa = sb; kc = kx; vc = vx;
  }
  SCHED_FENCE();
  float l = swap_lo(o[10]);
  {
    float t = __builtin_fabsf(o[0]);
#pragma unroll
    for (int i = 1; i < 10; i++) t += __builtin_fabsf(o[i]);
    const bool good = l >= 0x1p-60f && l <= 0x1p60f && t <= 0x1p126f;
    if (__builtin_amdgcn_ballot_w64(!good) != 0) {
      const int mmin = rb >= 2 ? 0 : (4 * (2 - rb) - qt);
      const int kh = (lane >> 4) & 1, q4 = (lane >> 2) & 3, p4 = lane & 3, pi = ((p4 & 1) << 1) | (p4 >> 1);
      const int row = 18 * hh + 8 * h + q4, rowt = 18 * hh + 16 + (q4 & 1);
      const int vrow = c < 18 ? 18 * hh + c : ((c >= 20 && c < 28) ? 18 * hh : 36);
      const int vkey = vrow == 36 ? (hh ? 0 : 2) : (vrow & 15);
      auto scores = [&](int m) -> f32x16 {
        const int kt = qt + m;
        LAS char* a = att + A_K + row * 768 + ((kt >> 2) << 8) + (((kt & 3) ^ (row & 3)) << 6) + kh * 32 + pi * 8;
        const s16x4 lo = trr(a), hi = trr(a + 4 * 768), tl = trr(att + A_K + rowt * 768 + ((kt >> 2) << 8) + (((kt & 3) ^ (rowt & 3)) << 6) + kh * 32 + pi * 8);
        f32x16 s = MFMA32(cat44(lo, hi), q0, zero);
        s = MFMA32T(tl, q1, s);
        const int lo8 = m == 0 ? th + 1 : -64, hi8 = m == 8 ? th : 64;
#pragma unroll
        for (int i = 0; i < 16; i++) {
          const int idx = 16 * (i >> 3) + (i & 7);
          if (idx < lo8 || idx > hi8) s[i] = -__builtin_inff();
        }
        return s;
      };
      float mx = -__builtin_inff();
#pragma unroll 1
      for (int m = mmin; m <= 8; m++) {
        const f32x16 s = scores(m);
#pragma unroll
        for (int i = 0; i < 16; i++) mx = fmaxf(mx, s[i]);
      }
      { auto r = __builtin_amdgcn_permlane32_swap(__float_as_uint(mx), __float_as_uint(mx), false, false); mx = fmaxf(__uint_as_float(r[0]), __uint_as_float(r[1])); }
      o = zero;
#pragma unroll 1
      for (int m = mmin; m <= 8; m++) {
        const f32x16 s = scores(m);
        u32x4 p0, p1;
#pragma unroll
        for (int k = 0; k < 4; k++) { p0[k] = pk_bf16(ex2(s[2 * k] - mx), ex2(s[2 * k + 1] - mx)); p1[k] = pk_bf16(ex2(s[8 + 2 * k] - mx), ex2(s[8 + 2 * k + 1] - mx)); }
        const int kt = qt + m;
        LAS char* a = att + A_V + vrow * 768 + ((kt >> 2) << 8) + (((4 * (kt & 3) + h) ^ vkey) << 4);
        o = MFMA32(*(const LAS bf16x8*)a, __builtin_bit_cast(bf16x8, p0), o);
        o = MFMA32(*(const LAS bf16x8*)((LAS char*)((unsigned)(size_t)a ^ 32u)), __builtin_bit_cast(bf16x8, p1), o);
      }
      l = swap_lo(o[10]);
    }
  }
  const float inv = __builtin_amdgcn_rcpf(l);
  u32x4 om = {pk_bf16(o[0] * inv, o[1] * inv), pk_bf16(o[2] * inv, o[3] * inv), pk_bf16(o[4] * inv, o[5] * inv), pk_bf16(o[6] * inv, o[7] * inv)};
  const __amdgpu_buffer_rsrc_t ro = __builtin_amdgcn_make_buffer_rsrc((void*)(p.ws + WS_OM), 0, 128 * 16 * 1024, 0x00020000);
  const __amdgpu_buffer_rsrc_t rt = __builtin_amdgcn_make_buffer_rsrc((void*)(p.ws + WS_OT), 0, 32 * S * 4, 0x00020000);
  const int qt16 = 8 * rb + 2 * qt + (c >> 4);
  __builtin_amdgcn_raw_buffer_store_b128(om, ro, (unsigned)(((qt16 * 16 + j) * 64 + (c & 15) + 16 * (2 * hh + h)) * 16), 0, AUX_SC1);
  __builtin_amdgcn_raw_buffer_store_b32(pk_bf16(o[8] * inv, o[9] * inv), rt, h ? OOB : (unsigned)(((2 * j + hh) * S + 128 * rb + 32 * qt + c) * 4), 0, AUX_SC1);
}

DEV void p3_wave(const Params& p, LAS char* wl, int qb, int ns, int w, int lane, u64 tag) {
  const int r = lane & 15, g = lane >> 4, q4 = (lane >> 2) & 3, p4 = lane & 3;
  const int qt16 = 4 * qb + w;
  poll_block(p, F_O + (qt16 >> 1) * 32 + (lane & 31), true, tag);
  STAMPW(11, 0);
  const __amdgpu_buffer_rsrc_t ro = __builtin_amdgcn_make_buffer_rsrc((void*)(p.ws + WS_OM), 0, 128 * 16 * 1024, 0x00020000);
  const __amdgpu_buffer_rsrc_t rt = __builtin_amdgcn_make_buffer_rsrc((void*)(p.ws + WS_OT), 0, 32 * S * 4, 0x00020000);
  u32x4 of[18];
#pragma unroll
  for (int s = 0; s < 16; s++) of[s] = __builtin_amdgcn_raw_buffer_load_b128(ro, (unsigned)(lane * 16), (unsigned)((qt16 * 16 + s) * 1024), AUX_SC1);
#pragma unroll
  for (int s = 0; s < 2; s++)
#pragma unroll
    for (int w4 = 0; w4 < 4; w4++) of[16 + s][w4] = __builtin_amdgcn_raw_buffer_load_b32(rt, (unsigned)(((4 * g + w4) * S + 16 * qt16 + r) * 4), (unsigned)(16 * s * S * 4), AUX_SC1);
  FENCE();
  f32x4 acc[5];
#pragma unroll
  for (int i = 0; i < 5; i++) acc[i] = (f32x4){0.f, 0.f, 0.f, 0.f};
  LAS char* am = wl + (18 * (g >> 1) + 4 * (g & 1) + q4) * WO_RS + p4 * 8;
  LAS char* atl = wl + (18 * (4 * g + (q4 >> 1)) + 16 + (q4 & 1)) * WO_RS + p4 * 8;
#pragma unroll
  for (int s = 0; s < 18; s++) {
#pragma unroll
    for (int nt = 0; nt < 5; nt++) {
      LAS char* a = (s < 16 ? am + 36 * s * WO_RS : atl + 18 * 16 * (s - 16) * WO_RS) + nt * 32;
      const int tstep = s < 16 ? 8 * WO_RS : 36 * WO_RS;
      bf16x8 A = cat44(trr(a), trr(a + tstep));
      acc[nt] = __builtin_amdgcn_mfma_f32_16x16x32_bf16(A, __builtin_bit_cast(bf16x8, of[s]), acc[nt], 0, 0, 0);
    }
  }
  STAMPW(12, 0);
  GAS u32x2* out = (GAS u32x2*)(p.out + (size_t)(16 * qt16 + r) * 1152 + ns * 72 + 4 * g);
#pragma unroll
  for (int nt = 0; nt < 5; nt++)
    if (nt < 4 || g < 2) out[nt * 4] = (u32x2){pk_bf16(acc[nt][0], acc[nt][1]), pk_bf16(acc[nt][2], acc[nt][3])};
}
template <int NT>
DEV void b3_wave(const Params& p, LAS char* zl, int tb, int cb, int w4, int lane, u64 tag) {
  const int ch0 = 64 * cb + 16 * w4, c = lane & 1, r32 = lane >> 1;
  const GAS u32x4* qp = (const GAS u32x4*)p.q;
  poll_block(p, F_Z + (tb >> 4) * D + ch0 + (lane & 15) + (NT == 2 ? 64 * ((lane >> 4) & 1) : 0), true, tag);
  const __amdgpu_buffer_rsrc_t rz = __builtin_amdgcn_make_buffer_rsrc((void*)(p.ws + WS_ZT), 0, D * S * 2, 0x00020000);
#pragma unroll
  for (int n = 0; n < NT; n++)
#pragma unroll
    for (int u = 0; u < 2; u++)
      __builtin_amdgcn_raw_ptr_buffer_load_lds(rz, zl + n * 8192 + u * 1024, 16, (unsigned)((ch0 + 64 * n + 8 * u + (lane >> 3)) * 4096 + tb * 128 + (((lane & 7) ^ (4 * u)) << 4)), 0, 0, AUX_SC1);
  u32x4 qv[NT][2];
#pragma unroll
  for (int n = 0; n < NT; n++)
#pragma unroll
    for (int u = 0; u < 2; u++) qv[n][u] = qp[(64 * tb + 32 * u + r32) * 72 + ((ch0 + 64 * n) >> 3) + c];
  WAIT_VM(0);
#pragma unroll
  for (int n = 0; n < NT; n++)
#pragma unroll
    for (int u = 0; u < 2; u++) {
      const int tl = 32 * u + r32;
      LAS char* zr = zl + n * 8192 + c * 1024 + (((tl >> 3) ^ (4 * c)) << 4) + (tl & 7) * 2;
      u32x4 o;
#pragma unroll
      for (int e = 0; e < 4; e++) {
        const unsigned z0 = *(const LAS u16*)(zr + (2 * e) * 128), z1 = *(const LAS u16*)(zr + (2 * e + 1) * 128);
        const f32x2 qq = unpk(qv[n][u][e]);
        o[e] = pk_bf16(qq.x * __uint_as_float(z0 << 16), qq.y * __uint_as_float(z1 << 16));
      }
      *(GAS u32x4*)(p.out + (size_t)(64 * tb + tl) * 1152 + 576 + ch0 + 64 * n + 8 * c) = o;
    }
}

__global__ void __launch_bounds__(512, 2) fused(Params p) {
  extern __shared__ __attribute__((aligned(16))) char smem[];
  LAS char* lds = (LAS char*)smem;
  const int wave = __builtin_amdgcn_readfirstlane(threadIdx.x >> 6), lane = threadIdx.x & 63;
  const int b = blockIdx.x, xcd = b & 7, loc = b >> 3;
  const bool heavy = loc < 28;
  const int w4 = wave & 3, tid4 = w4 * 64 + lane;
  const bool isH = wave >= 4;
  STAMP_INIT();
  STAMPW(0, 0);
  u64 tag;
  const int arb = heavy ? 2 + (loc >> 1) : ((loc - 28) >> 1), aj = 2 * xcd + (loc & 1);
  const int li = xcd * 4 + (loc - 28);
  const int hi = xcd * 28 + loc;
  const int ttb = heavy ? hi / 7 : li, tcb = heavy ? hi - 7 * (hi / 7) : 7;
  const int cch = heavy ? 2 * hi : 448 + 4 * li;
  const int kvidx = F_KV + (cch >> 4) * 32 + (lane & 31);
  int zidx = -1;
  LAS char* att = lds + L_ATT;

  if (heavy) {
    const int rb = (xcd >> 1) * 4 + (loc & 3), c = (xcd & 1) * 7 + (loc >> 2);
    if (isH) {
      const int hw = w4;
      const u32x4 rx = mk_rs(p.x + (size_t)rb * 128 * D, 128 * D * 2), rw = mk_rs(p.wqkv, D * 3 * D * 2);
      const unsigned vx = (lane >> 3) * 1152 + (((lane & 7) ^ (lane >> 3)) << 4);
      const int cm = (c == 13) ? 7 : 15;
      const unsigned vw0 = (lane >> 4) * 3456 + c * 256 + ((((lane & 15) ^ ((lane >> 4) << 1)) & cm) << 4);
      const unsigned vw1 = (lane >> 4) * 3456 + c * 256 + ((((lane & 15) ^ (((lane >> 4) | 4) << 1)) & cm) << 4);
      const int s0 = ROT ? (2 * (loc & 3) + 4 * (loc >> 2)) % 9 : 0;
      u64 gen = 0;
      if (hw == 0) asm volatile("global_load_dwordx2 %0, %1, off sc1" : "=v"(gen) : "v"(flagp(p, F_GEN)) : "memory");
      FENCE();
#pragma unroll
      for (int s = 0; s < 3; s++) p1_dma_stage(rx, rw, s, s0, hw, vx, vw0, vw1);
      FENCE();
      RawOff ro;
      b1_raw_off(ro, ttb, tcb, hw, lane);
      FENCE();
      if (hw == 0) {
        asm volatile("s_waitcnt vmcnt(24)" : "+v"(gen) :: "memory");
        tag = mk_tag(((u64)(unsigned)__builtin_amdgcn_readfirstlane((unsigned)(gen >> 32)) << 32) | (unsigned)__builtin_amdgcn_readfirstlane((unsigned)gen));
        if (lane < 20) *(LAS unsigned*)(lds + L_MISC + lane * 4) = lane == 10 ? (unsigned)tag : (lane == 11 ? (unsigned)(tag >> 32) : 0u);
      }
      WAIT_LGKM0();
      WAIT_VM(16); barrier_raw();
      p1_dma_stage(rx, rw, 3, s0, hw, vx, vw0, vw1); FENCE();
      tag = lds_tag(lds); FENCE();
      if (hw == 0 && lane == 0) st_flag(flagp(p, F_SEEN + b), tag);
      FENCE();
      WAIT_VM(16); barrier_raw();
      p1_dma_stage(rx, rw, 4, s0, hw, vx, vw0, vw1); FENCE();
      WAIT_VM(16); barrier_raw();
      p1_dma_stage(rx, rw, 5, s0, hw, vx, vw0, vw1); FENCE();
      if (RAWIT == 2) { b1_raw_dma(p, ro, hw); FENCE(); }
      STAMP3(0);
      if (RAWIT == 2) WAIT_VM(20); else WAIT_VM(16);
      STAMP3(1);
      barrier_raw();
      p1_dma_stage(rx, rw, 6, s0, hw, vx, vw0, vw1); FENCE();
      if (RAWIT == 3) { b1_raw_dma(p, ro, hw); FENCE(); }
      WAIT_VM(20); barrier_raw();
      p1_dma_stage(rx, rw, 7, s0, hw, vx, vw0, vw1); FENCE();
      if (RAWIT == 2) WAIT_VM(16); else WAIT_VM(20);
      barrier_raw();
      p1_dma_stage(rx, rw, 8, s0, hw, vx, vw0, vw1); FENCE();
      HkRegs hr;
      hk_load(p, hr, cch, tid4);
      FENCE();
      if (hw < 3) dma4(mk_rs(p.wo, D * D * 2), L_DUMMY, PFWO ? (unsigned)(((loc * 3 + hw) * 64 + lane) * 128) : OOB);
      else dma4(mk_rs(p.q, S * D * 2), L_DUMMY, PFWO ? (unsigned)((64 * ttb + lane) * 1152 + tcb * 128) : OOB);
      FENCE();
      B1Regs br;
      if (RAWIT == 2) { b1_read_raw(br, lds, tid4); b1_pin(br); FENCE(); }
      STAMP3(2);
      WAIT_VM(23);
      barrier_raw();
      if (RAWIT != 2) b1_read_raw(br, lds, tid4);
      b1_compute<0, 2>(br, lds + L_B1, tid4);
      FENCE();
      STAMP3(3);
      WAIT_VM(15); barrier_raw();
      b1_compute<2, 4>(br, lds + L_B1, tid4);
      WAIT_LGKM0();
      WAIT_VM(7); barrier_raw();
      b1_store(p, lds + L_B1, ttb, tcb, hw, lane); FENCE();
      barrier_raw();
      barrier_raw();
      const __amdgpu_buffer_rsrc_t rq = __builtin_amdgcn_make_buffer_rsrc((void*)(p.ws + WS_QKV), 0, 1728 * S * 2, 0x00020000);
      const bool cvalid = c * 128 + hw * 32 < 3 * D;
#pragma unroll
      for (int u = 0; u < 8; u++) {
        const int n = hw * 32 + u * 4 + (lane >> 4);
        u32x4 v = *(const LAS u32x4*)(lds + L_CIMG + n * 256 + (((lane & 15) ^ (n & 15)) << 4));
        __builtin_amdgcn_raw_buffer_store_b128(v, rq, cvalid ? (unsigned)((c * 128 + n) * (S * 2) + rb * 256 + (lane & 15) * 16) : 0x7ffffff0u, 0, AUX_SC1);
      }
      FENCE();
      hk_write_main(hr, lds + L_CONV, tid4, 0);
      hk_write_main(hr, lds + L_CONV, tid4, 1);
      hk_write_edge(hr, lds + L_CONV, tid4);
      WAIT_LGKM0();
      if (lane == 0) *(volatile LAS unsigned*)(lds + L_RDY + 16 + hw * 4) = 1u;
      WAIT_VM(8);
      if (lane == 0) st_flag(flagp(p, F_KV + (tcb * 4 + hw) * 32 + ttb), tag);
      WAIT_VM(1);
      if (cvalid && lane == 0) st_flag(flagp(p, F_Q + rb * 64 + 4 * c + hw), tag);
      STAMPW(7, 4);
      att_fetch(p, lds, arb, aj, hw, lane, tag);
      WAIT_VM(0);
    } else {
      const int wm = wave & 1, wn = wave >> 1, g = lane >> 4;

      f32x4 acc[4][4];
#pragma unroll
      for (int i = 0; i < 4; i++)
#pragma unroll
        for (int j = 0; j < 4; j++) acc[i][j] = (f32x4){0.f, 0.f, 0.f, 0.f};
      const unsigned xa0 = (64 * wm + (lane & 15)) * 128 + ((g ^ (lane & 7)) << 4);
      const int q = (lane >> 2) & 3, pp = lane & 3, f = q | ((g & 1) << 2);
      unsigned wb[4];
#pragma unroll
      for (int nt = 0; nt < 4; nt++) wb[nt] = 16384 + (8 * g + q) * 256 + (((4 * wn + nt) ^ f) << 5) + pp * 8;
      bf16x8 a[2][4], bq[2][4];
      auto load_frags = [&](int buf, int s, int ks) {
        LAS char* base = lds + ring_slot(s);
#pragma unroll
        for (int mt = 0; mt < 4; mt++) a[buf][mt] = *(const LAS bf16x8*)(base + ((xa0 ^ (ks << 6)) + mt * 2048));
#pragma unroll
        for (int nt = 0; nt < 4; nt++) {
          s16x4 lo = trr(base + wb[nt] + ks * 8192), hi = trr(base + wb[nt] + ks * 8192 + 1024);
          bq[buf][nt] = cat44(lo, hi);
        }
      };
      auto mm = [&](int buf) {
#pragma unroll
        for (int mt = 0; mt < 4; mt++)
#pragma unroll
          for (int nt = 0; nt < 4; nt++) acc[mt][nt] = __builtin_amdgcn_mfma_f32_16x16x32_bf16(a[buf][mt], bq[buf][nt], acc[mt][nt], 0, 0, 0);
      };
      WAIT_LGKM0();
      barrier_raw();
      STAMPW(1, 0);
      tag = lds_tag(lds);
      load_frags(0, 0, 0);
#pragma unroll
      for (int s = 0; s < 9; s++) {
        SCHED_FENCE();
        load_frags(1, s, 1);
        mm(0);
        INTERLEAVE12();
        SCHED_FENCE();
        WAIT_LGKM0();
        if (s < 8) {
          STAMP2(s, 0);
          barrier_raw();
          SCHED_FENCE(); load_frags(0, s + 1, 0); mm(1); INTERLEAVE12();
        } else mm(1);
      }
      SCHED_FENCE();
      STAMPW(2, 0);
      barrier_raw();
      FENCE();
      const float qs = 0.23570226039551584f * 1.4426950408889634f;
#pragma unroll
      for (int nt = 0; nt < 4; nt++) {
        const int n = 64 * wn + 16 * nt + (lane & 15);
        const float sc = (c * 128 + n < D) ? qs : 1.0f;
#pragma unroll
        for (int mt = 0; mt < 4; mt++) {
          const int t8 = 8 * wm + 2 * mt + (g >> 1);
          u32x2 v = {pk_bf16(acc[mt][nt][0] * sc, acc[mt][nt][1] * sc), pk_bf16(acc[mt][nt][2] * sc, acc[mt][nt][3] * sc)};
          *(LAS u32x2*)(lds + L_CIMG + n * 256 + ((t8 ^ (lane & 15)) << 4) + (g & 1) * 8) = v;
        }
      }
      WAIT_LGKM0();
      barrier_raw();
      STAMPW(3, 0);
      if (w4 == 0) {
        if (KVPRE) __builtin_amdgcn_s_sleep(KVPRE);
        poll_block<KVSL>(p, kvidx, true, tag);
        if (lane == 0) *(volatile LAS unsigned*)(lds + L_KVRDY) = 1u;
      } else lds_wait(lds + L_KVRDY);
      ur_dma(p, lds + L_CONV, cch, w4, lane);
      STAMPW(15, 0);
      __builtin_amdgcn_s_waitcnt(0x0F70);
      asm volatile("" ::: "memory");
      if (lane == 0) *(volatile LAS unsigned*)(lds + L_RDY + w4 * 4) = 1u;
      lds_wait_rdy(lds + L_RDY, w4 ^ 1);
      STAMPW(4, 0);
      {
        f32x4 ca[8]; unsigned dbits;
        conv_mfma(p, lds + L_CONV, cch, w4, lane, ca, dbits);
        conv_finish(p, lds + L_CONV, lds + L_XCH, lds + L_XF0, cch, w4, lane, ca, dbits);
      }
      STAMPW(6, 0);
      zidx = F_Z + (w4 & 1) * D + cch + (w4 >> 1);
    }
  } else {
    const int grp = wave >> 2;
    LAS char* cv = lds + (grp ? L_CONV2 : L_CONV);
    LAS char* xc = lds + L_ATT + grp * 16384;
    const int ch = cch + 2 * grp;
    u64 gen = 0;
    if (wave == 0) gen = ld_flag(flagp(p, F_GEN));
    FENCE();
    B1Regs br; HkRegs hr;
    b1_load(p, br, ttb, tcb + grp, tid4);
    hk_load(p, hr, ch, tid4);
    FENCE();
    unsigned pf[5] = {0u, 0u, 0u, 0u, 0u};
    if (PFKV && grp == 1) {
      const __amdgpu_buffer_rsrc_t bk = __builtin_amdgcn_make_buffer_rsrc((void*)p.k, 0, S * D * 2, 0x00020000);
      const __amdgpu_buffer_rsrc_t bv = __builtin_amdgcn_make_buffer_rsrc((void*)p.v, 0, S * D * 2, 0x00020000);
      const int r7 = lane / 7, c7 = lane - 7 * r7, ptb = 4 * xcd + (loc - 28);
#pragma unroll
      for (int i = 0; i < 2; i++) {
        const int rk = 64 * ptb - 8 + 9 * (2 * w4 + i) + r7, rv = 64 * ptb + 8 * (2 * w4 + i) + r7;
        pf[i] = __builtin_amdgcn_raw_buffer_load_b32(bk, (lane < 63 && rk >= 0) ? (unsigned)(rk * 1152 + c7 * 128) : OOB, 0, 0);
        pf[2 + i] = __builtin_amdgcn_raw_buffer_load_b32(bv, lane < 56 ? (unsigned)(rv * 1152 + c7 * 128) : OOB, 0, 0);
      }
      if (w4 < 2) {
        const __amdgpu_buffer_rsrc_t bs = __builtin_amdgcn_make_buffer_rsrc((void*)(w4 ? p.Dd : p.sk), 0, w4 ? D * 2 : D * 4 * 2, 0x00020000);
        pf[4] = __builtin_amdgcn_raw_buffer_load_b32(bs, (unsigned)(lane * 128), 0, 0);
      }
    }
    FENCE();
    b1_zero_halo(br, ttb, tid4);
    b1_compute(br, xc, tid4);
    if (wave == 0) {
      asm volatile("" : "+v"(gen));
      tag = mk_tag(((u64)(unsigned)__builtin_amdgcn_readfirstlane((unsigned)(gen >> 32)) << 32) | (unsigned)__builtin_amdgcn_readfirstlane((unsigned)gen));
      if (lane < 20) *(LAS unsigned*)(lds + L_MISC + lane * 4) = lane == 10 ? (unsigned)tag : (lane == 11 ? (unsigned)(tag >> 32) : 0u);
    }
    WAIT_LGKM0(); barrier_raw();
    tag = lds_tag(lds);
    if (grp == 0) {
      b1_store(p, lds + L_ATT, ttb, tcb, w4, lane);
      b1_store(p, lds + L_ATT + 16384, ttb, tcb + 1, w4, lane);
      WAIT_VM(0);
      if (lane < 2) st_flag(flagp(p, F_KV + ((tcb + lane) * 4 + w4) * 32 + ttb), tag);
      if (wave == 1 && lane == 0) st_flag(flagp(p, F_SEEN + b), tag);
    }
    FENCE();
    hk_write_main(hr, cv, tid4, 0);
    hk_write_main(hr, cv, tid4, 1);
    hk_write_edge(hr, cv, tid4);
    WAIT_LGKM0();
    if (wave == 0) poll_block<4>(p, kvidx, true, tag);
    barrier_raw();
    ur_dma(p, cv, ch, w4, lane);
    __builtin_amdgcn_s_waitcnt(0x0F70);
    asm volatile("" :: "v"(pf[0]), "v"(pf[1]), "v"(pf[2]), "v"(pf[3]), "v"(pf[4]) : "memory");
    barrier_raw();
    STAMPW(4, 0);
    if (PFWO && (wave == 4 || wave == 5)) dma4(mk_rs(p.q, S * D * 2), L_DUMMY, (unsigned)((64 * ttb + lane) * 1152 + (tcb + wave - 4) * 128));
    {
      f32x4 ca[8]; unsigned dbits;
      conv_mfma(p, cv, ch, w4, lane, ca, dbits);
      conv_finish(p, cv, xc, lds + L_XF0 + 16 * grp, ch, w4, lane, ca, dbits);
    }
    STAMPW(6, 0);
    WAIT_VM(0);
    if (lane == 0) st_flag(flagp(p, F_Z + (w4 & 1) * D + ch + (w4 >> 1)), tag);
    WAIT_LGKM0(); barrier_raw();
    if (b == 255 && wave == 3) {
#pragma unroll 1
      for (int i = 0; i < 4; i++) poll_block<8>(p, F_SEEN + 64 * i + lane, true, tag);
      if (lane == 0) st_flag(flagp(p, F_GEN), tag);
    }
    if (isH) { att_fetch(p, lds, arb, aj, w4, lane, tag); WAIT_VM(0); }
  }
  const int qb = 4 * xcd + (loc & 3), ns = loc >> 2;
  AttAddr aa;
  att_setup(aa, p, att, ns, wave, lane);
  WAIT_LGKM0();
  barrier_raw();
  STAMPW(9, 0);
  STAMP_FLUSH();
  att_wave(p, att, aa, arb, aj, wave, lane, zidx, tag);
  FENCE();
  STAMPW(10, 0);
  WAIT_VM(0);
  if (lane == 0) st_flag(flagp(p, F_O + (arb * 4 + w4) * 32 + 2 * aj + (wave >> 2)), tag);
  WAIT_LGKM0();
  barrier_raw();
  if (isH) {
    if (heavy) b3_wave<1>(p, lds + L_Z + w4 * 2048, ttb, tcb, w4, lane, tag);
    else b3_wave<2>(p, lds + L_Z + w4 * 2048, ttb, tcb, w4, lane, tag);
    STAMPW(14, 4);
  } else {
    p3_wave(p, lds + L_WO, qb, ns, w4, lane, tag);
    STAMPW(13, 0);
  }
}

extern "C" void kernel_launch(void* const* d_in, const int* in_sizes, int n_in, void* d_out, int out_size, void* d_ws, size_t ws_size, hipStream_t stream) {
  Params p;
  p.x = (const u16*)d_in[0]; p.wqkv = (const u16*)d_in[1]; p.wo = (const u16*)d_in[2]; p.q = (const u16*)d_in[3]; p.k = (const u16*)d_in[4]; p.v = (const u16*)d_in[5];
  p.sk = (const u16*)d_in[6]; p.hk = (const u16*)d_in[7]; p.Dd = (const u16*)d_in[8]; p.out = (u16*)d_out; p.ws = (char*)d_ws;
  constexpr int LDS_BYTES = 160 * 1024;
  (void)hipFuncSetAttribute((const void*)fused, hipFuncAttributeMaxDynamicSharedMemorySize, LDS_BYTES);
  fused<<<256, 512, LDS_BYTES, stream>>>(p);
}
```

```cpp
#define RSL 2
#define HK 5
#define PFS 2
#include <hip/hip_runtime.h>
typedef unsigned short u16;
typedef unsigned long long u64;
typedef __bf16 bf16x8 __attribute__((ext_vector_type(8)));
typedef __bf16 bf16x2 __attribute__((ext_vector_type(2)));
typedef short s16x4 __attribute__((ext_vector_type(4)));
typedef float f32x2 __attribute__((ext_vector_type(2)));
typedef float f32x4 __attribute__((ext_vector_type(4)));
typedef float f32x16 __attribute__((ext_vector_type(16)));
typedef unsigned u32x2 __attribute__((ext_vector_type(2)));
typedef unsigned u32x4 __attribute__((ext_vector_type(4)));
#define GAS __attribute__((address_space(1)))
#define LAS __attribute__((address_space(3)))
#define DEV __device__ __forceinline__

constexpr int S = 2048, D = 576;
constexpr size_t WS_QKV = 0;
constexpr size_t WS_KVT = 8u << 20;
constexpr size_t WS_ZT = 11u << 20;
constexpr size_t WS_OM = 14u << 20;
constexpr size_t WS_OT = 17u << 20;
constexpr size_t WS_FLAG = 18u << 20;
constexpr int F_GEN = 0, F_SEENC = 64, F_SEEN = 128, F_Q = 1280, F_Z = 2560, F_KV = 4096;

DEV unsigned pk_bf16(float a, float b) { f32x2 v = {a, b}; bf16x2 r = __builtin_convertvector(v, bf16x2); return __builtin_bit_cast(unsigned, r); }
DEV f32x2 unpk(unsigned w) { f32x2 r; r.x = __uint_as_float(w << 16); r.y = __uint_as_float(w & 0xffff0000u); return r; }
DEV void barrier_raw() { __builtin_amdgcn_s_barrier(); }
#define WAIT_VM(n) asm volatile("s_waitcnt vmcnt(" #n ")" ::: "memory")
#define WAIT_VMN(n) asm volatile("s_waitcnt vmcnt(%0)" :: "n"(n) : "memory")
#define WAIT_LGKM0() asm volatile("s_waitcnt lgkmcnt(0)" ::: "memory")
#define SCHED_FENCE() __builtin_amdgcn_sched_barrier(0)
#define FENCE() do { asm volatile("" ::: "memory"); __builtin_amdgcn_sched_barrier(0); } while (0)
#define INTERLEAVE12() do { _Pragma("unroll") for (int i_ = 0; i_ < 12; i_++) { __builtin_amdgcn_sched_group_barrier(0x008, 1, 0); __builtin_amdgcn_sched_group_barrier(0x100, 1, 0); } __builtin_amdgcn_sched_group_barrier(0x008, 4, 0); } while (0)
constexpr int AUX_SC1 = 16;
constexpr int SPIN_MAX = 20000;
#define PRO 2
#define ROT 2
#define QSL 8
#define AX 0
#define OUTB 0
#define OUTA 0
#define PPQ 2
#define PPO 0
#define PFWO 1
#define FS 1
#define PRI 0
#define ZC 6
#define ZM 4
#define PD 0
#define PB 0
#define BS 6
#define SPL 6
#define RAWL 0
#define S03 1
#define PF8 1
#define RW (SPL + 2)
#define RAWB 0
#define KF (SPL + 2)
#define MSL 16
#define RS0 1
#define MKP 1
#define KL 6
#define HW0 1
#define RI 0
#define MK 7
#define KVSL 8
#define GENW 1
#define PQ0 3
#define PQ1 PQ0
#define PQ2 PQ1
#define PQ3 PQ2
#define PE 0
#define PR 0

struct Params { const u16 *x, *wqkv, *wo, *q, *k, *v, *sk, *hk, *Dd; u16* out; char* ws; };

constexpr int STG = 32768;
constexpr int CV_HP = 4160, CV_HP0 = 0, CV_UR0 = 16640, CV_URS = 4576, CV_HP1 = 28608;
constexpr int L_CONV = 0;
constexpr int L_MISC = 25856;
constexpr int L_XF0 = L_MISC, L_QRDY = L_MISC + 32, L_KVRDY = L_MISC + 36, L_KVRD2 = L_MISC + 48, L_TAG = L_MISC + 40, L_HRDY = L_MISC + 48, L_RAWRDY = L_MISC + 64;
constexpr int L_XCH = 45312;
constexpr int L_DUMMY16 = 28672;
constexpr int L_DUMMY = 26624;
constexpr int L_RK = 0, L_RV = 9216, L_B1 = 17408, L_RS = 26880;
constexpr int L_RAW2 = 45312;
constexpr int L_CIMG = 65536;
constexpr int L_CONV2 = 45312;
constexpr int L_ATT = 98304;
constexpr int A_K = 0, A_V = 18 * 1024, A_ONES = 36 * 1024, A_Q = 37 * 1024;
constexpr int WO_RS = 160, L_WO = 0;
constexpr int L_Z = L_ATT;
DEV constexpr int ring_slot(int s) { return (s & 3) == 0 ? 98304 : ((s & 3) == 1 ? 32768 : ((s & 3) == 2 ? 65536 : 131072)); }

DEV GAS u64* flagp(const Params& p, int idx) { return (GAS u64*)(p.ws + WS_FLAG) + idx * 16; }
DEV u64 ld_flag(GAS u64* a) { return __hip_atomic_load(a, __ATOMIC_RELAXED, __HIP_MEMORY_SCOPE_AGENT); }
DEV void st_flag(GAS u64* a, u64 v) { __hip_atomic_store(a, v, __ATOMIC_RELAXED, __HIP_MEMORY_SCOPE_AGENT); }
DEV u64 mk_tag(u64 gen) { return gen * 6364136223846793005ull + 1442695040888963407ull; }
template <int SLEEP = 4>
DEV void poll_block(const Params& p, int idx, bool active, u64 tag) {
  GAS u64* a = flagp(p, idx);
  u64 v = active ? ~tag : tag;
#pragma unroll 1
  for (int it = 0; it < SPIN_MAX; it++) {
    if (v != tag) v = ld_flag(a);
    if (__builtin_amdgcn_ballot_w64(v != tag) == 0) break;
    __builtin_amdgcn_s_sleep(SLEEP);
  }
  asm volatile("" ::: "memory");
}
struct PollRegs { u64 r0, r1, r2; };
template <int SLEEP>
DEV void poll_pipe(PollRegs& r, const Params& p, int idx, u64 tag) {
  GAS u64* a = flagp(p, idx);
  int n;
  asm volatile(
    "global_load_dwordx2 %0, %4, off sc1\n\t"
    "s_movk_i32 %3, 0x2000\n\t"
    "s_waitcnt vmcnt(0)\n\tv_cmp_ne_u64 vcc, %5, %0\n\tv_mov_b64 %1, %0\n\tv_mov_b64 %2, %0\n\ts_cbranch_vccz 2f\n\t"
    "s_mov_b64 exec, vcc\n\tglobal_load_dwordx2 %0, %4, off sc1\n\ts_sleep %6\n\tglobal_load_dwordx2 %1, %4, off sc1\n\ts_sleep %6\n\tglobal_load_dwordx2 %2, %4, off sc1\n\ts_mov_b64 exec, -1\n"
    "1:\n\t"
    "s_waitcnt vmcnt(2)\n\tv_cmp_ne_u64 vcc, %5, %0\n\ts_cbranch_vccz 2f\n\ts_sleep %6\n\ts_mov_b64 exec, vcc\n\tglobal_load_dwordx2 %0, %4, off sc1\n\ts_mov_b64 exec, -1\n\t"
    "s_waitcnt vmcnt(2)\n\tv_cmp_ne_u64 vcc, %5, %1\n\ts_cbranch_vccz 2f\n\ts_sleep %6\n\ts_mov_b64 exec, vcc\n\tglobal_load_dwordx2 %1, %4, off sc1\n\ts_mov_b64 exec, -1\n\t"
    "s_waitcnt vmcnt(2)\n\tv_cmp_ne_u64 vcc, %5, %2\n\ts_cbranch_vccz 2f\n\ts_sleep %6\n\ts_mov_b64 exec, vcc\n\tglobal_load_dwordx2 %2, %4, off sc1\n\ts_mov_b64 exec, -1\n\t"
    "s_sub_u32 %3, %3, 1\n\ts_cmp_lg_u32 %3, 0\n\ts_cbranch_scc1 1b\n"
    "2:"
    : "=&v"(r.r0), "=&v"(r.r1), "=&v"(r.r2), "=&s"(n) : "v"(a), "s"(tag), "n"(SLEEP) : "vcc", "scc", "memory");
}
DEV void poll_pin(PollRegs& r) { asm volatile("" :: "v"(r.r0), "v"(r.r1), "v"(r.r2) : "memory"); }
DEV u64 lds_tag(LAS char* lds) {
  const u32x2 t = *(const LAS u32x2*)(lds + L_TAG);
  return ((u64)(unsigned)__builtin_amdgcn_readfirstlane(t[1]) << 32) | (unsigned)__builtin_amdgcn_readfirstlane(t[0]);
}
DEV void lds_wait(LAS char* w) {
#pragma unroll 1
  for (int it = 0; it < (1 << 24); it++) {
    if (*(volatile LAS unsigned*)w == 1u) break;
    __builtin_amdgcn_s_sleep(1);
  }
  asm volatile("" ::: "memory");
}

DEV unsigned lds_wait_any(LAS char* w) {
  unsigned v = 0;
#pragma unroll 1
  for (int it = 0; it < (1 << 24); it++) {
    v = *(volatile LAS unsigned*)w;
    if (v != 0u) break;
    __builtin_amdgcn_s_sleep(1);
  }
  asm volatile("" ::: "memory");
  return (unsigned)__builtin_amdgcn_readfirstlane(v);
}

DEV u32x4 mk_rs(const void* base, unsigned bytes) { const u64 a = (u64)base; return (u32x4){(unsigned)a, (unsigned)(a >> 32) & 0xffffu, bytes, 0x00020000u}; }
DEV void dma16(u32x4 rs, unsigned ldsa, unsigned voff, unsigned soff) {
  unsigned keep;
  asm volatile("s_mov_b32 %0, m0\n\ts_mov_b32 m0, %1\n\ts_nop 0\n\tbuffer_load_dwordx4 %2, %3, %4 offen lds\n\ts_mov_b32 m0, %0" : "=&s"(keep) : "s"(ldsa), "v"(voff), "s"(rs), "s"(soff) : "memory");
}
DEV void dma16c(u32x4 rs, unsigned ldsa, unsigned voff, unsigned soff) {
  unsigned keep;
  asm volatile("s_mov_b32 %0, m0\n\ts_mov_b32 m0, %1\n\ts_nop 0\n\tbuffer_load_dwordx4 %2, %3, %4 offen sc1 lds\n\ts_mov_b32 m0, %0" : "=&s"(keep) : "s"(ldsa), "v"(voff), "s"(rs), "s"(soff) : "memory");
}
DEV void dma4(u32x4 rs, unsigned ldsa, unsigned voff) {
  unsigned keep;
  asm volatile("s_mov_b32 %0, m0\n\ts_mov_b32 m0, %1\n\ts_nop 0\n\tbuffer_load_dword %2, %3, 0 offen lds\n\ts_mov_b32 m0, %0" : "=&s"(keep) : "s"(ldsa), "v"(voff), "s"(rs) : "memory");
}
constexpr unsigned OOB = 0x7ffffff0u;

DEV void p1_dma_stage(u32x4 rx, u32x4 rw, int s, int s0, int hw, unsigned vx, unsigned vw0, unsigned vw1) {
  const unsigned xs = (unsigned)ring_slot(s), wsl = xs + 16384;
  int ks = s + s0;
  if (ROT == 2) ks = s < 8 ? (ks & 7) : 8; else ks = ks >= 9 ? ks - 9 : ks;
#pragma unroll
  for (int u = 0; u < 4; u++) dma16(rx, xs + (4 * hw + u) * 1024, (u & 1) ? vx ^ 64u : vx, (unsigned)((4 * hw + u) * 8 * 1152 + ks * 128));
#pragma unroll
  for (int u = 0; u < 4; u++) dma16(rw, wsl + (4 * hw + u) * 1024, (u & 2) ? vw1 : vw0, (unsigned)((64 * ks + (4 * hw + u) * 4) * 3456));
}

DEV void b1_raw_dma(const Params& p, int base, int tb, int cb, int w, int lane) {
  const u32x4 rk = mk_rs(p.k, S * D * 2), rv = mk_rs(p.v, S * D * 2), rs = mk_rs(p.sk, D * 4 * 2);
  const unsigned vo = (unsigned)((64 * tb + (lane >> 3)) * 1152 + cb * 128 + (((lane & 7) ^ (lane >> 3)) << 4));
  const unsigned ve = w == 0 ? (tb ? vo - 9216u : OOB) : (lane < 32 ? (unsigned)(cb * 512 + lane * 16) : OOB);
  dma16(rk, (unsigned)(base + L_RK + (1 + w) * 1024), vo, (unsigned)(w * 9216));
  dma16(rk, (unsigned)(base + L_RK + (5 + w) * 1024), vo, (unsigned)((4 + w) * 9216));
  dma16(rv, (unsigned)(base + L_RV + w * 1024), vo, (unsigned)(w * 9216));
  dma16(rv, (unsigned)(base + L_RV + (4 + w) * 1024), vo, (unsigned)((4 + w) * 9216));
  if (w == 0) dma16(rk, (unsigned)(base + L_RK), ve, 0u);
  if (w == 1) dma16(rs, (unsigned)(base + L_RS), ve, 0u);
}
struct B1In { bf16x8 a[4][2]; s16x4 vv[4]; u32x2 w; };
DEV void b1_read(B1In& r, LAS char* base, LAS char* sk, int w4, int lane) {
  const int n = lane & 15, g = lane >> 4, q = (lane >> 2) & 3, pp = lane & 3;
  r.w = *(const LAS u32x2*)(sk + (16 * w4 + n) * 8);
#pragma unroll
  for (int tg = 0; tg < 4; tg++) {
#pragma unroll
    for (int mm = 0; mm < 2; mm++) {
      const int row = 32 * (tg >> 1) + 4 * (tg & 1) + 8 * (n >> 2) + (n & 3) + 5 + 2 * mm + (g >> 1);
      r.a[tg][mm] = *(const LAS bf16x8*)(base + L_RK + row * 128 + (((2 * w4 + (g & 1)) ^ (row & 7)) << 4));
    }
    const int rv = 32 * (tg >> 1) + 4 * (tg & 1) + 8 * g + q;
    r.vv[tg] = __builtin_amdgcn_ds_read_tr16_b64_v4i16((LAS s16x4*)(base + L_RV + rv * 128 + (((2 * w4 + (pp >> 1)) ^ (rv & 7)) << 4) + (pp & 1) * 8));
  }
}
struct B1Addr { unsigned a[4], v[2], w, st[2]; };
DEV void b1_addr(B1Addr& A, int w4, int lane) {
  const int n = lane & 15, g = lane >> 4, q = (lane >> 2) & 3, pp = lane & 3;
  A.w = L_RS + (16 * w4 + n) * 8;
#pragma unroll
  for (int e = 0; e < 2; e++) {
#pragma unroll
    for (int mm = 0; mm < 2; mm++) {
      const int row = 4 * e + 8 * (n >> 2) + (n & 3) + 5 + 2 * mm + (g >> 1);
      A.a[2 * e + mm] = L_RK + row * 128 + (((2 * w4 + (g & 1)) ^ (row & 7)) << 4);
    }
    const int rv = 4 * e + 8 * g + q;
    A.v[e] = L_RV + rv * 128 + (((2 * w4 + (pp >> 1)) ^ (rv & 7)) << 4) + (pp & 1) * 8;
    A.st[e] = L_B1 + w4 * 2048 + n * 128 + (((7 - 4 * e - g) ^ (n & 7)) << 4);
  }
  asm volatile("" : "+v"(A.a[0]), "+v"(A.a[1]), "+v"(A.a[2]), "+v"(A.a[3]), "+v"(A.v[0]), "+v"(A.v[1]), "+v"(A.w), "+v"(A.st[0]), "+v"(A.st[1]));
}
DEV void b1_read2(B1In& r, const B1Addr& A, LAS char* lds) {
  r.w = *(const LAS u32x2*)(lds + A.w);
#pragma unroll
  for (int tg = 0; tg < 4; tg++) {
#pragma unroll
    for (int mm = 0; mm < 2; mm++) r.a[tg][mm] = *(const LAS bf16x8*)(lds + A.a[2 * (tg & 1) + mm] + (tg >> 1) * 4096);
    r.vv[tg] = __builtin_amdgcn_ds_read_tr16_b64_v4i16((LAS s16x4*)(lds + A.v[tg & 1] + (tg >> 1) * 4096));
  }
}
template <bool PRE>
DEV void b1_math(const B1In& r, LAS char* stage, int w4, int lane, unsigned st0 = 0, unsigned st1 = 0) {
  const int n = lane & 15, g = lane >> 4;
  bf16x8 wd[2];
  {
    const bool act = (n >> 3) == (g & 1);
    const int sh = g < 2 ? 16 : 0, e = n & 7;
#pragma unroll
    for (int mm = 0; mm < 2; mm++) {
      const unsigned v = ((r.w[1 - mm] >> sh) & 0xffffu) << (16 * (e & 1));
      u32x4 f;
#pragma unroll
      for (int d = 0; d < 4; d++) f[d] = (act && (e >> 1) == d) ? v : 0u;
      wd[mm] = __builtin_bit_cast(bf16x8, f);
    }
  }
  f32x4 sa[4];
#pragma unroll
  for (int tg = 0; tg < 4; tg++) sa[tg] = __builtin_amdgcn_mfma_f32_16x16x32_bf16(r.a[tg][0], wd[0], (f32x4){0.f, 0.f, 0.f, 0.f}, 0, 0, 0);
#pragma unroll
  for (int tg = 0; tg < 4; tg++) sa[tg] = __builtin_amdgcn_mfma_f32_16x16x32_bf16(r.a[tg][1], wd[1], sa[tg], 0, 0, 0);
  LAS char* stg = stage + w4 * 2048;
#pragma unroll
  for (int u = 0; u < 2; u++) {
    u32x4 o;
#pragma unroll
    for (int e = 0; e < 2; e++) {
      const f32x4 s = sa[2 * u + e];
      const u32x2 vb = __builtin_bit_cast(u32x2, r.vv[2 * u + e]);
      const float s0 = s[0] * __uint_as_float(vb[0] << 16), s1 = s[1] * __uint_as_float(vb[0] & 0xffff0000u);
      const float s2 = s[2] * __uint_as_float(vb[1] << 16), s3 = s[3] * __uint_as_float(vb[1] & 0xffff0000u);
      o[2 - 2 * e] = pk_bf16(s3, s2); o[3 - 2 * e] = pk_bf16(s1, s0);
    }
    if (PRE) *(LAS u32x4*)(stage + (u ? st1 : st0)) = o;
    else *(LAS u32x4*)(stg + n * 128 + (((7 - 4 * u - g) ^ (n & 7)) << 4)) = o;
  }
}
DEV void b1_store(const Params& p, LAS char* stage, int tb, int cb, int w4, int lane) {
  LAS char* stg = stage + w4 * 2048;
  const __amdgpu_buffer_rsrc_t rk = __builtin_amdgcn_make_buffer_rsrc((void*)(p.ws + WS_KVT), 0, D * S * 2, 0x00020000);
#pragma unroll
  for (int u = 0; u < 2; u++) {
    const int ch = 8 * u + (lane >> 3);
    u32x4 v = *(const LAS u32x4*)(stg + ch * 128 + (lane & 7) * 16);
    __builtin_amdgcn_raw_buffer_store_b128(v, rk, (unsigned)((64 * cb + 16 * w4 + ch) * 4096 + (1984 - 64 * tb) * 2 + (((lane & 7) ^ (lane >> 3)) << 4)), 0, AUX_SC1);
  }
  FENCE();
}
DEV void b1_math_store(const Params& p, const B1In& r, LAS char* stage, int tb, int cb, int w4, int lane) {
  b1_math<false>(r, stage, w4, lane);
  FENCE();
  b1_store(p, stage, tb, cb, w4, lane);
}

struct HkRegs { u32x4 c0[3]; u32x2 c1[3]; };
DEV void hk_load_asm(const Params& p, HkRegs& r, int ch0, int tid4) {
  const GAS u32x4* hk = (const GAS u32x4*)p.hk;
#pragma unroll
  for (int cs = 0; cs < 2; cs++) {
    const int i0 = tid4 - 2, i1 = tid4 - 1;
    asm volatile("global_load_dwordx4 %0, %1, off" : "=v"(r.c0[cs]) : "v"(hk + (ch0 + cs) * 256 + (i0 < 0 ? 0 : i0)) : "memory");
    asm volatile("global_load_dwordx2 %0, %1, off" : "=v"(r.c1[cs]) : "v"(hk + (ch0 + cs) * 256 + (i1 < 0 ? 0 : i1)) : "memory");
  }
  const int cs = (tid4 >> 1) & 1, i0 = 254 + (tid4 & 1);
  asm volatile("global_load_dwordx4 %0, %1, off" : "=v"(r.c0[2]) : "v"(hk + (ch0 + cs) * 256 + i0) : "memory");
  asm volatile("global_load_dwordx2 %0, %1, off" : "=v"(r.c1[2]) : "v"(hk + (ch0 + cs) * 256 + 255) : "memory");
}
DEV void hk_pin(HkRegs& r) {
  asm volatile("" : "+v"(r.c0[0]), "+v"(r.c0[1]), "+v"(r.c0[2]), "+v"(r.c1[0]), "+v"(r.c1[1]), "+v"(r.c1[2]) :: "memory");
}
DEV void hk_put(LAS char* b, u32x4 c0, u32x2 c1) {
  const unsigned a0 = __builtin_amdgcn_alignbit(c0[1], c0[0], 16), a1 = __builtin_amdgcn_alignbit(c0[2], c0[1], 16), a2 = __builtin_amdgcn_alignbit(c0[3], c0[2], 16),
                 a3 = __builtin_amdgcn_alignbit(c1[0], c0[3], 16), a4 = __builtin_amdgcn_alignbit(c1[1], c1[0], 16);
  *(LAS u32x4*)(b) = (u32x4){a0, a1, a2, a3};
  *(LAS u32x4*)(b + CV_HP) = (u32x4){c0[1], c0[2], c0[3], c1[0]};
  *(LAS u32x4*)(b + 2 * CV_HP) = (u32x4){a1, a2, a3, a4};
  *(LAS u32x4*)(b + 3 * CV_HP) = (u32x4){c0[2], c0[3], c1[0], c1[1]};
}
DEV void hk_write_main(HkRegs& r, LAS char* cv, int tid4, int cs) {
  const u32x4 z4 = {0, 0, 0, 0};
  const u32x2 z2 = {0, 0};
  u32x4 c0 = r.c0[cs]; u32x2 c1 = r.c1[cs];
  if (tid4 < 2) c0 = z4;
  if (tid4 < 1) c1 = z2;
  hk_put(cv + (cs ? CV_HP1 : CV_HP0) + tid4 * 16, c0, c1);
}
DEV void hk_write_edge(HkRegs& r, LAS char* cv, int tid4) {
  const u32x4 z4 = {0, 0, 0, 0};
  const u32x2 z2 = {0, 0};
  if (tid4 < 4) hk_put(cv + (((tid4 >> 1) & 1) ? CV_HP1 : CV_HP0) + (256 + (tid4 & 1)) * 16, r.c0[2], (tid4 & 1) ? z2 : r.c1[2]);
  if (tid4 < 60) { const int cs = tid4 >= 30; *(LAS u32x4*)(cv + CV_UR0 + cs * CV_URS + (256 + tid4 - 30 * cs) * 16) = z4; }
}
DEV void ur_dma(const Params& p, LAS char* cv, int ch0, int w4, int lane) {
  const __amdgpu_buffer_rsrc_t rk = __builtin_amdgcn_make_buffer_rsrc((void*)(p.ws + WS_KVT), 0, D * S * 2, 0x00020000);
  const int cs = w4 >> 1;
#pragma unroll
  for (int u = 0; u < 2; u++) {
    const int pc = (w4 & 1) * 2 + u;
    __builtin_amdgcn_raw_ptr_buffer_load_lds(rk, cv + CV_UR0 + cs * CV_URS + pc * 1024, 16, (unsigned)(lane * 16), (ch0 + cs) * 4096 + pc * 1024, 0, AUX_SC1);
  }
}
DEV void ur_dma_h(const Params& p, int ch0, int w4, int lane, bool on) {
  const u32x4 rk = mk_rs(p.ws + WS_KVT, D * S * 2);
  const int cs = w4 >> 1;
#pragma unroll
  for (int u = 0; u < 2; u++) {
    const int pc = (w4 & 1) * 2 + u;
    dma16c(rk, (unsigned)(L_CONV + CV_UR0 + cs * CV_URS + pc * 1024), on ? (unsigned)(lane * 16) : OOB, (unsigned)((ch0 + cs) * 4096 + pc * 1024));
  }
}
DEV void ur_dma_asm(const Params& p, int ch0, int w4, int lane) {
  const u32x4 rk = mk_rs(p.ws + WS_KVT, D * S * 2);
  const int cs = w4 >> 1;
#pragma unroll
  for (int pc = 0; pc < 4; pc++)
    dma16c(rk, (unsigned)(L_CONV + CV_UR0 + cs * CV_URS + pc * 1024), (unsigned)(lane * 16), (unsigned)((ch0 + cs) * 4096 + pc * 1024));
}
template <bool PRE>
DEV void conv_mfma(const Params& p, LAS char* cv, int ch0, int w4, int lane, f32x4 (&acc)[8], unsigned& dbits) {
  const int cs = w4 >> 1, half = w4 & 1;
  const int r = lane & 15, g = lane >> 4;
  if (!PRE) dbits = p.Dd[ch0 + cs];
  FENCE();
  {
    LAS char* hp = cv + (cs ? CV_HP1 : CV_HP0) + (r & 3) * CV_HP + ((r & ~3) + 8 * g + half * 128) * 2;
    LAS char* ur = cv + CV_UR0 + cs * CV_URS + (2032 - 16 * r + 8 * g + half * 128) * 2;
#pragma unroll
    for (int i = 0; i < 8; i++) acc[i] = (f32x4){0.f, 0.f, 0.f, 0.f};
    bf16x8 A[2][8], F[2][8];
    auto ldA = [&](int ph, int a) {
      const u32x2 lo = *(const LAS u32x2*)(hp + (256 * a + 32 * ph) * 2), hi = *(const LAS u32x2*)(hp + (256 * a + 32 * ph) * 2 + 8);
      A[ph & 1][a] = __builtin_bit_cast(bf16x8, (u32x4){lo[0], lo[1], hi[0], hi[1]});
    };
    auto ldF = [&](int ph, int a) { F[ph & 1][a] = *(const LAS bf16x8*)(ur + (32 * ph - 256 * a) * 2); };
    ldA(0, 0);
#pragma unroll
    for (int a = 0; a < 8; a++) ldF(0, a);
#pragma unroll
    for (int a = 1; a < 8; a++) ldA(0, a);
#pragma unroll
    for (int ph = 0; ph < 4; ph++) {
      SCHED_FENCE();
      if (ph < 3) {
        ldA(ph + 1, 0);
#pragma unroll
        for (int a = 0; a < 8; a++) ldF(ph + 1, a);
#pragma unroll
        for (int a = 1; a < 8; a++) ldA(ph + 1, a);
      }
#pragma unroll
      for (int a = 0; a < 8; a++)
#pragma unroll
        for (int n = 0; n + a < 8; n++) acc[a + n] = __builtin_amdgcn_mfma_f32_16x16x32_bf16(A[ph & 1][a], F[ph & 1][n], acc[a + n], 0, 0, 0);
      if (ph < 3) {
#pragma unroll
        for (int i_ = 0; i_ < 24; i_++) { __builtin_amdgcn_sched_group_barrier(0x008, 1, 0); __builtin_amdgcn_sched_group_barrier(0x100, 1, 0); }
        __builtin_amdgcn_sched_group_barrier(0x008, 12, 0);
      }
    }
  }
  asm volatile("" : "+v"(dbits));

}
template <int HALF>
DEV void conv_finish_t(const Params& p, LAS char* cv, LAS char* xch, LAS char* xfl, int ch0, int w4, int lane, const f32x4 (&acc)[8], unsigned dbits) {
  const int cs = w4 >> 1;
  const int r = lane & 15, g = lane >> 4;
  LAS char* ur = cv + CV_UR0 + cs * CV_URS + (2044 - 1024 * HALF - 16 * r - 4 * g) * 2;
  u32x2 u[4];
#pragma unroll
  for (int i = 0; i < 4; i++) u[i] = *(const LAS u32x2*)(ur - 512 * i);
  {
    LAS char* x = xch + w4 * 4096 + lane * 16;
#pragma unroll
    for (int i = 0; i < 4; i++) *(LAS f32x4*)(x + i * 1024) = acc[4 * (1 - HALF) + i];
    FENCE();
    if (lane == 0) *(volatile LAS unsigned*)(xfl + w4 * 4) = 1u;
    FENCE();
#pragma unroll 1
    for (int it = 0; it < (1 << 24); it++)
      if (*(volatile LAS unsigned*)(xfl + (w4 ^ 1) * 4) == 1u) break;
    FENCE();
  }
  LAS char* x = xch + (w4 ^ 1) * 4096 + lane * 16;
  f32x4 o[4];
#pragma unroll
  for (int i = 0; i < 4; i++) o[i] = *(const LAS f32x4*)(x + i * 1024);
  const float Dv = __uint_as_float(dbits << 16);
  LAS char* zst = cv + (cs ? CV_HP1 : CV_HP0) + HALF * 2048;
#pragma unroll
  for (int i = 0; i < 4; i++) {
    f32x4 y = acc[4 * HALF + i] + o[i];
    y[3] += __uint_as_float(u[i][0] << 16) * Dv; y[2] += __uint_as_float(u[i][0] & 0xffff0000u) * Dv;
    y[1] += __uint_as_float(u[i][1] << 16) * Dv; y[0] += __uint_as_float(u[i][1] & 0xffff0000u) * Dv;
    *(LAS u32x2*)(zst + (256 * i + 16 * r + 4 * g) * 2) = (u32x2){pk_bf16(y[0], y[1]), pk_bf16(y[2], y[3])};
  }
  FENCE();
  const __amdgpu_buffer_rsrc_t rz = __builtin_amdgcn_make_buffer_rsrc((void*)(p.ws + WS_ZT), 0, D * S * 2, 0x00020000);
#pragma unroll
  for (int i = 0; i < 2; i++) {
    u32x4 v = *(const LAS u32x4*)(zst + i * 1024 + lane * 16);
    __builtin_amdgcn_raw_buffer_store_b128(v, rz, (unsigned)((ch0 + cs) * 4096 + HALF * 2048 + i * 1024 + lane * 16), 0, AUX_SC1);
  }
}
DEV void conv_finish(const Params& p, LAS char* cv, LAS char* xch, LAS char* xfl, int ch0, int w4, int lane, const f32x4 (&acc)[8], unsigned dbits) {
  if (w4 & 1) conv_finish_t<1>(p, cv, xch, xfl, ch0, w4, lane, acc, dbits);
  else conv_finish_t<0>(p, cv, xch, xfl, ch0, w4, lane, acc, dbits);
}

DEV void att_dma_kq(const Params& p, int rb, int head, int w, int lane) {
  const u32x4 rq = mk_rs(p.ws + WS_QKV, 1728 * S * 2);
  const int tok2 = 512 * (rb - 1) + lane * 16;
  const unsigned vk = tok2 < 0 ? OOB : (unsigned)(tok2 ^ (w << 6));
#pragma unroll
  for (int u = 0; u < 5; u++)
    if (u < 4 || w < 2) dma16c(rq, (unsigned)(L_ATT + A_K + (w + 4 * u) * 1024), vk, (unsigned)((576 + 18 * head + w + 4 * u) * 4096));
  const int w2 = (w + 2) & 3;
  const unsigned vq = (unsigned)((lane >> 5) * 4096 + rb * 512 + ((((lane & 31) ^ ((lane >> 5) << 2)) << 4) ^ ((w & 1) << 7)));
#pragma unroll
  for (int u = 0; u < 3; u++)
    if (u < 2 || w2 == 0) dma16c(rq, (unsigned)(L_ATT + A_Q + (w2 + 4 * u) * 1024), vq, (unsigned)((18 * head + 2 * (w2 + 4 * u)) * 4096));
}
DEV void att_dma_v(const Params& p, int rb, int head, int w, int lane) {
  const u32x4 rq = mk_rs(p.ws + WS_QKV, 1728 * S * 2);
  const int tok2 = 512 * (rb - 1) + lane * 16, w2 = (w + 2) & 3;
  const unsigned vv = tok2 < 0 ? OOB : (unsigned)(tok2 ^ (w2 << 4));
#pragma unroll
  for (int u = 0; u < 5; u++)
    if (u < 4 || w2 < 2) dma16c(rq, (unsigned)(L_ATT + A_V + (w2 + 4 * u) * 1024), vv ^ (unsigned)((u & 3) << 6), (unsigned)((1152 + 18 * head + w2 + 4 * u) * 4096));
}
DEV void att_poll(PollRegs& pr, const Params& p, int arb, int ahd, int qb, int lane, u64 tag) {
  const int l = lane < 24 ? lane : 0, r = l / 6, kind = (l - 6 * r) >> 1, e = l & 1;
  const int row = 2 * arb - 2 + r, col = kind * 576 + 18 * ahd + 17 * e;
  const bool act = lane < 24 && row >= 0 && !(kind == 0 && r < 2);
  const int mh = lane & 31, mb = (mh >> 2) + 8 * (4 * (qb >> 2) + (mh & 3));
  const int idx = act ? F_Q + row * 64 + (col >> 5) : (lane >= 32 ? F_SEEN + 4 * mb + (qb & 3) : F_Q + 2 * arb * 64 + ((18 * ahd) >> 5));
  if (PPQ) poll_pipe<PPQ ? PPQ : 1>(pr, p, idx, tag);
  else poll_block<QSL>(p, idx, true, tag);
}
DEV void att_ones(LAS char* lds, int arb, int tid4) {
  if (tid4 < 64) { const unsigned one = (8 * tid4 + 256 * (arb - 1) >= 0) ? 0x3f803f80u : 0u; *(LAS u32x4*)(lds + L_ATT + A_ONES + tid4 * 16) = (u32x4){one, one, one, one}; }
}
DEV void att_fetch(PollRegs& pr, const Params& p, LAS char* lds, int arb, int ahd, int qb, int w4, int lane, u64 tag) {
  att_ones(lds, arb, w4 * 64 + lane);
  FENCE();
  if (w4 == 0) {
    att_poll(pr, p, arb, ahd, qb, lane, tag);
    if (lane == 0) *(volatile LAS unsigned*)(lds + L_QRDY) = 1u;
  } else lds_wait(lds + L_QRDY);

  att_dma_kq(p, arb, ahd, w4, lane);
  att_dma_v(p, arb, ahd, w4, lane);
}
struct WoDma { u32x4 rs; unsigned off, l0, s0; };
DEV void wo_init(WoDma& d, const Params& p, int ns, int w, int lane) {
  const u64 a = (u64)p.wo;
  d.rs = (u32x4){(unsigned)a, (unsigned)(a >> 32) & 0xffffu, (unsigned)(D * D * 2), 0x00020000u};
  const int row = lane / 10, ch = lane - row * 10;
  d.off = ch == 9 ? OOB : (unsigned)(row * 1152 + ns * 144 + ch * 16);
  d.l0 = (unsigned)(L_WO + w * 24 * 960);
  d.s0 = (unsigned)(w * 24 * 6912);
}
DEV void wo_piece(const WoDma& d, int u, int lane) {
  if (u < 23 || lane < 60) dma16(d.rs, d.l0 + u * 960, d.off, d.s0 + u * 6912);
}
DEV bf16x8 cat44(s16x4 lo, s16x4 hi) { return __builtin_bit_cast(bf16x8, __builtin_shufflevector(lo, hi, 0, 1, 2, 3, 4, 5, 6, 7)); }
DEV s16x4 trr(LAS char* a) { return __builtin_amdgcn_ds_read_tr16_b64_v4i16((LAS s16x4*)a); }
#define MFMA32(a, b, c) __builtin_amdgcn_mfma_f32_32x32x16_bf16((a), (b), (c), 0, 0, 0)
#define MFMA32T(a, b, c) __builtin_amdgcn_mfma_f32_32x32x8bf16_1k((a), (b), (c), 0, 0, 0)
DEV float ex2(float x) { return __builtin_amdgcn_exp2f(x); }
DEV float swap_lo(float x) { auto r = __builtin_amdgcn_permlane32_swap(__float_as_uint(x), __float_as_uint(x), false, false); return __uint_as_float(r[0]); }

constexpr unsigned MISSING = 0xffffffffu;
DEV void o_store(const Params& p, int rb, int head, int wave, int lane, u32x4 om, unsigned tl) {
  const int c = lane & 31, h = lane >> 5;
  const __amdgpu_buffer_rsrc_t ro = __builtin_amdgcn_make_buffer_rsrc((void*)(p.ws + WS_OM), 0, 128 * 16 * 1024, 0x00020000);
  const __amdgpu_buffer_rsrc_t rt = __builtin_amdgcn_make_buffer_rsrc((void*)(p.ws + WS_OT), 0, 32 * S * 4, 0x00020000);
  const int qt16 = 16 * rb + 2 * wave + (c >> 4);
  __builtin_amdgcn_raw_buffer_store_b128(om, ro, (unsigned)(((qt16 * 16 + (head >> 1)) * 64 + (c & 15) + 16 * (2 * (head & 1) + h)) * 16), 0, AUX_SC1);
  __builtin_amdgcn_raw_buffer_store_b32(tl, rt, h ? OOB : (unsigned)((head * S + 256 * rb + 32 * wave + c) * 4), 0, AUX_SC1);
}
DEV void o_mark(const Params& p, int rb, int head, int wave, int lane) { o_store(p, rb, head, wave, lane, (u32x4){MISSING, MISSING, MISSING, MISSING}, MISSING); }
DEV unsigned pk_res(float a, float b) { const unsigned v = pk_bf16(a, b); return v < MISSING - 1 ? v : MISSING - 1; }
struct KFrag { s16x4 lo, hi, t; };
struct VFrag { bf16x8 v0, v1; };
struct AttAddr { LAS char *ak[4], *at[4], *av[4], *aw[4], *aq, *aqt; WoDma wd; unsigned mk[8]; };
constexpr u64 tri_mask(int lim) { u64 m = 0; for (int l = 0; l < 64; l++) if ((l & 31) - 8 * (l >> 5) < lim) m |= 1ull << l; return m; }
DEV void att_setup(AttAddr& A, const Params& p, LAS char* att, int ns, int wave, int lane) {
  const int c = lane & 31, h = lane >> 5;
  const int kh = (lane >> 4) & 1, q4 = (lane >> 2) & 3, p4 = lane & 3, pi = ((p4 & 1) << 1) | (p4 >> 1);
  const int row = 8 * h + q4, rowt = 16 + (q4 & 1);
  const int vrow = c < 18 ? c : ((c >= 20 && c < 28) ? 0 : 18);
  const int vkey = vrow == 18 ? 2 : (vrow & 15);
  A.aq = att + A_Q + row * 512 + ((wave >> 2) << 8) + (((wave & 3) ^ (row & 3)) << 6) + kh * 32 + p4 * 8;
  A.aqt = att + A_Q + rowt * 512 + ((wave >> 2) << 8) + (((wave & 3) ^ (rowt & 3)) << 6) + kh * 32 + p4 * 8;
#pragma unroll
  for (int mm = 0; mm < 4; mm++) {
    const int kt = wave + mm;
    A.ak[mm] = att + A_K + row * 1024 + ((kt >> 2) << 8) + (((kt & 3) ^ (row & 3)) << 6) + kh * 32 + pi * 8;
    A.at[mm] = att + A_K + rowt * 1024 + ((kt >> 2) << 8) + (((kt & 3) ^ (rowt & 3)) << 6) + kh * 32 + pi * 8;
    A.av[mm] = att + A_V + vrow * 1024 + ((kt >> 2) << 8) + (((4 * (kt & 3) + h) ^ vkey) << 4);
    A.aw[mm] = (LAS char*)((unsigned)(size_t)A.av[mm] ^ 32u);
    asm volatile("" : "+v"(A.ak[mm]), "+v"(A.at[mm]), "+v"(A.av[mm]), "+v"(A.aw[mm]));
  }
  wo_init(A.wd, p, ns, wave & 3, lane);
#pragma unroll
  for (int i = 0; i < 8; i++) {
    const int tv = c - 8 * h - 16 * (i >> 2) - 2 * (i & 3);
    A.mk[i] = (tv < 0 ? 0xffffu : 0u) | (tv < 1 ? 0xffff0000u : 0u);
    asm volatile("" : "+v"(A.mk[i]));
  }
  asm volatile("" : "+v"(A.aq), "+v"(A.aqt), "+v"(A.wd.off));
}
DEV void att_wave(const Params& p, LAS char* att, AttAddr& A, int rb, int head, int wave, int lane, int zidx, bool gate, int zwait, u64& zf, u64 tag) {
  const int c = lane & 31, h = lane >> 5;
  bf16x8 q0; s16x4 q1;
  q0 = cat44(trr(A.aq), trr(A.aq + 4 * 512));
  { const u32x2 tt = __builtin_bit_cast(u32x2, trr(A.aqt)); q1 = __builtin_bit_cast(s16x4, (u32x2){h ? 0u : tt[0], 0u}); }
  LAS char **ak = A.ak, **at = A.at, **av = A.av, **aw = A.aw;
  WoDma& wd = A.wd;
  auto ldk = [&](int m) { KFrag k; LAS char* a = ak[m & 3] + (m >> 2) * 256; k.lo = trr(a); k.hi = trr(a + 4 * 1024); k.t = trr(at[m & 3] + (m >> 2) * 256); return k; };
  auto ldv = [&](int m) { VFrag v; v.v0 = *(const LAS bf16x8*)(av[m & 3] + (m >> 2) * 256); v.v1 = *(const LAS bf16x8*)(aw[m & 3] + (m >> 2) * 256); return v; };
  f32x16 zero;
#pragma unroll
  for (int i = 0; i < 16; i++) zero[i] = 0.f;
  const int th = c - 8 * h;
  f32x16 o, sa, sb;
  {
    KFrag k0 = ldk(0), k8 = ldk(8), kn = ldk(1);
    VFrag v8 = ldv(8), v0 = ldv(0);
    SCHED_FENCE();
    sa = MFMA32(cat44(k0.lo, k0.hi), q0, zero);
    sb = MFMA32(cat44(k8.lo, k8.hi), q0, zero);
    sa = MFMA32T(k0.t, q1, sa);
    sb = MFMA32T(k8.t, q1, sb);
    f32x16 s1 = MFMA32(cat44(kn.lo, kn.hi), q0, zero);
    s1 = MFMA32T(kn.t, q1, s1);
    SCHED_FENCE();
    u32x4 pf, pd;
#pragma unroll
    for (int g = 0; g < 2; g++) {
#pragma unroll
      for (int k = 0; k < 4; k++) {
        const bool c0 = __builtin_amdgcn_inverse_ballot_w64(tri_mask(16 * g + 2 * k)), c1 = __builtin_amdgcn_inverse_ballot_w64(tri_mask(16 * g + 2 * k + 1));
        const float a0 = c0 ? sa[8 * g + 2 * k] : sb[8 * g + 2 * k], a1 = c1 ? sa[8 * g + 2 * k + 1] : sb[8 * g + 2 * k + 1];
        const unsigned pp = pk_bf16(ex2(a0), ex2(a1));
        pf[k] = pp & A.mk[4 * g + k]; pd[k] = pp ^ pf[k];
        if (FS && (k & 1)) SCHED_FENCE();
      }
      o = MFMA32(g ? v8.v1 : v8.v0, __builtin_bit_cast(bf16x8, pd), g ? o : zero);
      o = MFMA32(g ? v0.v1 : v0.v0, __builtin_bit_cast(bf16x8, pf), o);
    }
    sa = s1;
    SCHED_FENCE();
  }
  KFrag kc = ldk(2);
  VFrag vc = ldv(1);
#pragma unroll
  for (int m = 1; m < 8; m++) {
    KFrag kx; VFrag vx;
    SCHED_FENCE();
    if (m == ZC && gate) asm volatile("global_load_dwordx2 %0, %1, off sc1" : "+v"(zf) : "v"(flagp(p, zwait)) : "memory");
    if (m < 7 && !gate) {
#pragma unroll
      for (int i = 0; i < 4; i++) wo_piece(wd, 4 * m - 4 + i, lane);
    }
    if (m < 6) kx = ldk(m + 2);
    if (m < 7) vx = ldv(m + 1);
    SCHED_FENCE();
    if (m < 7) sb = MFMA32(cat44(kc.lo, kc.hi), q0, zero);
    SCHED_FENCE();
    float e[16];
#pragma unroll
    for (int i = 0; i < 8; i++) e[i] = ex2(sa[i]);
    SCHED_FENCE();
    if (m < 7) sb = MFMA32T(kc.t, q1, sb);
    SCHED_FENCE();
    u32x4 p0, p1;
#pragma unroll
    for (int k = 0; k < 4; k++) p0[k] = pk_bf16(e[2 * k], e[2 * k + 1]);
#pragma unroll
    for (int i = 8; i < 16; i++) e[i] = ex2(sa[i]);
    SCHED_FENCE();
    o = MFMA32(vc.v0, __builtin_bit_cast(bf16x8, p0), o);
    SCHED_FENCE();
#pragma unroll
    for (int k = 0; k < 4; k++) p1[k] = pk_bf16(e[8 + 2 * k], e[8 + 2 * k + 1]);
    SCHED_FENCE();
    o = MFMA32(vc.v1, __builtin_bit_cast(bf16x8, p1), o);
    if (m == ZM && zidx >= 0) {
      WAIT_VMN(4 * ZM);
      if (lane == 0) st_flag(flagp(p, zidx), tag);
    }
    sa = sb; kc = kx; vc = vx;
  }
  SCHED_FENCE();
  float l = swap_lo(o[10]);
  {
    float t = __builtin_fabsf(o[0]);
#pragma unroll
    for (int i = 1; i < 10; i++) t += __builtin_fabsf(o[i]);
    const bool good = l >= 0x1p-60f && l <= 0x1p60f && t <= 0x1p126f;
    if (__builtin_amdgcn_ballot_w64(!good) != 0) {
      const int mmin = rb >= 1 ? 0 : 8 - wave;
      const int kh = (lane >> 4) & 1, q4 = (lane >> 2) & 3, p4 = lane & 3, pi = ((p4 & 1) << 1) | (p4 >> 1);
      const int row = 8 * h + q4, rowt = 16 + (q4 & 1);
      const int vrow = c < 18 ? c : ((c >= 20 && c < 28) ? 0 : 18);
      const int vkey = vrow == 18 ? 2 : (vrow & 15);
      auto scores = [&](int m) -> f32x16 {
        const int kt = wave + m;
        LAS char* a = att + A_K + row * 1024 + ((kt >> 2) << 8) + (((kt & 3) ^ (row & 3)) << 6) + kh * 32 + pi * 8;
        const s16x4 lo = trr(a), hi = trr(a + 4 * 1024), tl = trr(att + A_K + rowt * 1024 + ((kt >> 2) << 8) + (((kt & 3) ^ (rowt & 3)) << 6) + kh * 32 + pi * 8);
        f32x16 s = MFMA32(cat44(lo, hi), q0, zero);
        s = MFMA32T(tl, q1, s);
        const int lo8 = m == 0 ? th + 1 : -64, hi8 = m == 8 ? th : 64;
#pragma unroll
        for (int i = 0; i < 16; i++) {
          const int idx = 16 * (i >> 3) + (i & 7);
          if (idx < lo8 || idx > hi8) s[i] = -__builtin_inff();
        }
        return s;
      };
      float mx = -__builtin_inff();
#pragma unroll 1
      for (int m = mmin; m <= 8; m++) {
        const f32x16 s = scores(m);
#pragma unroll
        for (int i = 0; i < 16; i++) mx = fmaxf(mx, s[i]);
      }
      { auto r = __builtin_amdgcn_permlane32_swap(__float_as_uint(mx), __float_as_uint(mx), false, false); mx = fmaxf(__uint_as_float(r[0]), __uint_as_float(r[1])); }
      o = zero;
#pragma unroll 1
      for (int m = mmin; m <= 8; m++) {
        const f32x16 s = scores(m);
        u32x4 p0, p1;
#pragma unroll
        for (int k = 0; k < 4; k++) { p0[k] = pk_bf16(ex2(s[2 * k] - mx), ex2(s[2 * k + 1] - mx)); p1[k] = pk_bf16(ex2(s[8 + 2 * k] - mx), ex2(s[8 + 2 * k + 1] - mx)); }
        const int kt = wave + m;
        LAS char* a = att + A_V + vrow * 1024 + ((kt >> 2) << 8) + (((4 * (kt & 3) + h) ^ vkey) << 4);
        o = MFMA32(*(const LAS bf16x8*)a, __builtin_bit_cast(bf16x8, p0), o);
        o = MFMA32(*(const LAS bf16x8*)((LAS char*)((unsigned)(size_t)a ^ 32u)), __builtin_bit_cast(bf16x8, p1), o);
      }
      l = swap_lo(o[10]);
    }
  }
  const float inv = __builtin_amdgcn_rcpf(l);
  o_store(p, rb, head, wave, lane, (u32x4){pk_res(o[0] * inv, o[1] * inv), pk_res(o[2] * inv, o[3] * inv), pk_res(o[4] * inv, o[5] * inv), pk_res(o[6] * inv, o[7] * inv)},
          pk_res(o[8] * inv, o[9] * inv));
}

DEV unsigned wo_frag(int lane, int nt) {
  return (unsigned)(nt < 2 ? nt * 1024 + lane * 16 : (nt == 2 ? (lane < 32 ? 2048 + lane * 16 : 18 * WO_RS + (lane - 32) * 16) : 18 * WO_RS + 512 + (nt - 3) * 1024 + lane * 16));
}
#define P3_LOAD0 \
    "s_add_u32 %[t], %[so], 0\n\t" \
    "buffer_load_dwordx4 v[80:83], %[vo], %[ro], %[t] offen sc1\n\t" \
    "buffer_load_dwordx4 v[84:87], %[vo], %[ro], %[t] offen offset:1024 sc1\n\t" \
    "buffer_load_dwordx4 v[88:91], %[vo], %[ro], %[t] offen offset:2048 sc1\n\t" \
    "buffer_load_dwordx4 v[92:95], %[vo], %[ro], %[t] offen offset:3072 sc1\n\t"
#define P3_LOAD1 \
    "s_add_u32 %[t], %[so], 4096\n\t" \
    "buffer_load_dwordx4 v[96:99], %[vo], %[ro], %[t] offen sc1\n\t" \
    "buffer_load_dwordx4 v[100:103], %[vo], %[ro], %[t] offen offset:1024 sc1\n\t" \
    "buffer_load_dwordx4 v[104:107], %[vo], %[ro], %[t] offen offset:2048 sc1\n\t" \
    "buffer_load_dwordx4 v[108:111], %[vo], %[ro], %[t] offen offset:3072 sc1\n\t" \
    "s_mov_b32 %[t], 0x0\n\t" \
    "buffer_load_dword v144, %[vt], %[rt], %[t] offen sc1\n\t" \
    "s_mov_b32 %[t], 0x2000\n\t" \
    "buffer_load_dword v145, %[vt], %[rt], %[t] offen sc1\n\t" \
    "s_mov_b32 %[t], 0x4000\n\t" \
    "buffer_load_dword v146, %[vt], %[rt], %[t] offen sc1\n\t" \
    "s_mov_b32 %[t], 0x6000\n\t" \
    "buffer_load_dword v147, %[vt], %[rt], %[t] offen sc1\n\t"
#define P3_LOAD2 \
    "s_add_u32 %[t], %[so], 8192\n\t" \
    "buffer_load_dwordx4 v[112:115], %[vo], %[ro], %[t] offen sc1\n\t" \
    "buffer_load_dwordx4 v[116:119], %[vo], %[ro], %[t] offen offset:1024 sc1\n\t" \
    "buffer_load_dwordx4 v[120:123], %[vo], %[ro], %[t] offen offset:2048 sc1\n\t" \
    "buffer_load_dwordx4 v[124:127], %[vo], %[ro], %[t] offen offset:3072 sc1\n\t"
#define P3_LOAD3 \
    "s_add_u32 %[t], %[so], 12288\n\t" \
    "buffer_load_dwordx4 v[128:131], %[vo], %[ro], %[t] offen sc1\n\t" \
    "buffer_load_dwordx4 v[132:135], %[vo], %[ro], %[t] offen offset:1024 sc1\n\t" \
    "buffer_load_dwordx4 v[136:139], %[vo], %[ro], %[t] offen offset:2048 sc1\n\t" \
    "buffer_load_dwordx4 v[140:143], %[vo], %[ro], %[t] offen offset:3072 sc1\n\t" \
    "s_mov_b32 %[t], 0x20000\n\t" \
    "buffer_load_dword v148, %[vt], %[rt], %[t] offen sc1\n\t" \
    "s_mov_b32 %[t], 0x22000\n\t" \
    "buffer_load_dword v149, %[vt], %[rt], %[t] offen sc1\n\t" \
    "s_mov_b32 %[t], 0x24000\n\t" \
    "buffer_load_dword v150, %[vt], %[rt], %[t] offen sc1\n\t" \
    "s_mov_b32 %[t], 0x26000\n\t" \
    "buffer_load_dword v151, %[vt], %[rt], %[t] offen sc1\n\t"
#define P3_L0A ""
#define P3_L1A ""
#define P3_L2A ""
#define P3_L3A ""
#define P3_DA ""
#define P3_L0B ""
#define P3_L1B ""
#define P3_L2B ""
#define P3_L3B ""
#define P3_DB ""
#define P3_L0C ""
#define P3_L1C ""
#define P3_L2C ""
#define P3_L3C ""
#define P3_DC ""
#define P3_L0D P3_LOAD0
#define P3_L1D P3_LOAD1
#define P3_L2D P3_LOAD2
#define P3_L3D P3_LOAD3
#define P3_DD "s_sleep %[pd]\n\t"
#define P3_ASM \
    P3_DA P3_L0A P3_L1A P3_L2A P3_L3A \
    "ds_read_b64_tr_b16 v[152:153], %[am] offset:0\n\t" \
    "ds_read_b64_tr_b16 v[154:155], %[am] offset:1280\n\t" \
    "ds_read_b64_tr_b16 v[156:157], %[am] offset:32\n\t" \
    "ds_read_b64_tr_b16 v[158:159], %[am] offset:1312\n\t" \
    "ds_read_b64_tr_b16 v[160:161], %[am] offset:64\n\t" \
    "ds_read_b64_tr_b16 v[162:163], %[am] offset:1344\n\t" \
    "ds_read_b64_tr_b16 v[164:165], %[am] offset:96\n\t" \
    "ds_read_b64_tr_b16 v[166:167], %[am] offset:1376\n\t" \
    "ds_read_b64_tr_b16 v[168:169], %[am] offset:128\n\t" \
    "ds_read_b64_tr_b16 v[170:171], %[am] offset:1408\n\t" \
    "ds_read_b64_tr_b16 v[172:173], %[am] offset:5760\n\t" \
    "ds_read_b64_tr_b16 v[174:175], %[am] offset:7040\n\t" \
    "ds_read_b64_tr_b16 v[176:177], %[am] offset:5792\n\t" \
    "ds_read_b64_tr_b16 v[178:179], %[am] offset:7072\n\t" \
    "ds_read_b64_tr_b16 v[180:181], %[am] offset:5824\n\t" \
    "ds_read_b64_tr_b16 v[182:183], %[am] offset:7104\n\t" \
    "ds_read_b64_tr_b16 v[184:185], %[am] offset:5856\n\t" \
    "ds_read_b64_tr_b16 v[186:187], %[am] offset:7136\n\t" \
    "ds_read_b64_tr_b16 v[188:189], %[am] offset:5888\n\t" \
    "ds_read_b64_tr_b16 v[190:191], %[am] offset:7168\n\t" \
    "s_waitcnt lgkmcnt(0)\n\t" \
    "ds_write_b128 %[w0], v[152:155] offset:0\n\t" \
    "ds_write_b128 %[w0], v[156:159] offset:1024\n\t" \
    "ds_write_b128 %[w2], v[160:163] offset:0\n\t" \
    "ds_write_b128 %[w0], v[164:167] offset:3392\n\t" \
    "ds_write_b128 %[w0], v[168:171] offset:4416\n\t" \
    "ds_write_b128 %[w0], v[172:175] offset:5760\n\t" \
    "ds_write_b128 %[w0], v[176:179] offset:6784\n\t" \
    "ds_write_b128 %[w2], v[180:183] offset:5760\n\t" \
    "ds_write_b128 %[w0], v[184:187] offset:9152\n\t" \
    "ds_write_b128 %[w0], v[188:191] offset:10176\n\t" \
    P3_DB P3_L0B P3_L1B P3_L2B P3_L3B \
    "ds_read_b64_tr_b16 v[152:153], %[am] offset:11520\n\t" \
    "ds_read_b64_tr_b16 v[154:155], %[am] offset:12800\n\t" \
    "ds_read_b64_tr_b16 v[156:157], %[am] offset:11552\n\t" \
    "ds_read_b64_tr_b16 v[158:159], %[am] offset:12832\n\t" \
    "ds_read_b64_tr_b16 v[160:161], %[am] offset:11584\n\t" \
    "ds_read_b64_tr_b16 v[162:163], %[am] offset:12864\n\t" \
    "ds_read_b64_tr_b16 v[164:165], %[am] offset:11616\n\t" \
    "ds_read_b64_tr_b16 v[166:167], %[am] offset:12896\n\t" \
    "ds_read_b64_tr_b16 v[168:169], %[am] offset:11648\n\t" \
    "ds_read_b64_tr_b16 v[170:171], %[am] offset:12928\n\t" \
    "ds_read_b64_tr_b16 v[172:173], %[am] offset:17280\n\t" \
    "ds_read_b64_tr_b16 v[174:175], %[am] offset:18560\n\t" \
    "ds_read_b64_tr_b16 v[176:177], %[am] offset:17312\n\t" \
    "ds_read_b64_tr_b16 v[178:179], %[am] offset:18592\n\t" \
    "ds_read_b64_tr_b16 v[180:181], %[am] offset:17344\n\t" \
    "ds_read_b64_tr_b16 v[182:183], %[am] offset:18624\n\t" \
    "ds_read_b64_tr_b16 v[184:185], %[am] offset:17376\n\t" \
    "ds_read_b64_tr_b16 v[186:187], %[am] offset:18656\n\t" \
    "ds_read_b64_tr_b16 v[188:189], %[am] offset:17408\n\t" \
    "ds_read_b64_tr_b16 v[190:191], %[am] offset:18688\n\t" \
    "s_waitcnt lgkmcnt(0)\n\t" \
    "ds_write_b128 %[w0], v[152:155] offset:11520\n\t" \
    "ds_write_b128 %[w0], v[156:159] offset:12544\n\t" \
    "ds_write_b128 %[w2], v[160:163] offset:11520\n\t" \
    "ds_write_b128 %[w0], v[164:167] offset:14912\n\t" \
    "ds_write_b128 %[w0], v[168:171] offset:15936\n\t" \
    "ds_write_b128 %[w0], v[172:175] offset:17280\n\t" \
    "ds_write_b128 %[w0], v[176:179] offset:18304\n\t" \
    "ds_write_b128 %[w2], v[180:183] offset:17280\n\t" \
    "ds_write_b128 %[w0], v[184:187] offset:20672\n\t" \
    "ds_write_b128 %[w0], v[188:191] offset:21696\n\t" \
    P3_DC P3_L0C P3_L1C P3_L2C P3_L3C \
    "v_add_u32 v253, 46080, %[f0]\n\t" \
    "v_add_u32 v254, 46080, %[f2]\n\t" \
    "s_waitcnt lgkmcnt(0)\n\t" \
    "s_barrier\n\t" \
    P3_DD P3_L0D P3_L1D P3_L2D P3_L3D \
    "ds_read_b64_tr_b16 v[152:153], %[atl] offset:0\n\t" \
    "ds_read_b64_tr_b16 v[154:155], %[atl] offset:5760\n\t" \
    "ds_read_b64_tr_b16 v[156:157], %[atl] offset:32\n\t" \
    "ds_read_b64_tr_b16 v[158:159], %[atl] offset:5792\n\t" \
    "ds_read_b64_tr_b16 v[160:161], %[atl] offset:64\n\t" \
    "ds_read_b64_tr_b16 v[162:163], %[atl] offset:5824\n\t" \
    "ds_read_b64_tr_b16 v[164:165], %[atl] offset:96\n\t" \
    "ds_read_b64_tr_b16 v[166:167], %[atl] offset:5856\n\t" \
    "ds_read_b64_tr_b16 v[168:169], %[atl] offset:128\n\t" \
    "ds_read_b64_tr_b16 v[170:171], %[atl] offset:5888\n\t" \
    "ds_read_b64_tr_b16 v[172:173], %[atl] offset:46080\n\t" \
    "ds_read_b64_tr_b16 v[174:175], %[atl] offset:51840\n\t" \
    "ds_read_b64_tr_b16 v[176:177], %[atl] offset:46112\n\t" \
    "ds_read_b64_tr_b16 v[178:179], %[atl] offset:51872\n\t" \
    "ds_read_b64_tr_b16 v[180:181], %[atl] offset:46144\n\t" \
    "ds_read_b64_tr_b16 v[182:183], %[atl] offset:51904\n\t" \
    "ds_read_b64_tr_b16 v[184:185], %[atl] offset:46176\n\t" \
    "ds_read_b64_tr_b16 v[186:187], %[atl] offset:51936\n\t" \
    "ds_read_b64_tr_b16 v[188:189], %[atl] offset:46208\n\t" \
    "ds_read_b64_tr_b16 v[190:191], %[atl] offset:51968\n\t" \
    "ds_read_b128 v[192:195], %[f0] offset:0\n\t" \
    "ds_read_b128 v[196:199], %[f0] offset:1024\n\t" \
    "ds_read_b128 v[200:203], %[f2] offset:0\n\t" \
    "ds_read_b128 v[204:207], %[f0] offset:3392\n\t" \
    "ds_read_b128 v[208:211], %[f0] offset:4416\n\t" \
    "ds_read_b128 v[212:215], %[f0] offset:5760\n\t" \
    "ds_read_b128 v[216:219], %[f0] offset:6784\n\t" \
    "ds_read_b128 v[220:223], %[f2] offset:5760\n\t" \
    "ds_read_b128 v[224:227], %[f0] offset:9152\n\t" \
    "ds_read_b128 v[228:231], %[f0] offset:10176\n\t" \
    "s_mov_b32 %[ag], 0\n\t" \
    "s_mov_b32 %[cnt], %[spin]\n\t" \
    "s_waitcnt vmcnt(20)\n\t" \
    "20:\n\t" \
    "v_max3_u32 v252, v80, v81, v82\n\t" \
    "v_max3_u32 v252, v252, v83, v84\n\t" \
    "v_max3_u32 v252, v252, v85, v86\n\t" \
    "v_max3_u32 v252, v252, v87, v88\n\t" \
    "v_max3_u32 v252, v252, v89, v90\n\t" \
    "v_max3_u32 v252, v252, v91, v92\n\t" \
    "v_max3_u32 v252, v252, v93, v94\n\t" \
    "v_max_u32 v252, v252, v95\n\t" \
    "v_cmp_eq_u32 vcc, -1, v252\n\t" \
    "s_cbranch_vccnz 30f\n\t" \
    "40:\n\t" \
    "s_waitcnt lgkmcnt(9)\n\t" \
    "v_mfma_f32_16x16x32_bf16 %[a0], v[192:195], v[80:83], 0\n\t" \
    "ds_read_b128 v[232:235], %[f0] offset:11520\n\t" \
    "s_waitcnt lgkmcnt(9)\n\t" \
    "v_mfma_f32_16x16x32_bf16 %[a1], v[196:199], v[80:83], 0\n\t" \
    "ds_read_b128 v[236:239], %[f0] offset:12544\n\t" \
    "s_waitcnt lgkmcnt(9)\n\t" \
    "v_mfma_f32_16x16x32_bf16 %[a2], v[200:203], v[80:83], 0\n\t" \
    "ds_read_b128 v[240:243], %[f2] offset:11520\n\t" \
    "s_waitcnt lgkmcnt(9)\n\t" \
    "v_mfma_f32_16x16x32_bf16 %[a3], v[204:207], v[80:83], 0\n\t" \
    "ds_read_b128 v[244:247], %[f0] offset:14912\n\t" \
    "s_waitcnt lgkmcnt(9)\n\t" \
    "v_mfma_f32_16x16x32_bf16 %[a4], v[208:211], v[80:83], 0\n\t" \
    "ds_read_b128 v[248:251], %[f0] offset:15936\n\t" \
    "s_waitcnt lgkmcnt(9)\n\t" \
    "v_mfma_f32_16x16x32_bf16 %[a0], v[212:215], v[84:87], %[a0]\n\t" \
    "ds_read_b128 v[192:195], %[f0] offset:17280\n\t" \
    "s_waitcnt lgkmcnt(9)\n\t" \
    "v_mfma_f32_16x16x32_bf16 %[a1], v[216:219], v[84:87], %[a1]\n\t" \
    "ds_read_b128 v[196:199], %[f0] offset:18304\n\t" \
    "s_waitcnt lgkmcnt(9)\n\t" \
    "v_mfma_f32_16x16x32_bf16 %[a2], v[220:223], v[84:87], %[a2]\n\t" \
    "ds_read_b128 v[200:203], %[f2] offset:17280\n\t" \
    "s_waitcnt lgkmcnt(9)\n\t" \
    "v_mfma_f32_16x16x32_bf16 %[a3], v[224:227], v[84:87], %[a3]\n\t" \
    "ds_read_b128 v[204:207], %[f0] offset:20672\n\t" \
    "s_waitcnt lgkmcnt(9)\n\t" \
    "v_mfma_f32_16x16x32_bf16 %[a4], v[228:231], v[84:87], %[a4]\n\t" \
    "ds_read_b128 v[208:211], %[f0] offset:21696\n\t" \
    "s_waitcnt lgkmcnt(9)\n\t" \
    "v_mfma_f32_16x16x32_bf16 %[a0], v[232:235], v[88:91], %[a0]\n\t" \
    "ds_read_b128 v[212:215], %[f0] offset:23040\n\t" \
    "s_waitcnt lgkmcnt(9)\n\t" \
    "v_mfma_f32_16x16x32_bf16 %[a1], v[236:239], v[88:91], %[a1]\n\t" \
    "ds_read_b128 v[216:219], %[f0] offset:24064\n\t" \
    "s_waitcnt lgkmcnt(9)\n\t" \
    "v_mfma_f32_16x16x32_bf16 %[a2], v[240:243], v[88:91], %[a2]\n\t" \
    "ds_read_b128 v[220:223], %[f2] offset:23040\n\t" \
    "s_waitcnt lgkmcnt(9)\n\t" \
    "v_mfma_f32_16x16x32_bf16 %[a3], v[244:247], v[88:91], %[a3]\n\t" \
    "ds_read_b128 v[224:227], %[f0] offset:26432\n\t" \
    "s_waitcnt lgkmcnt(9)\n\t" \
    "v_mfma_f32_16x16x32_bf16 %[a4], v[248:251], v[88:91], %[a4]\n\t" \
    "ds_read_b128 v[228:231], %[f0] offset:27456\n\t" \
    "s_waitcnt lgkmcnt(9)\n\t" \
    "v_mfma_f32_16x16x32_bf16 %[a0], v[192:195], v[92:95], %[a0]\n\t" \
    "ds_read_b128 v[232:235], %[f0] offset:28800\n\t" \
    "s_waitcnt lgkmcnt(9)\n\t" \
    "v_mfma_f32_16x16x32_bf16 %[a1], v[196:199], v[92:95], %[a1]\n\t" \
    "ds_read_b128 v[236:239], %[f0] offset:29824\n\t" \
    "s_waitcnt lgkmcnt(9)\n\t" \
    "v_mfma_f32_16x16x32_bf16 %[a2], v[200:203], v[92:95], %[a2]\n\t" \
    "ds_read_b128 v[240:243], %[f2] offset:28800\n\t" \
    "s_waitcnt lgkmcnt(9)\n\t" \
    "v_mfma_f32_16x16x32_bf16 %[a3], v[204:207], v[92:95], %[a3]\n\t" \
    "ds_read_b128 v[244:247], %[f0] offset:32192\n\t" \
    "s_waitcnt lgkmcnt(9)\n\t" \
    "v_mfma_f32_16x16x32_bf16 %[a4], v[208:211], v[92:95], %[a4]\n\t" \
    "ds_read_b128 v[248:251], %[f0] offset:33216\n\t" \
    "s_cmp_eq_u32 %[ag], 0\n\t" \
    "s_cbranch_scc1 11f\n\t" \
    "s_waitcnt vmcnt(0)\n\t" \
    "11:\n\t" \
    "s_waitcnt vmcnt(12)\n\t" \
    "21:\n\t" \
    "v_max3_u32 v252, v96, v97, v98\n\t" \
    "v_max3_u32 v252, v252, v99, v100\n\t" \
    "v_max3_u32 v252, v252, v101, v102\n\t" \
    "v_max3_u32 v252, v252, v103, v104\n\t" \
    "v_max3_u32 v252, v252, v105, v106\n\t" \
    "v_max3_u32 v252, v252, v107, v108\n\t" \
    "v_max3_u32 v252, v252, v109, v110\n\t" \
    "v_max3_u32 v252, v252, v111, v144\n\t" \
    "v_max3_u32 v252, v252, v145, v146\n\t" \
    "v_max_u32 v252, v252, v147\n\t" \
    "v_cmp_eq_u32 vcc, -1, v252\n\t" \
    "s_cbranch_vccnz 31f\n\t" \
    "41:\n\t" \
    "s_waitcnt lgkmcnt(9)\n\t" \
    "v_mfma_f32_16x16x32_bf16 %[a0], v[212:215], v[96:99], %[a0]\n\t" \
    "ds_read_b128 v[192:195], %[f0] offset:34560\n\t" \
    "s_waitcnt lgkmcnt(9)\n\t" \
    "v_mfma_f32_16x16x32_bf16 %[a1], v[216:219], v[96:99], %[a1]\n\t" \
    "ds_read_b128 v[196:199], %[f0] offset:35584\n\t" \
    "s_waitcnt lgkmcnt(9)\n\t" \
    "v_mfma_f32_16x16x32_bf16 %[a2], v[220:223], v[96:99], %[a2]\n\t" \
    "ds_read_b128 v[200:203], %[f2] offset:34560\n\t" \
    "s_waitcnt lgkmcnt(9)\n\t" \
    "v_mfma_f32_16x16x32_bf16 %[a3], v[224:227], v[96:99], %[a3]\n\t" \
    "ds_read_b128 v[204:207], %[f0] offset:37952\n\t" \
    "s_waitcnt lgkmcnt(9)\n\t" \
    "v_mfma_f32_16x16x32_bf16 %[a4], v[228:231], v[96:99], %[a4]\n\t" \
    "ds_read_b128 v[208:211], %[f0] offset:38976\n\t" \
    "s_waitcnt lgkmcnt(9)\n\t" \
    "v_mfma_f32_16x16x32_bf16 %[a0], v[232:235], v[100:103], %[a0]\n\t" \
    "ds_read_b128 v[212:215], %[f0] offset:40320\n\t" \
    "s_waitcnt lgkmcnt(9)\n\t" \
    "v_mfma_f32_16x16x32_bf16 %[a1], v[236:239], v[100:103], %[a1]\n\t" \
    "ds_read_b128 v[216:219], %[f0] offset:41344\n\t" \
    "s_waitcnt lgkmcnt(9)\n\t" \
    "v_mfma_f32_16x16x32_bf16 %[a2], v[240:243], v[100:103], %[a2]\n\t" \
    "ds_read_b128 v[220:223], %[f2] offset:40320\n\t" \
    "s_waitcnt lgkmcnt(9)\n\t" \
    "v_mfma_f32_16x16x32_bf16 %[a3], v[244:247], v[100:103], %[a3]\n\t" \
    "ds_read_b128 v[224:227], %[f0] offset:43712\n\t" \
    "s_waitcnt lgkmcnt(9)\n\t" \
    "v_mfma_f32_16x16x32_bf16 %[a4], v[248:251], v[100:103], %[a4]\n\t" \
    "ds_read_b128 v[228:231], %[f0] offset:44736\n\t" \
    "s_waitcnt lgkmcnt(9)\n\t" \
    "v_mfma_f32_16x16x32_bf16 %[a0], v[192:195], v[104:107], %[a0]\n\t" \
    "ds_read_b128 v[232:235], v253 offset:0\n\t" \
    "s_waitcnt lgkmcnt(9)\n\t" \
    "v_mfma_f32_16x16x32_bf16 %[a1], v[196:199], v[104:107], %[a1]\n\t" \
    "ds_read_b128 v[236:239], v253 offset:1024\n\t" \
    "s_waitcnt lgkmcnt(9)\n\t" \
    "v_mfma_f32_16x16x32_bf16 %[a2], v[200:203], v[104:107], %[a2]\n\t" \
    "ds_read_b128 v[240:243], v254 offset:0\n\t" \
    "s_waitcnt lgkmcnt(9)\n\t" \
    "v_mfma_f32_16x16x32_bf16 %[a3], v[204:207], v[104:107], %[a3]\n\t" \
    "ds_read_b128 v[244:247], v253 offset:3392\n\t" \
    "s_waitcnt lgkmcnt(9)\n\t" \
    "v_mfma_f32_16x16x32_bf16 %[a4], v[208:211], v[104:107], %[a4]\n\t" \
    "ds_read_b128 v[248:251], v253 offset:4416\n\t" \
    "s_waitcnt lgkmcnt(9)\n\t" \
    "v_mfma_f32_16x16x32_bf16 %[a0], v[212:215], v[108:111], %[a0]\n\t" \
    "ds_read_b128 v[192:195], v253 offset:5760\n\t" \
    "s_waitcnt lgkmcnt(9)\n\t" \
    "v_mfma_f32_16x16x32_bf16 %[a1], v[216:219], v[108:111], %[a1]\n\t" \
    "ds_read_b128 v[196:199], v253 offset:6784\n\t" \
    "s_waitcnt lgkmcnt(9)\n\t" \
    "v_mfma_f32_16x16x32_bf16 %[a2], v[220:223], v[108:111], %[a2]\n\t" \
    "ds_read_b128 v[200:203], v254 offset:5760\n\t" \
    "s_waitcnt lgkmcnt(9)\n\t" \
    "v_mfma_f32_16x16x32_bf16 %[a3], v[224:227], v[108:111], %[a3]\n\t" \
    "ds_read_b128 v[204:207], v253 offset:9152\n\t" \
    "s_waitcnt lgkmcnt(9)\n\t" \
    "v_mfma_f32_16x16x32_bf16 %[a4], v[228:231], v[108:111], %[a4]\n\t" \
    "ds_read_b128 v[208:211], v253 offset:10176\n\t" \
    "v_mfma_f32_16x16x32_bf16 %[a0], v[152:155], v[144:147], %[a0]\n\t" \
    "v_mfma_f32_16x16x32_bf16 %[a1], v[156:159], v[144:147], %[a1]\n\t" \
    "v_mfma_f32_16x16x32_bf16 %[a2], v[160:163], v[144:147], %[a2]\n\t" \
    "v_mfma_f32_16x16x32_bf16 %[a3], v[164:167], v[144:147], %[a3]\n\t" \
    "v_mfma_f32_16x16x32_bf16 %[a4], v[168:171], v[144:147], %[a4]\n\t" \
    "s_cmp_eq_u32 %[ag], 0\n\t" \
    "s_cbranch_scc1 12f\n\t" \
    "s_waitcnt vmcnt(0)\n\t" \
    "12:\n\t" \
    "s_waitcnt vmcnt(8)\n\t" \
    "22:\n\t" \
    "v_max3_u32 v252, v112, v113, v114\n\t" \
    "v_max3_u32 v252, v252, v115, v116\n\t" \
    "v_max3_u32 v252, v252, v117, v118\n\t" \
    "v_max3_u32 v252, v252, v119, v120\n\t" \
    "v_max3_u32 v252, v252, v121, v122\n\t" \
    "v_max3_u32 v252, v252, v123, v124\n\t" \
    "v_max3_u32 v252, v252, v125, v126\n\t" \
    "v_max_u32 v252, v252, v127\n\t" \
    "v_cmp_eq_u32 vcc, -1, v252\n\t" \
    "s_cbranch_vccnz 32f\n\t" \
    "42:\n\t" \
    "s_waitcnt lgkmcnt(9)\n\t" \
    "v_mfma_f32_16x16x32_bf16 %[a0], v[232:235], v[112:115], %[a0]\n\t" \
    "ds_read_b128 v[212:215], v253 offset:11520\n\t" \
    "s_waitcnt lgkmcnt(9)\n\t" \
    "v_mfma_f32_16x16x32_bf16 %[a1], v[236:239], v[112:115], %[a1]\n\t" \
    "ds_read_b128 v[216:219], v253 offset:12544\n\t" \
    "s_waitcnt lgkmcnt(9)\n\t" \
    "v_mfma_f32_16x16x32_bf16 %[a2], v[240:243], v[112:115], %[a2]\n\t" \
    "ds_read_b128 v[220:223], v254 offset:11520\n\t" \
    "s_waitcnt lgkmcnt(9)\n\t" \
    "v_mfma_f32_16x16x32_bf16 %[a3], v[244:247], v[112:115], %[a3]\n\t" \
    "ds_read_b128 v[224:227], v253 offset:14912\n\t" \
    "s_waitcnt lgkmcnt(9)\n\t" \
    "v_mfma_f32_16x16x32_bf16 %[a4], v[248:251], v[112:115], %[a4]\n\t" \
    "ds_read_b128 v[228:231], v253 offset:15936\n\t" \
    "s_waitcnt lgkmcnt(9)\n\t" \
    "v_mfma_f32_16x16x32_bf16 %[a0], v[192:195], v[116:119], %[a0]\n\t" \
    "ds_read_b128 v[232:235], v253 offset:17280\n\t" \
    "s_waitcnt lgkmcnt(9)\n\t" \
    "v_mfma_f32_16x16x32_bf16 %[a1], v[196:199], v[116:119], %[a1]\n\t" \
    "ds_read_b128 v[236:239], v253 offset:18304\n\t" \
    "s_waitcnt lgkmcnt(9)\n\t" \
    "v_mfma_f32_16x16x32_bf16 %[a2], v[200:203], v[116:119], %[a2]\n\t" \
    "ds_read_b128 v[240:243], v254 offset:17280\n\t" \
    "s_waitcnt lgkmcnt(9)\n\t" \
    "v_mfma_f32_16x16x32_bf16 %[a3], v[204:207], v[116:119], %[a3]\n\t" \
    "ds_read_b128 v[244:247], v253 offset:20672\n\t" \
    "s_waitcnt lgkmcnt(9)\n\t" \
    "v_mfma_f32_16x16x32_bf16 %[a4], v[208:211], v[116:119], %[a4]\n\t" \
    "ds_read_b128 v[248:251], v253 offset:21696\n\t" \
    "s_waitcnt lgkmcnt(9)\n\t" \
    "v_mfma_f32_16x16x32_bf16 %[a0], v[212:215], v[120:123], %[a0]\n\t" \
    "ds_read_b128 v[192:195], v253 offset:23040\n\t" \
    "s_waitcnt lgkmcnt(9)\n\t" \
    "v_mfma_f32_16x16x32_bf16 %[a1], v[216:219], v[120:123], %[a1]\n\t" \
    "ds_read_b128 v[196:199], v253 offset:24064\n\t" \
    "s_waitcnt lgkmcnt(9)\n\t" \
    "v_mfma_f32_16x16x32_bf16 %[a2], v[220:223], v[120:123], %[a2]\n\t" \
    "ds_read_b128 v[200:203], v254 offset:23040\n\t" \
    "s_waitcnt lgkmcnt(9)\n\t" \
    "v_mfma_f32_16x16x32_bf16 %[a3], v[224:227], v[120:123], %[a3]\n\t" \
    "ds_read_b128 v[204:207], v253 offset:26432\n\t" \
    "s_waitcnt lgkmcnt(9)\n\t" \
    "v_mfma_f32_16x16x32_bf16 %[a4], v[228:231], v[120:123], %[a4]\n\t" \
    "ds_read_b128 v[208:211], v253 offset:27456\n\t" \
    "s_waitcnt lgkmcnt(9)\n\t" \
    "v_mfma_f32_16x16x32_bf16 %[a0], v[232:235], v[124:127], %[a0]\n\t" \
    "ds_read_b128 v[212:215], v253 offset:28800\n\t" \
    "s_waitcnt lgkmcnt(9)\n\t" \
    "v_mfma_f32_16x16x32_bf16 %[a1], v[236:239], v[124:127], %[a1]\n\t" \
    "ds_read_b128 v[216:219], v253 offset:29824\n\t" \
    "s_waitcnt lgkmcnt(9)\n\t" \
    "v_mfma_f32_16x16x32_bf16 %[a2], v[240:243], v[124:127], %[a2]\n\t" \
    "ds_read_b128 v[220:223], v254 offset:28800\n\t" \
    "s_waitcnt lgkmcnt(9)\n\t" \
    "v_mfma_f32_16x16x32_bf16 %[a3], v[244:247], v[124:127], %[a3]\n\t" \
    "ds_read_b128 v[224:227], v253 offset:32192\n\t" \
    "s_waitcnt lgkmcnt(9)\n\t" \
    "v_mfma_f32_16x16x32_bf16 %[a4], v[248:251], v[124:127], %[a4]\n\t" \
    "ds_read_b128 v[228:231], v253 offset:33216\n\t" \
    "s_cmp_eq_u32 %[ag], 0\n\t" \
    "s_cbranch_scc1 13f\n\t" \
    "s_waitcnt vmcnt(0)\n\t" \
    "13:\n\t" \
    "s_waitcnt vmcnt(0)\n\t" \
    "23:\n\t" \
    "v_max3_u32 v252, v128, v129, v130\n\t" \
    "v_max3_u32 v252, v252, v131, v132\n\t" \
    "v_max3_u32 v252, v252, v133, v134\n\t" \
    "v_max3_u32 v252, v252, v135, v136\n\t" \
    "v_max3_u32 v252, v252, v137, v138\n\t" \
    "v_max3_u32 v252, v252, v139, v140\n\t" \
    "v_max3_u32 v252, v252, v141, v142\n\t" \
    "v_max3_u32 v252, v252, v143, v148\n\t" \
    "v_max3_u32 v252, v252, v149, v150\n\t" \
    "v_max_u32 v252, v252, v151\n\t" \
    "v_cmp_eq_u32 vcc, -1, v252\n\t" \
    "s_cbranch_vccnz 33f\n\t" \
    "43:\n\t" \
    "s_waitcnt lgkmcnt(9)\n\t" \
    "v_mfma_f32_16x16x32_bf16 %[a0], v[192:195], v[128:131], %[a0]\n\t" \
    "ds_read_b128 v[232:235], v253 offset:34560\n\t" \
    "s_waitcnt lgkmcnt(9)\n\t" \
    "v_mfma_f32_16x16x32_bf16 %[a1], v[196:199], v[128:131], %[a1]\n\t" \
    "ds_read_b128 v[236:239], v253 offset:35584\n\t" \
    "s_waitcnt lgkmcnt(9)\n\t" \
    "v_mfma_f32_16x16x32_bf16 %[a2], v[200:203], v[128:131], %[a2]\n\t" \
    "ds_read_b128 v[240:243], v254 offset:34560\n\t" \
    "s_waitcnt lgkmcnt(9)\n\t" \
    "v_mfma_f32_16x16x32_bf16 %[a3], v[204:207], v[128:131], %[a3]\n\t" \
    "ds_read_b128 v[244:247], v253 offset:37952\n\t" \
    "s_waitcnt lgkmcnt(9)\n\t" \
    "v_mfma_f32_16x16x32_bf16 %[a4], v[208:211], v[128:131], %[a4]\n\t" \
    "ds_read_b128 v[248:251], v253 offset:38976\n\t" \
    "s_waitcnt lgkmcnt(9)\n\t" \
    "v_mfma_f32_16x16x32_bf16 %[a0], v[212:215], v[132:135], %[a0]\n\t" \
    "ds_read_b128 v[192:195], v253 offset:40320\n\t" \
    "s_waitcnt lgkmcnt(9)\n\t" \
    "v_mfma_f32_16x16x32_bf16 %[a1], v[216:219], v[132:135], %[a1]\n\t" \
    "ds_read_b128 v[196:199], v253 offset:41344\n\t" \
    "s_waitcnt lgkmcnt(9)\n\t" \
    "v_mfma_f32_16x16x32_bf16 %[a2], v[220:223], v[132:135], %[a2]\n\t" \
    "ds_read_b128 v[200:203], v254 offset:40320\n\t" \
    "s_waitcnt lgkmcnt(9)\n\t" \
    "v_mfma_f32_16x16x32_bf16 %[a3], v[224:227], v[132:135], %[a3]\n\t" \
    "ds_read_b128 v[204:207], v253 offset:43712\n\t" \
    "s_waitcnt lgkmcnt(9)\n\t" \
    "v_mfma_f32_16x16x32_bf16 %[a4], v[228:231], v[132:135], %[a4]\n\t" \
    "ds_read_b128 v[208:211], v253 offset:44736\n\t" \
    "s_waitcnt lgkmcnt(9)\n\t" \
    "v_mfma_f32_16x16x32_bf16 %[a0], v[232:235], v[136:139], %[a0]\n\t" \
    "s_waitcnt lgkmcnt(8)\n\t" \
    "v_mfma_f32_16x16x32_bf16 %[a1], v[236:239], v[136:139], %[a1]\n\t" \
    "s_waitcnt lgkmcnt(7)\n\t" \
    "v_mfma_f32_16x16x32_bf16 %[a2], v[240:243], v[136:139], %[a2]\n\t" \
    "s_waitcnt lgkmcnt(6)\n\t" \
    "v_mfma_f32_16x16x32_bf16 %[a3], v[244:247], v[136:139], %[a3]\n\t" \
    "s_waitcnt lgkmcnt(5)\n\t" \
    "v_mfma_f32_16x16x32_bf16 %[a4], v[248:251], v[136:139], %[a4]\n\t" \
    "s_waitcnt lgkmcnt(4)\n\t" \
    "v_mfma_f32_16x16x32_bf16 %[a0], v[192:195], v[140:143], %[a0]\n\t" \
    "s_waitcnt lgkmcnt(3)\n\t" \
    "v_mfma_f32_16x16x32_bf16 %[a1], v[196:199], v[140:143], %[a1]\n\t" \
    "s_waitcnt lgkmcnt(2)\n\t" \
    "v_mfma_f32_16x16x32_bf16 %[a2], v[200:203], v[140:143], %[a2]\n\t" \
    "s_waitcnt lgkmcnt(1)\n\t" \
    "v_mfma_f32_16x16x32_bf16 %[a3], v[204:207], v[140:143], %[a3]\n\t" \
    "s_waitcnt lgkmcnt(0)\n\t" \
    "v_mfma_f32_16x16x32_bf16 %[a4], v[208:211], v[140:143], %[a4]\n\t" \
    "v_mfma_f32_16x16x32_bf16 %[a0], v[172:175], v[148:151], %[a0]\n\t" \
    "v_mfma_f32_16x16x32_bf16 %[a1], v[176:179], v[148:151], %[a1]\n\t" \
    "v_mfma_f32_16x16x32_bf16 %[a2], v[180:183], v[148:151], %[a2]\n\t" \
    "v_mfma_f32_16x16x32_bf16 %[a3], v[184:187], v[148:151], %[a3]\n\t" \
    "v_mfma_f32_16x16x32_bf16 %[a4], v[188:191], v[148:151], %[a4]\n\t" \
    "s_branch 50f\n\t" \
    "30:\n\t" \
    "s_waitcnt vmcnt(0)\n\t" \
    "s_sub_u32 %[cnt], %[cnt], 1\n\t" \
    "s_cbranch_scc1 40b\n\t" \
    "s_sleep %[pr]\n\t" \
    P3_LOAD0 \
    "s_mov_b32 %[ag], 0\n\t" \
    "v_max3_u32 v252, v96, v97, v98\n\t" \
    "v_max3_u32 v252, v252, v99, v100\n\t" \
    "v_max3_u32 v252, v252, v101, v102\n\t" \
    "v_max3_u32 v252, v252, v103, v104\n\t" \
    "v_max3_u32 v252, v252, v105, v106\n\t" \
    "v_max3_u32 v252, v252, v107, v108\n\t" \
    "v_max3_u32 v252, v252, v109, v110\n\t" \
    "v_max3_u32 v252, v252, v111, v144\n\t" \
    "v_max3_u32 v252, v252, v145, v146\n\t" \
    "v_max_u32 v252, v252, v147\n\t" \
    "v_cmp_eq_u32 vcc, -1, v252\n\t" \
    "s_cbranch_vccz 61f\n\t" \
    P3_LOAD1 \
    "s_add_u32 %[ag], %[ag], 8\n\t" \
    "61:\n\t" \
    "v_max3_u32 v252, v112, v113, v114\n\t" \
    "v_max3_u32 v252, v252, v115, v116\n\t" \
    "v_max3_u32 v252, v252, v117, v118\n\t" \
    "v_max3_u32 v252, v252, v119, v120\n\t" \
    "v_max3_u32 v252, v252, v121, v122\n\t" \
    "v_max3_u32 v252, v252, v123, v124\n\t" \
    "v_max3_u32 v252, v252, v125, v126\n\t" \
    "v_max_u32 v252, v252, v127\n\t" \
    "v_cmp_eq_u32 vcc, -1, v252\n\t" \
    "s_cbranch_vccz 62f\n\t" \
    P3_LOAD2 \
    "s_add_u32 %[ag], %[ag], 4\n\t" \
    "62:\n\t" \
    "v_max3_u32 v252, v128, v129, v130\n\t" \
    "v_max3_u32 v252, v252, v131, v132\n\t" \
    "v_max3_u32 v252, v252, v133, v134\n\t" \
    "v_max3_u32 v252, v252, v135, v136\n\t" \
    "v_max3_u32 v252, v252, v137, v138\n\t" \
    "v_max3_u32 v252, v252, v139, v140\n\t" \
    "v_max3_u32 v252, v252, v141, v142\n\t" \
    "v_max3_u32 v252, v252, v143, v148\n\t" \
    "v_max3_u32 v252, v252, v149, v150\n\t" \
    "v_max_u32 v252, v252, v151\n\t" \
    "v_cmp_eq_u32 vcc, -1, v252\n\t" \
    "s_cbranch_vccz 63f\n\t" \
    P3_LOAD3 \
    "s_add_u32 %[ag], %[ag], 8\n\t" \
    "63:\n\t" \
    "s_cmp_ge_u32 %[ag], 16\n\t" \
    "s_cbranch_scc0 84f\n\t" \
    "s_waitcnt vmcnt(16)\n\t" \
    "s_mov_b32 %[ag], 1\n\t" \
    "s_branch 20b\n\t" \
    "84:\n\t" \
    "s_cmp_ge_u32 %[ag], 12\n\t" \
    "s_cbranch_scc0 83f\n\t" \
    "s_waitcnt vmcnt(12)\n\t" \
    "s_mov_b32 %[ag], 1\n\t" \
    "s_branch 20b\n\t" \
    "83:\n\t" \
    "s_cmp_ge_u32 %[ag], 8\n\t" \
    "s_cbranch_scc0 82f\n\t" \
    "s_waitcnt vmcnt(8)\n\t" \
    "s_mov_b32 %[ag], 1\n\t" \
    "s_branch 20b\n\t" \
    "82:\n\t" \
    "s_cmp_ge_u32 %[ag], 4\n\t" \
    "s_cbranch_scc0 81f\n\t" \
    "s_waitcnt vmcnt(4)\n\t" \
    "s_mov_b32 %[ag], 1\n\t" \
    "s_branch 20b\n\t" \
    "81:\n\t" \
    "s_waitcnt vmcnt(0)\n\t" \
    "s_mov_b32 %[ag], 1\n\t" \
    "s_branch 20b\n\t" \
    "31:\n\t" \
    "s_waitcnt vmcnt(0)\n\t" \
    "s_sub_u32 %[cnt], %[cnt], 1\n\t" \
    "s_cbranch_scc1 41b\n\t" \
    "s_sleep %[pr]\n\t" \
    P3_LOAD1 \
    "s_mov_b32 %[ag], 0\n\t" \
    "v_max3_u32 v252, v112, v113, v114\n\t" \
    "v_max3_u32 v252, v252, v115, v116\n\t" \
    "v_max3_u32 v252, v252, v117, v118\n\t" \
    "v_max3_u32 v252, v252, v119, v120\n\t" \
    "v_max3_u32 v252, v252, v121, v122\n\t" \
    "v_max3_u32 v252, v252, v123, v124\n\t" \
    "v_max3_u32 v252, v252, v125, v126\n\t" \
    "v_max_u32 v252, v252, v127\n\t" \
    "v_cmp_eq_u32 vcc, -1, v252\n\t" \
    "s_cbranch_vccz 66f\n\t" \
    P3_LOAD2 \
    "s_add_u32 %[ag], %[ag], 4\n\t" \
    "66:\n\t" \
    "v_max3_u32 v252, v128, v129, v130\n\t" \
    "v_max3_u32 v252, v252, v131, v132\n\t" \
    "v_max3_u32 v252, v252, v133, v134\n\t" \
    "v_max3_u32 v252, v252, v135, v136\n\t" \
    "v_max3_u32 v252, v252, v137, v138\n\t" \
    "v_max3_u32 v252, v252, v139, v140\n\t" \
    "v_max3_u32 v252, v252, v141, v142\n\t" \
    "v_max3_u32 v252, v252, v143, v148\n\t" \
    "v_max3_u32 v252, v252, v149, v150\n\t" \
    "v_max_u32 v252, v252, v151\n\t" \
    "v_cmp_eq_u32 vcc, -1, v252\n\t" \
    "s_cbranch_vccz 67f\n\t" \
    P3_LOAD3 \
    "s_add_u32 %[ag], %[ag], 8\n\t" \
    "67:\n\t" \
    "s_cmp_ge_u32 %[ag], 12\n\t" \
    "s_cbranch_scc0 88f\n\t" \
    "s_waitcnt vmcnt(12)\n\t" \
    "s_mov_b32 %[ag], 1\n\t" \
    "s_branch 21b\n\t" \
    "88:\n\t" \
    "s_cmp_ge_u32 %[ag], 8\n\t" \
    "s_cbranch_scc0 87f\n\t" \
    "s_waitcnt vmcnt(8)\n\t" \
    "s_mov_b32 %[ag], 1\n\t" \
    "s_branch 21b\n\t" \
    "87:\n\t" \
    "s_cmp_ge_u32 %[ag], 4\n\t" \
    "s_cbranch_scc0 86f\n\t" \
    "s_waitcnt vmcnt(4)\n\t" \
    "s_mov_b32 %[ag], 1\n\t" \
    "s_branch 21b\n\t" \
    "86:\n\t" \
    "s_waitcnt vmcnt(0)\n\t" \
    "s_mov_b32 %[ag], 1\n\t" \
    "s_branch 21b\n\t" \
    "32:\n\t" \
    "s_waitcnt vmcnt(0)\n\t" \
    "s_sub_u32 %[cnt], %[cnt], 1\n\t" \
    "s_cbranch_scc1 42b\n\t" \
    "s_sleep %[pr]\n\t" \
    P3_LOAD2 \
    "s_mov_b32 %[ag], 0\n\t" \
    "v_max3_u32 v252, v128, v129, v130\n\t" \
    "v_max3_u32 v252, v252, v131, v132\n\t" \
    "v_max3_u32 v252, v252, v133, v134\n\t" \
    "v_max3_u32 v252, v252, v135, v136\n\t" \
    "v_max3_u32 v252, v252, v137, v138\n\t" \
    "v_max3_u32 v252, v252, v139, v140\n\t" \
    "v_max3_u32 v252, v252, v141, v142\n\t" \
    "v_max3_u32 v252, v252, v143, v148\n\t" \
    "v_max3_u32 v252, v252, v149, v150\n\t" \
    "v_max_u32 v252, v252, v151\n\t" \
    "v_cmp_eq_u32 vcc, -1, v252\n\t" \
    "s_cbranch_vccz 71f\n\t" \
    P3_LOAD3 \
    "s_add_u32 %[ag], %[ag], 8\n\t" \
    "71:\n\t" \
    "s_cmp_ge_u32 %[ag], 8\n\t" \
    "s_cbranch_scc0 92f\n\t" \
    "s_waitcnt vmcnt(8)\n\t" \
    "s_mov_b32 %[ag], 1\n\t" \
    "s_branch 22b\n\t" \
    "92:\n\t" \
    "s_cmp_ge_u32 %[ag], 4\n\t" \
    "s_cbranch_scc0 91f\n\t" \
    "s_waitcnt vmcnt(4)\n\t" \
    "s_mov_b32 %[ag], 1\n\t" \
    "s_branch 22b\n\t" \
    "91:\n\t" \
    "s_waitcnt vmcnt(0)\n\t" \
    "s_mov_b32 %[ag], 1\n\t" \
    "s_branch 22b\n\t" \
    "33:\n\t" \
    "s_waitcnt vmcnt(0)\n\t" \
    "s_sub_u32 %[cnt], %[cnt], 1\n\t" \
    "s_cbranch_scc1 43b\n\t" \
    "s_sleep %[pr]\n\t" \
    P3_LOAD3 \
    "s_mov_b32 %[ag], 0\n\t" \
    "s_waitcnt vmcnt(0)\n\t" \
    "s_mov_b32 %[ag], 1\n\t" \
    "s_branch 23b\n\t" \
    "50:\n\t" \
    "s_nop 7\n\t" \
    "s_nop 7\n\t" \
    "s_nop 7\n\t"
#define P3_CLOB "v80", "v81", "v82", "v83", "v84", "v85", "v86", "v87", "v88", "v89", "v90", "v91", "v92", "v93", "v94", "v95", "v96", "v97", "v98", "v99", "v100", "v101", "v102", "v103", "v104", "v105", "v106", "v107", "v108", "v109", "v110", "v111", "v112", "v113", "v114", "v115", "v116", "v117", "v118", "v119", "v120", "v121", "v122", "v123", "v124", "v125", "v126", "v127", "v128", "v129", "v130", "v131", "v132", "v133", "v134", "v135", "v136", "v137", "v138", "v139", "v140", "v141", "v142", "v143", "v144", "v145", "v146", "v147", "v148", "v149", "v150", "v151", "v152", "v153", "v154", "v155", "v156", "v157", "v158", "v159", "v160", "v161", "v162", "v163", "v164", "v165", "v166", "v167", "v168", "v169", "v170", "v171", "v172", "v173", "v174", "v175", "v176", "v177", "v178", "v179", "v180", "v181", "v182", "v183", "v184", "v185", "v186", "v187", "v188", "v189", "v190", "v191", "v192", "v193", "v194", "v195", "v196", "v197", "v198", "v199", "v200", "v201", "v202", "v203", "v204", "v205", "v206", "v207", "v208", "v209", "v210", "v211", "v212", "v213", "v214", "v215", "v216", "v217", "v218", "v219", "v220", "v221", "v222", "v223", "v224", "v225", "v226", "v227", "v228", "v229", "v230", "v231", "v232", "v233", "v234", "v235", "v236", "v237", "v238", "v239", "v240", "v241", "v242", "v243", "v244", "v245", "v246", "v247", "v248", "v249", "v250", "v251", "v252", "v253", "v254"
DEV void p3_wave(const Params& p, LAS char* wl, int qb, int ns, int w, int lane) {
  const int r = lane & 15, g = lane >> 4, q4 = (lane >> 2) & 3, p4 = lane & 3;
  const int qt16 = 4 * qb + w;
  f32x4 acc[5];
  const unsigned atl = (unsigned)(size_t)(wl + (18 * (4 * g + (q4 >> 1)) + 16 + (q4 & 1)) * WO_RS + p4 * 8);
  const unsigned f0 = (unsigned)(size_t)(wl + wo_frag(lane, 0)), f2 = (unsigned)(size_t)(wl + wo_frag(lane, 2));
  const u32x4 ro = mk_rs(p.ws + WS_OM, 128 * 16 * 1024), rt = mk_rs(p.ws + WS_OT, 32 * S * 4);
  const unsigned vo = (unsigned)(lane * 16), so = (unsigned)(qt16 * 16 * 1024), vt = (unsigned)((4 * g * S + 16 * qt16 + r) * 4);
  const unsigned am = (unsigned)(size_t)(wl + (144 * w + 18 * (g >> 1) + 4 * (g & 1) + q4) * WO_RS + p4 * 8);
  const unsigned w0 = f0 + 144 * w * WO_RS, w2 = f2 + 144 * w * WO_RS;
  unsigned t, ag, cnt;
  asm volatile(
    P3_ASM
    : [a0] "=&v"(acc[0]), [a1] "=&v"(acc[1]), [a2] "=&v"(acc[2]), [a3] "=&v"(acc[3]), [a4] "=&v"(acc[4]), [t] "=&s"(t), [ag] "=&s"(ag), [cnt] "=&s"(cnt)
    : [vo] "v"(vo), [ro] "s"(ro), [so] "s"(so), [vt] "v"(vt), [rt] "s"(rt), [f0] "v"(f0), [f2] "v"(f2), [atl] "v"(atl), [am] "v"(am), [w0] "v"(w0), [w2] "v"(w2),
      [spin] "n"(SPIN_MAX), [pr] "n"(PR), [pd] "n"(PD), [pe] "n"(PE)
    : "vcc", "scc", "memory", P3_CLOB);

  const __amdgpu_buffer_rsrc_t rout = __builtin_amdgcn_make_buffer_rsrc((void*)p.out, 0, S * 2 * D * 2, 0x00020000);
  const unsigned oo = (unsigned)(((16 * qt16 + r) * 1152 + ns * 72 + 4 * g) * 2);
#pragma unroll
  for (int nt = 0; nt < 5; nt++)
    __builtin_amdgcn_raw_buffer_store_b64((u32x2){pk_bf16(acc[nt][0], acc[nt][1]), pk_bf16(acc[nt][2], acc[nt][3])}, rout, (nt < 4 || g < 2) ? oo + nt * 32 : OOB, 0, OUTA);
}
struct B3Regs { u32x4 z[2][2], q[2][2]; };
DEV int b3_flag(int tb, int cb, int w4, int lane, bool two) { return F_Z + (tb >> 4) * D + 64 * cb + 16 * w4 + (lane & 15) + (two ? 64 * ((lane >> 4) & 1) : 0); }
template <int NT>
DEV void b3_load(B3Regs& R, const Params& p, int tb, int cb, int w4, int lane) {
  const int ch0 = 64 * cb + 16 * w4, tl = 8 * (lane >> 4) + 4 * ((lane >> 3) & 1) + (lane & 3), b = (lane >> 2) & 1;
  const __amdgpu_buffer_rsrc_t rz = __builtin_amdgcn_make_buffer_rsrc((void*)(p.ws + WS_ZT), 0, D * S * 2, 0x00020000);
  const __amdgpu_buffer_rsrc_t rq = __builtin_amdgcn_make_buffer_rsrc((void*)p.q, 0, S * D * 2, 0x00020000);
#pragma unroll
  for (int n = 0; n < NT; n++)
#pragma unroll
    for (int u = 0; u < 2; u++) R.z[n][u] = __builtin_amdgcn_raw_buffer_load_b128(rz, (unsigned)((ch0 + 64 * n + 8 * u + (lane >> 3)) * 4096 + tb * 128 + (lane & 7) * 16), 0, AUX_SC1);
#pragma unroll
  for (int n = 0; n < NT; n++)
#pragma unroll
    for (int u = 0; u < 2; u++) R.q[n][u] = __builtin_amdgcn_raw_buffer_load_b128(rq, (unsigned)(((64 * tb + 32 * u + tl) * D + ch0 + 64 * n + 8 * b) * 2), 0, 0);
}
template <int NT>
DEV void b3_gate(const B3Regs& R, const Params& p, LAS char* zl, int tb, int cb, int w4, int lane) {
  const int ch0 = 64 * cb + 16 * w4, tl = 8 * (lane >> 4) + 4 * ((lane >> 3) & 1) + (lane & 3), b = (lane >> 2) & 1;
  const int G = lane >> 4, q = (lane >> 2) & 3, pp = lane & 3;
#pragma unroll
  for (int n = 0; n < NT; n++)
#pragma unroll
    for (int u = 0; u < 2; u++) {
      const int ch = 8 * u + (lane >> 3);
      *(LAS u32x4*)(zl + n * 8192 + ch * 128 + (((lane & 7) ^ ((ch & 2) | (u << 2))) << 4)) = R.z[n][u];
    }
  WAIT_LGKM0();
  const __amdgpu_buffer_rsrc_t rout = __builtin_amdgcn_make_buffer_rsrc((void*)p.out, 0, S * 2 * D * 2, 0x00020000);
#pragma unroll
  for (int n = 0; n < NT; n++)
#pragma unroll
    for (int u = 0; u < 2; u++) {
      u32x4 o;
#pragma unroll
      for (int k = 0; k < 2; k++) {
        const int ch = 8 * (pp & 1) + 4 * k + q;
        const u32x2 z = __builtin_bit_cast(u32x2, trr(zl + n * 8192 + ch * 128 + (((4 * u + G) ^ ((ch & 2) | ((pp & 1) << 2))) << 4) + (pp >> 1) * 8));
#pragma unroll
        for (int e = 0; e < 2; e++) {
          const f32x2 qq = unpk(R.q[n][u][2 * k + e]), zz = unpk(z[e]);
          o[2 * k + e] = pk_bf16(qq.x * zz.x, qq.y * zz.y);
        }
      }
      __builtin_amdgcn_raw_buffer_store_b128(o, rout, (unsigned)(((64 * tb + 32 * u + tl) * 1152 + 576 + ch0 + 64 * n + 8 * b) * 2), 0, OUTB);
    }
}

constexpr int h_younger(int target, int stop) {
  int code[64] = {}, n = 0;
  for (int s = 0; s < PRO; s++) code[n++] = 100 + s;
  if (RSL < 0) code[n++] = 3;
  if (PFS < 0) code[n++] = 8;
  for (int i = 0; i < 9; i++) {
    code[n++] = 200 + i;
    code[n++] = 300 + i;
    if (i == KF) code[n++] = 4;
    if (i == SPL) code[n++] = 2;
    if (i == 0) { for (int s = PRO; s < 3 + S03; s++) code[n++] = 100 + s; }
    else if (i == 1) { if (!S03) code[n++] = 103; code[n++] = 104; }
    else if (i <= 5) code[n++] = 103 + i;
    if (i == RSL) code[n++] = 3;
    if (i == PFS) code[n++] = PF8 ? 8 : 4;
    if (i == HK) code[n++] = 5;
  }
  code[n++] = 9; code[n++] = 400;
  int cnt = -1;
  for (int j = 0; j < n && code[j] != stop; j++) {
    const int c = code[j];
    if (cnt >= 0) cnt += c >= 200 ? 0 : (c >= 100 ? 8 : (c == 4 || c == 7 ? 1 : (c == 5 ? 6 : (c == 8 || c == 2 ? 2 : (c == 9 ? 8 : 4)))));
    if (c == target) cnt = 0;
  }
  return cnt;
}
constexpr int h_min(int a, int b) { return a < b ? a : b; }
constexpr int h_allow(int k) {
  return k == SPL - 1 + RAWL ? h_min(h_younger(100 + k, 200 + k), h_younger(3, 200 + k)) : (k == KF ? h_min(h_younger(100 + k, 200 + k), h_younger(2, 200 + k)) : h_younger(100 + k, 200 + k));
}
static_assert(PRO >= 2 && PRO <= 3 && PFS <= 5 && (PFS >= 0 || PF8) && SPL >= 3 && RW > SPL && RW <= 8 && KF > SPL && KF <= 8 && RSL < SPL - 1 && HK <= 7, "schedule");
static_assert(h_allow(0) >= 0 && h_allow(8) >= 0 && h_allow(8) < 64 && h_allow(3) < 64 && h_younger(5, 400) >= 0, "schedule");
#define P1_KVFLAG "s_waitcnt vmcnt(0)\n\ts_mov_b64 exec, 3\n\tglobal_store_dwordx2 %[fa], %[tg], off sc1\n\ts_mov_b64 exec, -1\n\t"
#define P1_RAW(k) P1_RAW_##k
#define P1_RAW_1
#define P1_RAW_2
#define P1_RAW_3
#define P1_RAW_4
#define P1_RAW_5
#define P1_RAW_6
#define P1_RAW_7
#define P1_RAW_8 P1_KVFLAG
#define P1_ASM_A \
    "v_add_u32 v192, 0x10000, %[xa0]\n\t" \
    "v_add_u32 v193, 0x10000, %[xa1]\n\t" \
    "v_add_u32 v194, 0x10000, %[wb0]\n\t" \
    "v_add_u32 v195, 0x10000, %[wb1]\n\t" \
    "v_add_u32 v196, 0x10000, %[wb2]\n\t" \
    "v_add_u32 v197, 0x10000, %[wb3]\n\t" \
    "ds_read_b128 v[128:131], v192 offset:0\n\t" \
    "ds_read_b64_tr_b16 v[144:145], v194 offset:0\n\t" \
    "ds_read_b64_tr_b16 v[146:147], v194 offset:1024\n\t" \
    "ds_read_b64_tr_b16 v[148:149], v195 offset:0\n\t" \
    "ds_read_b64_tr_b16 v[150:151], v195 offset:1024\n\t" \
    "ds_read_b128 v[132:135], v192 offset:512\n\t" \
    "ds_read_b64_tr_b16 v[152:153], v196 offset:0\n\t" \
    "ds_read_b64_tr_b16 v[154:155], v196 offset:1024\n\t" \
    "ds_read_b64_tr_b16 v[156:157], v197 offset:0\n\t" \
    "ds_read_b64_tr_b16 v[158:159], v197 offset:1024\n\t" \
    "ds_read_b128 v[136:139], v192 offset:4096\n\t" \
    "ds_read_b128 v[140:143], v192 offset:4608\n\t" \
    "s_waitcnt lgkmcnt(9)\n\t" \
    "v_mfma_f32_16x16x32_bf16 %[c00], v[128:131], v[144:147], 0\n\t" \
    "ds_read_b128 v[160:163], v193 offset:0\n\t" \
    "ds_read_b64_tr_b16 v[176:177], v194 offset:8192\n\t" \
    "s_waitcnt lgkmcnt(9)\n\t" \
    "v_mfma_f32_16x16x32_bf16 %[c01], v[128:131], v[148:151], 0\n\t" \
    "ds_read_b64_tr_b16 v[178:179], v194 offset:9216\n\t" \
    "ds_read_b64_tr_b16 v[180:181], v195 offset:8192\n\t" \
    "s_waitcnt lgkmcnt(10)\n\t" \
    "v_mfma_f32_16x16x32_bf16 %[c10], v[132:135], v[144:147], 0\n\t" \
    "ds_read_b64_tr_b16 v[182:183], v195 offset:9216\n\t" \
    "ds_read_b128 v[164:167], v193 offset:512\n\t" \
    "v_mfma_f32_16x16x32_bf16 %[c11], v[132:135], v[148:151], 0\n\t" \
    "ds_read_b64_tr_b16 v[184:185], v196 offset:8192\n\t" \
    "ds_read_b64_tr_b16 v[186:187], v196 offset:9216\n\t" \
    "s_waitcnt lgkmcnt(12)\n\t" \
    "v_mfma_f32_16x16x32_bf16 %[c02], v[128:131], v[152:155], 0\n\t" \
    "ds_read_b64_tr_b16 v[188:189], v197 offset:8192\n\t" \
    "ds_read_b64_tr_b16 v[190:191], v197 offset:9216\n\t" \
    "v_mfma_f32_16x16x32_bf16 %[c12], v[132:135], v[152:155], 0\n\t" \
    "ds_read_b128 v[168:171], v193 offset:4096\n\t" \
    "ds_read_b128 v[172:175], v193 offset:4608\n\t" \
    "s_waitcnt lgkmcnt(14)\n\t" \
    "v_mfma_f32_16x16x32_bf16 %[c03], v[128:131], v[156:159], 0\n\t" \
    "v_mfma_f32_16x16x32_bf16 %[c13], v[132:135], v[156:159], 0\n\t" \
    "s_waitcnt lgkmcnt(13)\n\t" \
    "v_mfma_f32_16x16x32_bf16 %[c20], v[136:139], v[144:147], 0\n\t" \
    "v_mfma_f32_16x16x32_bf16 %[c21], v[136:139], v[148:151], 0\n\t" \
    "v_mfma_f32_16x16x32_bf16 %[c22], v[136:139], v[152:155], 0\n\t" \
    "v_mfma_f32_16x16x32_bf16 %[c23], v[136:139], v[156:159], 0\n\t" \
    "s_waitcnt lgkmcnt(12)\n\t" \
    "v_mfma_f32_16x16x32_bf16 %[c30], v[140:143], v[144:147], 0\n\t" \
    "v_mfma_f32_16x16x32_bf16 %[c31], v[140:143], v[148:151], 0\n\t" \
    "v_mfma_f32_16x16x32_bf16 %[c32], v[140:143], v[152:155], 0\n\t" \
    "v_mfma_f32_16x16x32_bf16 %[c33], v[140:143], v[156:159], 0\n\t" \
    "s_waitcnt lgkmcnt(0)\n\t" \
    P1_RAW(1) \
    "s_barrier\n\t" \
    "v_mfma_f32_16x16x32_bf16 %[c00], v[160:163], v[176:179], %[c00]\n\t" \
    "ds_read_b128 v[128:131], %[xa0] offset:0\n\t" \
    "v_mfma_f32_16x16x32_bf16 %[c01], v[160:163], v[180:183], %[c01]\n\t" \
    "ds_read_b64_tr_b16 v[144:145], %[wb0] offset:0\n\t" \
    "v_mfma_f32_16x16x32_bf16 %[c10], v[164:167], v[176:179], %[c10]\n\t" \
    "ds_read_b64_tr_b16 v[146:147], %[wb0] offset:1024\n\t" \
    "v_mfma_f32_16x16x32_bf16 %[c11], v[164:167], v[180:183], %[c11]\n\t" \
    "ds_read_b64_tr_b16 v[148:149], %[wb1] offset:0\n\t" \
    "v_mfma_f32_16x16x32_bf16 %[c02], v[160:163], v[184:187], %[c02]\n\t" \
    "ds_read_b64_tr_b16 v[150:151], %[wb1] offset:1024\n\t" \
    "v_mfma_f32_16x16x32_bf16 %[c12], v[164:167], v[184:187], %[c12]\n\t" \
    "ds_read_b128 v[132:135], %[xa0] offset:512\n\t" \
    "v_mfma_f32_16x16x32_bf16 %[c03], v[160:163], v[188:191], %[c03]\n\t" \
    "ds_read_b64_tr_b16 v[152:153], %[wb2] offset:0\n\t" \
    "v_mfma_f32_16x16x32_bf16 %[c13], v[164:167], v[188:191], %[c13]\n\t" \
    "ds_read_b64_tr_b16 v[154:155], %[wb2] offset:1024\n\t" \
    "v_mfma_f32_16x16x32_bf16 %[c20], v[168:171], v[176:179], %[c20]\n\t" \
    "ds_read_b64_tr_b16 v[156:157], %[wb3] offset:0\n\t" \
    "v_mfma_f32_16x16x32_bf16 %[c21], v[168:171], v[180:183], %[c21]\n\t" \
    "ds_read_b64_tr_b16 v[158:159], %[wb3] offset:1024\n\t" \
    "v_mfma_f32_16x16x32_bf16 %[c22], v[168:171], v[184:187], %[c22]\n\t" \
    "ds_read_b128 v[136:139], %[xa0] offset:4096\n\t" \
    "v_mfma_f32_16x16x32_bf16 %[c23], v[168:171], v[188:191], %[c23]\n\t" \
    "ds_read_b128 v[140:143], %[xa0] offset:4608\n\t" \
    "v_mfma_f32_16x16x32_bf16 %[c30], v[172:175], v[176:179], %[c30]\n\t" \
    "v_mfma_f32_16x16x32_bf16 %[c31], v[172:175], v[180:183], %[c31]\n\t" \
    "v_mfma_f32_16x16x32_bf16 %[c32], v[172:175], v[184:187], %[c32]\n\t" \
    "v_mfma_f32_16x16x32_bf16 %[c33], v[172:175], v[188:191], %[c33]\n\t" \
    "s_waitcnt lgkmcnt(9)\n\t" \
    "v_mfma_f32_16x16x32_bf16 %[c00], v[128:131], v[144:147], %[c00]\n\t" \
    "ds_read_b128 v[160:163], %[xa1] offset:0\n\t" \
    "ds_read_b64_tr_b16 v[176:177], %[wb0] offset:8192\n\t" \
    "s_waitcnt lgkmcnt(9)\n\t" \
    "v_mfma_f32_16x16x32_bf16 %[c01], v[128:131], v[148:151], %[c01]\n\t" \
    "ds_read_b64_tr_b16 v[178:179], %[wb0] offset:9216\n\t" \
    "ds_read_b64_tr_b16 v[180:181], %[wb1] offset:8192\n\t" \
    "s_waitcnt lgkmcnt(10)\n\t" \
    "v_mfma_f32_16x16x32_bf16 %[c10], v[132:135], v[144:147], %[c10]\n\t" \
    "ds_read_b64_tr_b16 v[182:183], %[wb1] offset:9216\n\t" \
    "ds_read_b128 v[164:167], %[xa1] offset:512\n\t" \
    "v_mfma_f32_16x16x32_bf16 %[c11], v[132:135], v[148:151], %[c11]\n\t" \
    "ds_read_b64_tr_b16 v[184:185], %[wb2] offset:8192\n\t" \
    "ds_read_b64_tr_b16 v[186:187], %[wb2] offset:9216\n\t" \
    "s_waitcnt lgkmcnt(12)\n\t" \
    "v_mfma_f32_16x16x32_bf16 %[c02], v[128:131], v[152:155], %[c02]\n\t" \
    "ds_read_b64_tr_b16 v[188:189], %[wb3] offset:8192\n\t" \
    "ds_read_b64_tr_b16 v[190:191], %[wb3] offset:9216\n\t" \
    "v_mfma_f32_16x16x32_bf16 %[c12], v[132:135], v[152:155], %[c12]\n\t" \
    "ds_read_b128 v[168:171], %[xa1] offset:4096\n\t" \
    "ds_read_b128 v[172:175], %[xa1] offset:4608\n\t" \
    "s_waitcnt lgkmcnt(14)\n\t" \
    "v_mfma_f32_16x16x32_bf16 %[c03], v[128:131], v[156:159], %[c03]\n\t" \
    "v_mfma_f32_16x16x32_bf16 %[c13], v[132:135], v[156:159], %[c13]\n\t" \
    "s_waitcnt lgkmcnt(13)\n\t" \
    "v_mfma_f32_16x16x32_bf16 %[c20], v[136:139], v[144:147], %[c20]\n\t" \
    "v_mfma_f32_16x16x32_bf16 %[c21], v[136:139], v[148:151], %[c21]\n\t" \
    "v_mfma_f32_16x16x32_bf16 %[c22], v[136:139], v[152:155], %[c22]\n\t" \
    "v_mfma_f32_16x16x32_bf16 %[c23], v[136:139], v[156:159], %[c23]\n\t" \
    "s_waitcnt lgkmcnt(12)\n\t" \
    "v_mfma_f32_16x16x32_bf16 %[c30], v[140:143], v[144:147], %[c30]\n\t" \
    "v_mfma_f32_16x16x32_bf16 %[c31], v[140:143], v[148:151], %[c31]\n\t" \
    "v_mfma_f32_16x16x32_bf16 %[c32], v[140:143], v[152:155], %[c32]\n\t" \
    "v_mfma_f32_16x16x32_bf16 %[c33], v[140:143], v[156:159], %[c33]\n\t" \
    "s_waitcnt lgkmcnt(0)\n\t" \
    P1_RAW(2) \
    "s_barrier\n\t" \
    "v_mfma_f32_16x16x32_bf16 %[c00], v[160:163], v[176:179], %[c00]\n\t" \
    "ds_read_b128 v[128:131], %[xa0] offset:32768\n\t" \
    "v_mfma_f32_16x16x32_bf16 %[c01], v[160:163], v[180:183], %[c01]\n\t" \
    "ds_read_b64_tr_b16 v[144:145], %[wb0] offset:32768\n\t" \
    "v_mfma_f32_16x16x32_bf16 %[c10], v[164:167], v[176:179], %[c10]\n\t" \
    "ds_read_b64_tr_b16 v[146:147], %[wb0] offset:33792\n\t" \
    "v_mfma_f32_16x16x32_bf16 %[c11], v[164:167], v[180:183], %[c11]\n\t" \
    "ds_read_b64_tr_b16 v[148:149], %[wb1] offset:32768\n\t" \
    "v_mfma_f32_16x16x32_bf16 %[c02], v[160:163], v[184:187], %[c02]\n\t" \
    "ds_read_b64_tr_b16 v[150:151], %[wb1] offset:33792\n\t" \
    "v_mfma_f32_16x16x32_bf16 %[c12], v[164:167], v[184:187], %[c12]\n\t" \
    "ds_read_b128 v[132:135], %[xa0] offset:33280\n\t" \
    "v_mfma_f32_16x16x32_bf16 %[c03], v[160:163], v[188:191], %[c03]\n\t" \
    "ds_read_b64_tr_b16 v[152:153], %[wb2] offset:32768\n\t" \
    "v_mfma_f32_16x16x32_bf16 %[c13], v[164:167], v[188:191], %[c13]\n\t" \
    "ds_read_b64_tr_b16 v[154:155], %[wb2] offset:33792\n\t" \
    "v_mfma_f32_16x16x32_bf16 %[c20], v[168:171], v[176:179], %[c20]\n\t" \
    "ds_read_b64_tr_b16 v[156:157], %[wb3] offset:32768\n\t" \
    "v_mfma_f32_16x16x32_bf16 %[c21], v[168:171], v[180:183], %[c21]\n\t" \
    "ds_read_b64_tr_b16 v[158:159], %[wb3] offset:33792\n\t" \
    "v_mfma_f32_16x16x32_bf16 %[c22], v[168:171], v[184:187], %[c22]\n\t" \
    "ds_read_b128 v[136:139], %[xa0] offset:36864\n\t" \
    "v_mfma_f32_16x16x32_bf16 %[c23], v[168:171], v[188:191], %[c23]\n\t" \
    "ds_read_b128 v[140:143], %[xa0] offset:37376\n\t" \
    "v_mfma_f32_16x16x32_bf16 %[c30], v[172:175], v[176:179], %[c30]\n\t" \
    "v_mfma_f32_16x16x32_bf16 %[c31], v[172:175], v[180:183], %[c31]\n\t" \
    "v_mfma_f32_16x16x32_bf16 %[c32], v[172:175], v[184:187], %[c32]\n\t" \
    "v_mfma_f32_16x16x32_bf16 %[c33], v[172:175], v[188:191], %[c33]\n\t" \
    "s_waitcnt lgkmcnt(9)\n\t" \
    "v_mfma_f32_16x16x32_bf16 %[c00], v[128:131], v[144:147], %[c00]\n\t" \
    "ds_read_b128 v[160:163], %[xa1] offset:32768\n\t" \
    "ds_read_b64_tr_b16 v[176:177], %[wb0] offset:40960\n\t" \
    "s_waitcnt lgkmcnt(9)\n\t" \
    "v_mfma_f32_16x16x32_bf16 %[c01], v[128:131], v[148:151], %[c01]\n\t" \
    "ds_read_b64_tr_b16 v[178:179], %[wb0] offset:41984\n\t" \
    "ds_read_b64_tr_b16 v[180:181], %[wb1] offset:40960\n\t" \
    "s_waitcnt lgkmcnt(10)\n\t" \
    "v_mfma_f32_16x16x32_bf16 %[c10], v[132:135], v[144:147], %[c10]\n\t" \
    "ds_read_b64_tr_b16 v[182:183], %[wb1] offset:41984\n\t" \
    "ds_read_b128 v[164:167], %[xa1] offset:33280\n\t" \
    "v_mfma_f32_16x16x32_bf16 %[c11], v[132:135], v[148:151], %[c11]\n\t" \
    "ds_read_b64_tr_b16 v[184:185], %[wb2] offset:40960\n\t" \
    "ds_read_b64_tr_b16 v[186:187], %[wb2] offset:41984\n\t" \
    "s_waitcnt lgkmcnt(12)\n\t" \
    "v_mfma_f32_16x16x32_bf16 %[c02], v[128:131], v[152:155], %[c02]\n\t" \
    "ds_read_b64_tr_b16 v[188:189], %[wb3] offset:40960\n\t" \
    "ds_read_b64_tr_b16 v[190:191], %[wb3] offset:41984\n\t" \
    "v_mfma_f32_16x16x32_bf16 %[c12], v[132:135], v[152:155], %[c12]\n\t" \
    "ds_read_b128 v[168:171], %[xa1] offset:36864\n\t" \
    "ds_read_b128 v[172:175], %[xa1] offset:37376\n\t" \
    "s_waitcnt lgkmcnt(14)\n\t" \
    "v_mfma_f32_16x16x32_bf16 %[c03], v[128:131], v[156:159], %[c03]\n\t" \
    "v_mfma_f32_16x16x32_bf16 %[c13], v[132:135], v[156:159], %[c13]\n\t" \
    "s_waitcnt lgkmcnt(13)\n\t" \
    "v_mfma_f32_16x16x32_bf16 %[c20], v[136:139], v[144:147], %[c20]\n\t" \
    "v_mfma_f32_16x16x32_bf16 %[c21], v[136:139], v[148:151], %[c21]\n\t" \
    "v_mfma_f32_16x16x32_bf16 %[c22], v[136:139], v[152:155], %[c22]\n\t" \
    "v_mfma_f32_16x16x32_bf16 %[c23], v[136:139], v[156:159], %[c23]\n\t" \
    "s_waitcnt lgkmcnt(12)\n\t" \
    "v_mfma_f32_16x16x32_bf16 %[c30], v[140:143], v[144:147], %[c30]\n\t" \
    "v_mfma_f32_16x16x32_bf16 %[c31], v[140:143], v[148:151], %[c31]\n\t" \
    "v_mfma_f32_16x16x32_bf16 %[c32], v[140:143], v[152:155], %[c32]\n\t" \
    "v_mfma_f32_16x16x32_bf16 %[c33], v[140:143], v[156:159], %[c33]\n\t" \
    "s_waitcnt lgkmcnt(0)\n\t" \
    P1_RAW(3) \
    "s_barrier\n\t" \
    "v_mfma_f32_16x16x32_bf16 %[c00], v[160:163], v[176:179], %[c00]\n\t" \
    "ds_read_b128 v[128:131], v192 offset:32768\n\t" \
    "v_mfma_f32_16x16x32_bf16 %[c01], v[160:163], v[180:183], %[c01]\n\t" \
    "ds_read_b64_tr_b16 v[144:145], v194 offset:32768\n\t" \
    "v_mfma_f32_16x16x32_bf16 %[c10], v[164:167], v[176:179], %[c10]\n\t" \
    "ds_read_b64_tr_b16 v[146:147], v194 offset:33792\n\t" \
    "v_mfma_f32_16x16x32_bf16 %[c11], v[164:167], v[180:183], %[c11]\n\t" \
    "ds_read_b64_tr_b16 v[148:149], v195 offset:32768\n\t" \
    "v_mfma_f32_16x16x32_bf16 %[c02], v[160:163], v[184:187], %[c02]\n\t" \
    "ds_read_b64_tr_b16 v[150:151], v195 offset:33792\n\t" \
    "v_mfma_f32_16x16x32_bf16 %[c12], v[164:167], v[184:187], %[c12]\n\t" \
    "ds_read_b128 v[132:135], v192 offset:33280\n\t" \
    "v_mfma_f32_16x16x32_bf16 %[c03], v[160:163], v[188:191], %[c03]\n\t" \
    "ds_read_b64_tr_b16 v[152:153], v196 offset:32768\n\t" \
    "v_mfma_f32_16x16x32_bf16 %[c13], v[164:167], v[188:191], %[c13]\n\t" \
    "ds_read_b64_tr_b16 v[154:155], v196 offset:33792\n\t" \
    "v_mfma_f32_16x16x32_bf16 %[c20], v[168:171], v[176:179], %[c20]\n\t" \
    "ds_read_b64_tr_b16 v[156:157], v197 offset:32768\n\t" \
    "v_mfma_f32_16x16x32_bf16 %[c21], v[168:171], v[180:183], %[c21]\n\t" \
    "ds_read_b64_tr_b16 v[158:159], v197 offset:33792\n\t" \
    "v_mfma_f32_16x16x32_bf16 %[c22], v[168:171], v[184:187], %[c22]\n\t" \
    "ds_read_b128 v[136:139], v192 offset:36864\n\t" \
    "v_mfma_f32_16x16x32_bf16 %[c23], v[168:171], v[188:191], %[c23]\n\t" \
    "ds_read_b128 v[140:143], v192 offset:37376\n\t" \
    "v_mfma_f32_16x16x32_bf16 %[c30], v[172:175], v[176:179], %[c30]\n\t" \
    "v_mfma_f32_16x16x32_bf16 %[c31], v[172:175], v[180:183], %[c31]\n\t" \
    "v_mfma_f32_16x16x32_bf16 %[c32], v[172:175], v[184:187], %[c32]\n\t" \
    "v_mfma_f32_16x16x32_bf16 %[c33], v[172:175], v[188:191], %[c33]\n\t" \
    "s_waitcnt lgkmcnt(9)\n\t" \
    "v_mfma_f32_16x16x32_bf16 %[c00], v[128:131], v[144:147], %[c00]\n\t" \
    "ds_read_b128 v[160:163], v193 offset:32768\n\t" \
    "ds_read_b64_tr_b16 v[176:177], v194 offset:40960\n\t" \
    "s_waitcnt lgkmcnt(9)\n\t" \
    "v_mfma_f32_16x16x32_bf16 %[c01], v[128:131], v[148:151], %[c01]\n\t" \
    "ds_read_b64_tr_b16 v[178:179], v194 offset:41984\n\t" \
    "ds_read_b64_tr_b16 v[180:181], v195 offset:40960\n\t" \
    "s_waitcnt lgkmcnt(10)\n\t" \
    "v_mfma_f32_16x16x32_bf16 %[c10], v[132:135], v[144:147], %[c10]\n\t" \
    "ds_read_b64_tr_b16 v[182:183], v195 offset:41984\n\t" \
    "ds_read_b128 v[164:167], v193 offset:33280\n\t" \
    "v_mfma_f32_16x16x32_bf16 %[c11], v[132:135], v[148:151], %[c11]\n\t" \
    "ds_read_b64_tr_b16 v[184:185], v196 offset:40960\n\t" \
    "ds_read_b64_tr_b16 v[186:187], v196 offset:41984\n\t" \
    "s_waitcnt lgkmcnt(12)\n\t" \
    "v_mfma_f32_16x16x32_bf16 %[c02], v[128:131], v[152:155], %[c02]\n\t" \
    "ds_read_b64_tr_b16 v[188:189], v197 offset:40960\n\t" \
    "ds_read_b64_tr_b16 v[190:191], v197 offset:41984\n\t" \
    "v_mfma_f32_16x16x32_bf16 %[c12], v[132:135], v[152:155], %[c12]\n\t" \
    "ds_read_b128 v[168:171], v193 offset:36864\n\t" \
    "ds_read_b128 v[172:175], v193 offset:37376\n\t" \
    "s_waitcnt lgkmcnt(14)\n\t" \
    "v_mfma_f32_16x16x32_bf16 %[c03], v[128:131], v[156:159], %[c03]\n\t" \
    "v_mfma_f32_16x16x32_bf16 %[c13], v[132:135], v[156:159], %[c13]\n\t" \
    "s_waitcnt lgkmcnt(13)\n\t" \
    "v_mfma_f32_16x16x32_bf16 %[c20], v[136:139], v[144:147], %[c20]\n\t" \
    "v_mfma_f32_16x16x32_bf16 %[c21], v[136:139], v[148:151], %[c21]\n\t" \
    "v_mfma_f32_16x16x32_bf16 %[c22], v[136:139], v[152:155], %[c22]\n\t" \
    "v_mfma_f32_16x16x32_bf16 %[c23], v[136:139], v[156:159], %[c23]\n\t" \
    "s_waitcnt lgkmcnt(12)\n\t" \
    "v_mfma_f32_16x16x32_bf16 %[c30], v[140:143], v[144:147], %[c30]\n\t" \
    "v_mfma_f32_16x16x32_bf16 %[c31], v[140:143], v[148:151], %[c31]\n\t" \
    "v_mfma_f32_16x16x32_bf16 %[c32], v[140:143], v[152:155], %[c32]\n\t" \
    "v_mfma_f32_16x16x32_bf16 %[c33], v[140:143], v[156:159], %[c33]\n\t" \
    "s_waitcnt lgkmcnt(0)\n\t" \
    P1_RAW(4) \
    "s_barrier\n\t" \
    "v_mfma_f32_16x16x32_bf16 %[c00], v[160:163], v[176:179], %[c00]\n\t" \
    "ds_read_b128 v[128:131], v192 offset:0\n\t" \
    "v_mfma_f32_16x16x32_bf16 %[c01], v[160:163], v[180:183], %[c01]\n\t" \
    "ds_read_b64_tr_b16 v[144:145], v194 offset:0\n\t" \
    "v_mfma_f32_16x16x32_bf16 %[c10], v[164:167], v[176:179], %[c10]\n\t" \
    "ds_read_b64_tr_b16 v[146:147], v194 offset:1024\n\t" \
    "v_mfma_f32_16x16x32_bf16 %[c11], v[164:167], v[180:183], %[c11]\n\t" \
    "ds_read_b64_tr_b16 v[148:149], v195 offset:0\n\t" \
    "v_mfma_f32_16x16x32_bf16 %[c02], v[160:163], v[184:187], %[c02]\n\t" \
    "ds_read_b64_tr_b16 v[150:151], v195 offset:1024\n\t" \
    "v_mfma_f32_16x16x32_bf16 %[c12], v[164:167], v[184:187], %[c12]\n\t" \
    "ds_read_b128 v[132:135], v192 offset:512\n\t" \
    "v_mfma_f32_16x16x32_bf16 %[c03], v[160:163], v[188:191], %[c03]\n\t" \
    "ds_read_b64_tr_b16 v[152:153], v196 offset:0\n\t" \
    "v_mfma_f32_16x16x32_bf16 %[c13], v[164:167], v[188:191], %[c13]\n\t" \
    "ds_read_b64_tr_b16 v[154:155], v196 offset:1024\n\t" \
    "v_mfma_f32_16x16x32_bf16 %[c20], v[168:171], v[176:179], %[c20]\n\t" \
    "ds_read_b64_tr_b16 v[156:157], v197 offset:0\n\t" \
    "v_mfma_f32_16x16x32_bf16 %[c21], v[168:171], v[180:183], %[c21]\n\t" \
    "ds_read_b64_tr_b16 v[158:159], v197 offset:1024\n\t" \
    "v_mfma_f32_16x16x32_bf16 %[c22], v[168:171], v[184:187], %[c22]\n\t" \
    "ds_read_b128 v[136:139], v192 offset:4096\n\t" \
    "v_mfma_f32_16x16x32_bf16 %[c23], v[168:171], v[188:191], %[c23]\n\t" \
    "ds_read_b128 v[140:143], v192 offset:4608\n\t" \
    "v_mfma_f32_16x16x32_bf16 %[c30], v[172:175], v[176:179], %[c30]\n\t" \
    "v_mfma_f32_16x16x32_bf16 %[c31], v[172:175], v[180:183], %[c31]\n\t" \
    "v_mfma_f32_16x16x32_bf16 %[c32], v[172:175], v[184:187], %[c32]\n\t" \
    "v_mfma_f32_16x16x32_bf16 %[c33], v[172:175], v[188:191], %[c33]\n\t" \
    "s_waitcnt lgkmcnt(9)\n\t" \
    "v_mfma_f32_16x16x32_bf16 %[c00], v[128:131], v[144:147], %[c00]\n\t" \
    "ds_read_b128 v[160:163], v193 offset:0\n\t" \
    "ds_read_b64_tr_b16 v[176:177], v194 offset:8192\n\t" \
    "s_waitcnt lgkmcnt(9)\n\t" \
    "v_mfma_f32_16x16x32_bf16 %[c01], v[128:131], v[148:151], %[c01]\n\t" \
    "ds_read_b64_tr_b16 v[178:179], v194 offset:9216\n\t" \
    "ds_read_b64_tr_b16 v[180:181], v195 offset:8192\n\t" \
    "s_waitcnt lgkmcnt(10)\n\t" \
    "v_mfma_f32_16x16x32_bf16 %[c10], v[132:135], v[144:147], %[c10]\n\t" \
    "ds_read_b64_tr_b16 v[182:183], v195 offset:9216\n\t" \
    "ds_read_b128 v[164:167], v193 offset:512\n\t" \
    "v_mfma_f32_16x16x32_bf16 %[c11], v[132:135], v[148:151], %[c11]\n\t" \
    "ds_read_b64_tr_b16 v[184:185], v196 offset:8192\n\t" \
    "ds_read_b64_tr_b16 v[186:187], v196 offset:9216\n\t" \
    "s_waitcnt lgkmcnt(12)\n\t" \
    "v_mfma_f32_16x16x32_bf16 %[c02], v[128:131], v[152:155], %[c02]\n\t" \
    "ds_read_b64_tr_b16 v[188:189], v197 offset:8192\n\t" \
    "ds_read_b64_tr_b16 v[190:191], v197 offset:9216\n\t" \
    "v_mfma_f32_16x16x32_bf16 %[c12], v[132:135], v[152:155], %[c12]\n\t" \
    "ds_read_b128 v[168:171], v193 offset:4096\n\t" \
    "ds_read_b128 v[172:175], v193 offset:4608\n\t" \
    "s_waitcnt lgkmcnt(14)\n\t" \
    "v_mfma_f32_16x16x32_bf16 %[c03], v[128:131], v[156:159], %[c03]\n\t" \
    "v_mfma_f32_16x16x32_bf16 %[c13], v[132:135], v[156:159], %[c13]\n\t" \
    "s_waitcnt lgkmcnt(13)\n\t" \
    "v_mfma_f32_16x16x32_bf16 %[c20], v[136:139], v[144:147], %[c20]\n\t" \
    "v_mfma_f32_16x16x32_bf16 %[c21], v[136:139], v[148:151], %[c21]\n\t" \
    "v_mfma_f32_16x16x32_bf16 %[c22], v[136:139], v[152:155], %[c22]\n\t" \
    "v_mfma_f32_16x16x32_bf16 %[c23], v[136:139], v[156:159], %[c23]\n\t" \
    "s_waitcnt lgkmcnt(12)\n\t" \
    "v_mfma_f32_16x16x32_bf16 %[c30], v[140:143], v[144:147], %[c30]\n\t" \
    "v_mfma_f32_16x16x32_bf16 %[c31], v[140:143], v[148:151], %[c31]\n\t" \
    "v_mfma_f32_16x16x32_bf16 %[c32], v[140:143], v[152:155], %[c32]\n\t" \
    "v_mfma_f32_16x16x32_bf16 %[c33], v[140:143], v[156:159], %[c33]\n\t" \
    "s_waitcnt lgkmcnt(0)\n\t" \
    P1_RAW(5) \
    "s_barrier\n\t" \
    "v_mfma_f32_16x16x32_bf16 %[c00], v[160:163], v[176:179], %[c00]\n\t" \
    "ds_read_b128 v[128:131], %[xa0] offset:0\n\t" \
    "v_mfma_f32_16x16x32_bf16 %[c01], v[160:163], v[180:183], %[c01]\n\t" \
    "ds_read_b64_tr_b16 v[144:145], %[wb0] offset:0\n\t" \
    "v_mfma_f32_16x16x32_bf16 %[c10], v[164:167], v[176:179], %[c10]\n\t" \
    "ds_read_b64_tr_b16 v[146:147], %[wb0] offset:1024\n\t" \
    "v_mfma_f32_16x16x32_bf16 %[c11], v[164:167], v[180:183], %[c11]\n\t" \
    "ds_read_b64_tr_b16 v[148:149], %[wb1] offset:0\n\t" \
    "v_mfma_f32_16x16x32_bf16 %[c02], v[160:163], v[184:187], %[c02]\n\t" \
    "ds_read_b64_tr_b16 v[150:151], %[wb1] offset:1024\n\t" \
    "v_mfma_f32_16x16x32_bf16 %[c12], v[164:167], v[184:187], %[c12]\n\t" \
    "ds_read_b128 v[132:135], %[xa0] offset:512\n\t" \
    "v_mfma_f32_16x16x32_bf16 %[c03], v[160:163], v[188:191], %[c03]\n\t" \
    "ds_read_b64_tr_b16 v[152:153], %[wb2] offset:0\n\t" \
    "v_mfma_f32_16x16x32_bf16 %[c13], v[164:167], v[188:191], %[c13]\n\t" \
    "ds_read_b64_tr_b16 v[154:155], %[wb2] offset:1024\n\t" \
    "v_mfma_f32_16x16x32_bf16 %[c20], v[168:171], v[176:179], %[c20]\n\t" \
    "ds_read_b64_tr_b16 v[156:157], %[wb3] offset:0\n\t" \
    "v_mfma_f32_16x16x32_bf16 %[c21], v[168:171], v[180:183], %[c21]\n\t" \
    "ds_read_b64_tr_b16 v[158:159], %[wb3] offset:1024\n\t" \
    "v_mfma_f32_16x16x32_bf16 %[c22], v[168:171], v[184:187], %[c22]\n\t" \
    "ds_read_b128 v[136:139], %[xa0] offset:4096\n\t" \
    "v_mfma_f32_16x16x32_bf16 %[c23], v[168:171], v[188:191], %[c23]\n\t" \
    "ds_read_b128 v[140:143], %[xa0] offset:4608\n\t" \
    "v_mfma_f32_16x16x32_bf16 %[c30], v[172:175], v[176:179], %[c30]\n\t" \
    "v_mfma_f32_16x16x32_bf16 %[c31], v[172:175], v[180:183], %[c31]\n\t" \
    "v_mfma_f32_16x16x32_bf16 %[c32], v[172:175], v[184:187], %[c32]\n\t" \
    "v_mfma_f32_16x16x32_bf16 %[c33], v[172:175], v[188:191], %[c33]\n\t" \
    "s_waitcnt lgkmcnt(9)\n\t" \
    "v_mfma_f32_16x16x32_bf16 %[c00], v[128:131], v[144:147], %[c00]\n\t" \
    "ds_read_b128 v[160:163], %[xa1] offset:0\n\t" \
    "ds_read_b64_tr_b16 v[176:177], %[wb0] offset:8192\n\t" \
    "s_waitcnt lgkmcnt(9)\n\t" \
    "v_mfma_f32_16x16x32_bf16 %[c01], v[128:131], v[148:151], %[c01]\n\t" \
    "ds_read_b64_tr_b16 v[178:179], %[wb0] offset:9216\n\t" \
    "ds_read_b64_tr_b16 v[180:181], %[wb1] offset:8192\n\t" \
    "s_waitcnt lgkmcnt(10)\n\t" \
    "v_mfma_f32_16x16x32_bf16 %[c10], v[132:135], v[144:147], %[c10]\n\t" \
    "ds_read_b64_tr_b16 v[182:183], %[wb1] offset:9216\n\t" \
    "ds_read_b128 v[164:167], %[xa1] offset:512\n\t" \
    "v_mfma_f32_16x16x32_bf16 %[c11], v[132:135], v[148:151], %[c11]\n\t" \
    "ds_read_b64_tr_b16 v[184:185], %[wb2] offset:8192\n\t" \
    "ds_read_b64_tr_b16 v[186:187], %[wb2] offset:9216\n\t" \
    "s_waitcnt lgkmcnt(12)\n\t" \
    "v_mfma_f32_16x16x32_bf16 %[c02], v[128:131], v[152:155], %[c02]\n\t" \
    "ds_read_b64_tr_b16 v[188:189], %[wb3] offset:8192\n\t" \
    "ds_read_b64_tr_b16 v[190:191], %[wb3] offset:9216\n\t" \
    "v_mfma_f32_16x16x32_bf16 %[c12], v[132:135], v[152:155], %[c12]\n\t" \
    "ds_read_b128 v[168:171], %[xa1] offset:4096\n\t" \
    "ds_read_b128 v[172:175], %[xa1] offset:4608\n\t" \
    "s_waitcnt lgkmcnt(14)\n\t" \
    "v_mfma_f32_16x16x32_bf16 %[c03], v[128:131], v[156:159], %[c03]\n\t" \
    "v_mfma_f32_16x16x32_bf16 %[c13], v[132:135], v[156:159], %[c13]\n\t" \
    "s_waitcnt lgkmcnt(13)\n\t" \
    "v_mfma_f32_16x16x32_bf16 %[c20], v[136:139], v[144:147], %[c20]\n\t" \
    "v_mfma_f32_16x16x32_bf16 %[c21], v[136:139], v[148:151], %[c21]\n\t" \
    "v_mfma_f32_16x16x32_bf16 %[c22], v[136:139], v[152:155], %[c22]\n\t" \
    "v_mfma_f32_16x16x32_bf16 %[c23], v[136:139], v[156:159], %[c23]\n\t" \
    "s_waitcnt lgkmcnt(12)\n\t" \
    "v_mfma_f32_16x16x32_bf16 %[c30], v[140:143], v[144:147], %[c30]\n\t" \
    "v_mfma_f32_16x16x32_bf16 %[c31], v[140:143], v[148:151], %[c31]\n\t" \
    "v_mfma_f32_16x16x32_bf16 %[c32], v[140:143], v[152:155], %[c32]\n\t" \
    "v_mfma_f32_16x16x32_bf16 %[c33], v[140:143], v[156:159], %[c33]\n\t" \
    "s_waitcnt lgkmcnt(9)\n\t" \
    "v_mfma_f32_16x16x32_bf16 %[c00], v[160:163], v[176:179], %[c00]\n\t" \
    "s_waitcnt lgkmcnt(7)\n\t" \
    "v_mfma_f32_16x16x32_bf16 %[c01], v[160:163], v[180:183], %[c01]\n\t" \
    "s_waitcnt lgkmcnt(6)\n\t" \
    "v_mfma_f32_16x16x32_bf16 %[c10], v[164:167], v[176:179], %[c10]\n\t" \
    "v_mfma_f32_16x16x32_bf16 %[c11], v[164:167], v[180:183], %[c11]\n\t" \
    "s_waitcnt lgkmcnt(4)\n\t" \
    "v_mfma_f32_16x16x32_bf16 %[c02], v[160:163], v[184:187], %[c02]\n\t" \
    "v_mfma_f32_16x16x32_bf16 %[c12], v[164:167], v[184:187], %[c12]\n\t" \
    "s_waitcnt lgkmcnt(2)\n\t" \
    "v_mfma_f32_16x16x32_bf16 %[c03], v[160:163], v[188:191], %[c03]\n\t" \
    "v_mfma_f32_16x16x32_bf16 %[c13], v[164:167], v[188:191], %[c13]\n\t" \
    "s_waitcnt lgkmcnt(1)\n\t" \
    "v_mfma_f32_16x16x32_bf16 %[c20], v[168:171], v[176:179], %[c20]\n\t" \
    "v_mfma_f32_16x16x32_bf16 %[c21], v[168:171], v[180:183], %[c21]\n\t" \
    "v_mfma_f32_16x16x32_bf16 %[c22], v[168:171], v[184:187], %[c22]\n\t" \
    "v_mfma_f32_16x16x32_bf16 %[c23], v[168:171], v[188:191], %[c23]\n\t" \
    "s_waitcnt lgkmcnt(0)\n\t" \
    "v_mfma_f32_16x16x32_bf16 %[c30], v[172:175], v[176:179], %[c30]\n\t" \
    "v_mfma_f32_16x16x32_bf16 %[c31], v[172:175], v[180:183], %[c31]\n\t" \
    "v_mfma_f32_16x16x32_bf16 %[c32], v[172:175], v[184:187], %[c32]\n\t" \
    "v_mfma_f32_16x16x32_bf16 %[c33], v[172:175], v[188:191], %[c33]\n\t" \
    "s_nop 7\n\t" \
    "s_nop 7\n\t" \
    "s_nop 7\n\t"
#define P1_ASM_B \
    "v_add_u32 v192, 0x10000, %[xa0]\n\t" \
    "v_add_u32 v193, 0x10000, %[xa1]\n\t" \
    "v_add_u32 v194, 0x10000, %[wb0]\n\t" \
    "v_add_u32 v195, 0x10000, %[wb1]\n\t" \
    "v_add_u32 v196, 0x10000, %[wb2]\n\t" \
    "v_add_u32 v197, 0x10000, %[wb3]\n\t" \
    "s_waitcnt lgkmcnt(0)\n\t" \
    P1_RAW(6) \
    "s_barrier\n\t" \
    "ds_read_b128 v[128:131], %[xa0] offset:32768\n\t" \
    "ds_read_b64_tr_b16 v[144:145], %[wb0] offset:32768\n\t" \
    "ds_read_b64_tr_b16 v[146:147], %[wb0] offset:33792\n\t" \
    "ds_read_b64_tr_b16 v[148:149], %[wb1] offset:32768\n\t" \
    "ds_read_b64_tr_b16 v[150:151], %[wb1] offset:33792\n\t" \
    "ds_read_b128 v[132:135], %[xa0] offset:33280\n\t" \
    "ds_read_b64_tr_b16 v[152:153], %[wb2] offset:32768\n\t" \
    "ds_read_b64_tr_b16 v[154:155], %[wb2] offset:33792\n\t" \
    "ds_read_b64_tr_b16 v[156:157], %[wb3] offset:32768\n\t" \
    "ds_read_b64_tr_b16 v[158:159], %[wb3] offset:33792\n\t" \
    "ds_read_b128 v[136:139], %[xa0] offset:36864\n\t" \
    "ds_read_b128 v[140:143], %[xa0] offset:37376\n\t" \
    "s_waitcnt lgkmcnt(9)\n\t" \
    "v_mfma_f32_16x16x32_bf16 %[c00], v[128:131], v[144:147], %[c00]\n\t" \
    "ds_read_b128 v[160:163], %[xa1] offset:32768\n\t" \
    "ds_read_b64_tr_b16 v[176:177], %[wb0] offset:40960\n\t" \
    "s_waitcnt lgkmcnt(9)\n\t" \
    "v_mfma_f32_16x16x32_bf16 %[c01], v[128:131], v[148:151], %[c01]\n\t" \
    "ds_read_b64_tr_b16 v[178:179], %[wb0] offset:41984\n\t" \
    "ds_read_b64_tr_b16 v[180:181], %[wb1] offset:40960\n\t" \
    "s_waitcnt lgkmcnt(10)\n\t" \
    "v_mfma_f32_16x16x32_bf16 %[c10], v[132:135], v[144:147], %[c10]\n\t" \
    "ds_read_b64_tr_b16 v[182:183], %[wb1] offset:41984\n\t" \
    "ds_read_b128 v[164:167], %[xa1] offset:33280\n\t" \
    "v_mfma_f32_16x16x32_bf16 %[c11], v[132:135], v[148:151], %[c11]\n\t" \
    "ds_read_b64_tr_b16 v[184:185], %[wb2] offset:40960\n\t" \
    "ds_read_b64_tr_b16 v[186:187], %[wb2] offset:41984\n\t" \
    "s_waitcnt lgkmcnt(12)\n\t" \
    "v_mfma_f32_16x16x32_bf16 %[c02], v[128:131], v[152:155], %[c02]\n\t" \
    "ds_read_b64_tr_b16 v[188:189], %[wb3] offset:40960\n\t" \
    "ds_read_b64_tr_b16 v[190:191], %[wb3] offset:41984\n\t" \
    "v_mfma_f32_16x16x32_bf16 %[c12], v[132:135], v[152:155], %[c12]\n\t" \
    "ds_read_b128 v[168:171], %[xa1] offset:36864\n\t" \
    "ds_read_b128 v[172:175], %[xa1] offset:37376\n\t" \
    "s_waitcnt lgkmcnt(14)\n\t" \
    "v_mfma_f32_16x16x32_bf16 %[c03], v[128:131], v[156:159], %[c03]\n\t" \
    "v_mfma_f32_16x16x32_bf16 %[c13], v[132:135], v[156:159], %[c13]\n\t" \
    "s_waitcnt lgkmcnt(13)\n\t" \
    "v_mfma_f32_16x16x32_bf16 %[c20], v[136:139], v[144:147], %[c20]\n\t" \
    "v_mfma_f32_16x16x32_bf16 %[c21], v[136:139], v[148:151], %[c21]\n\t" \
    "v_mfma_f32_16x16x32_bf16 %[c22], v[136:139], v[152:155], %[c22]\n\t" \
    "v_mfma_f32_16x16x32_bf16 %[c23], v[136:139], v[156:159], %[c23]\n\t" \
    "s_waitcnt lgkmcnt(12)\n\t" \
    "v_mfma_f32_16x16x32_bf16 %[c30], v[140:143], v[144:147], %[c30]\n\t" \
    "v_mfma_f32_16x16x32_bf16 %[c31], v[140:143], v[148:151], %[c31]\n\t" \
    "v_mfma_f32_16x16x32_bf16 %[c32], v[140:143], v[152:155], %[c32]\n\t" \
    "v_mfma_f32_16x16x32_bf16 %[c33], v[140:143], v[156:159], %[c33]\n\t" \
    "s_waitcnt lgkmcnt(0)\n\t" \
    P1_RAW(7) \
    "s_barrier\n\t" \
    "v_mfma_f32_16x16x32_bf16 %[c00], v[160:163], v[176:179], %[c00]\n\t" \
    "ds_read_b128 v[128:131], v192 offset:32768\n\t" \
    "v_mfma_f32_16x16x32_bf16 %[c01], v[160:163], v[180:183], %[c01]\n\t" \
    "ds_read_b64_tr_b16 v[144:145], v194 offset:32768\n\t" \
    "v_mfma_f32_16x16x32_bf16 %[c10], v[164:167], v[176:179], %[c10]\n\t" \
    "ds_read_b64_tr_b16 v[146:147], v194 offset:33792\n\t" \
    "v_mfma_f32_16x16x32_bf16 %[c11], v[164:167], v[180:183], %[c11]\n\t" \
    "ds_read_b64_tr_b16 v[148:149], v195 offset:32768\n\t" \
    "v_mfma_f32_16x16x32_bf16 %[c02], v[160:163], v[184:187], %[c02]\n\t" \
    "ds_read_b64_tr_b16 v[150:151], v195 offset:33792\n\t" \
    "v_mfma_f32_16x16x32_bf16 %[c12], v[164:167], v[184:187], %[c12]\n\t" \
    "ds_read_b128 v[132:135], v192 offset:33280\n\t" \
    "v_mfma_f32_16x16x32_bf16 %[c03], v[160:163], v[188:191], %[c03]\n\t" \
    "ds_read_b64_tr_b16 v[152:153], v196 offset:32768\n\t" \
    "v_mfma_f32_16x16x32_bf16 %[c13], v[164:167], v[188:191], %[c13]\n\t" \
    "ds_read_b64_tr_b16 v[154:155], v196 offset:33792\n\t" \
    "v_mfma_f32_16x16x32_bf16 %[c20], v[168:171], v[176:179], %[c20]\n\t" \
    "ds_read_b64_tr_b16 v[156:157], v197 offset:32768\n\t" \
    "v_mfma_f32_16x16x32_bf16 %[c21], v[168:171], v[180:183], %[c21]\n\t" \
    "ds_read_b64_tr_b16 v[158:159], v197 offset:33792\n\t" \
    "v_mfma_f32_16x16x32_bf16 %[c22], v[168:171], v[184:187], %[c22]\n\t" \
    "ds_read_b128 v[136:139], v192 offset:36864\n\t" \
    "v_mfma_f32_16x16x32_bf16 %[c23], v[168:171], v[188:191], %[c23]\n\t" \
    "ds_read_b128 v[140:143], v192 offset:37376\n\t" \
    "v_mfma_f32_16x16x32_bf16 %[c30], v[172:175], v[176:179], %[c30]\n\t" \
    "v_mfma_f32_16x16x32_bf16 %[c31], v[172:175], v[180:183], %[c31]\n\t" \
    "v_mfma_f32_16x16x32_bf16 %[c32], v[172:175], v[184:187], %[c32]\n\t" \
    "v_mfma_f32_16x16x32_bf16 %[c33], v[172:175], v[188:191], %[c33]\n\t" \
    "s_waitcnt lgkmcnt(9)\n\t" \
    "v_mfma_f32_16x16x32_bf16 %[c00], v[128:131], v[144:147], %[c00]\n\t" \
    "ds_read_b128 v[160:163], v193 offset:32768\n\t" \
    "ds_read_b64_tr_b16 v[176:177], v194 offset:40960\n\t" \
    "s_waitcnt lgkmcnt(9)\n\t" \
    "v_mfma_f32_16x16x32_bf16 %[c01], v[128:131], v[148:151], %[c01]\n\t" \
    "ds_read_b64_tr_b16 v[178:179], v194 offset:41984\n\t" \
    "ds_read_b64_tr_b16 v[180:181], v195 offset:40960\n\t" \
    "s_waitcnt lgkmcnt(10)\n\t" \
    "v_mfma_f32_16x16x32_bf16 %[c10], v[132:135], v[144:147], %[c10]\n\t" \
    "ds_read_b64_tr_b16 v[182:183], v195 offset:41984\n\t" \
    "ds_read_b128 v[164:167], v193 offset:33280\n\t" \
    "v_mfma_f32_16x16x32_bf16 %[c11], v[132:135], v[148:151], %[c11]\n\t" \
    "ds_read_b64_tr_b16 v[184:185], v196 offset:40960\n\t" \
    "ds_read_b64_tr_b16 v[186:187], v196 offset:41984\n\t" \
    "s_waitcnt lgkmcnt(12)\n\t" \
    "v_mfma_f32_16x16x32_bf16 %[c02], v[128:131], v[152:155], %[c02]\n\t" \
    "ds_read_b64_tr_b16 v[188:189], v197 offset:40960\n\t" \
    "ds_read_b64_tr_b16 v[190:191], v197 offset:41984\n\t" \
    "v_mfma_f32_16x16x32_bf16 %[c12], v[132:135], v[152:155], %[c12]\n\t" \
    "ds_read_b128 v[168:171], v193 offset:36864\n\t" \
    "ds_read_b128 v[172:175], v193 offset:37376\n\t" \
    "s_waitcnt lgkmcnt(14)\n\t" \
    "v_mfma_f32_16x16x32_bf16 %[c03], v[128:131], v[156:159], %[c03]\n\t" \
    "v_mfma_f32_16x16x32_bf16 %[c13], v[132:135], v[156:159], %[c13]\n\t" \
    "s_waitcnt lgkmcnt(13)\n\t" \
    "v_mfma_f32_16x16x32_bf16 %[c20], v[136:139], v[144:147], %[c20]\n\t" \
    "v_mfma_f32_16x16x32_bf16 %[c21], v[136:139], v[148:151], %[c21]\n\t" \
    "v_mfma_f32_16x16x32_bf16 %[c22], v[136:139], v[152:155], %[c22]\n\t" \
    "v_mfma_f32_16x16x32_bf16 %[c23], v[136:139], v[156:159], %[c23]\n\t" \
    "s_waitcnt lgkmcnt(12)\n\t" \
    "v_mfma_f32_16x16x32_bf16 %[c30], v[140:143], v[144:147], %[c30]\n\t" \
    "v_mfma_f32_16x16x32_bf16 %[c31], v[140:143], v[148:151], %[c31]\n\t" \
    "v_mfma_f32_16x16x32_bf16 %[c32], v[140:143], v[152:155], %[c32]\n\t" \
    "v_mfma_f32_16x16x32_bf16 %[c33], v[140:143], v[156:159], %[c33]\n\t" \
    "s_waitcnt lgkmcnt(0)\n\t" \
    P1_RAW(8) \
    "s_barrier\n\t" \
    "v_mfma_f32_16x16x32_bf16 %[c00], v[160:163], v[176:179], %[c00]\n\t" \
    "ds_read_b128 v[128:131], v192 offset:0\n\t" \
    "v_mfma_f32_16x16x32_bf16 %[c01], v[160:163], v[180:183], %[c01]\n\t" \
    "ds_read_b64_tr_b16 v[144:145], v194 offset:0\n\t" \
    "v_mfma_f32_16x16x32_bf16 %[c10], v[164:167], v[176:179], %[c10]\n\t" \
    "ds_read_b64_tr_b16 v[146:147], v194 offset:1024\n\t" \
    "v_mfma_f32_16x16x32_bf16 %[c11], v[164:167], v[180:183], %[c11]\n\t" \
    "ds_read_b64_tr_b16 v[148:149], v195 offset:0\n\t" \
    "v_mfma_f32_16x16x32_bf16 %[c02], v[160:163], v[184:187], %[c02]\n\t" \
    "ds_read_b64_tr_b16 v[150:151], v195 offset:1024\n\t" \
    "v_mfma_f32_16x16x32_bf16 %[c12], v[164:167], v[184:187], %[c12]\n\t" \
    "ds_read_b128 v[132:135], v192 offset:512\n\t" \
    "v_mfma_f32_16x16x32_bf16 %[c03], v[160:163], v[188:191], %[c03]\n\t" \
    "ds_read_b64_tr_b16 v[152:153], v196 offset:0\n\t" \
    "v_mfma_f32_16x16x32_bf16 %[c13], v[164:167], v[188:191], %[c13]\n\t" \
    "ds_read_b64_tr_b16 v[154:155], v196 offset:1024\n\t" \
    "v_mfma_f32_16x16x32_bf16 %[c20], v[168:171], v[176:179], %[c20]\n\t" \
    "ds_read_b64_tr_b16 v[156:157], v197 offset:0\n\t" \
    "v_mfma_f32_16x16x32_bf16 %[c21], v[168:171], v[180:183], %[c21]\n\t" \
    "ds_read_b64_tr_b16 v[158:159], v197 offset:1024\n\t" \
    "v_mfma_f32_16x16x32_bf16 %[c22], v[168:171], v[184:187], %[c22]\n\t" \
    "ds_read_b128 v[136:139], v192 offset:4096\n\t" \
    "v_mfma_f32_16x16x32_bf16 %[c23], v[168:171], v[188:191], %[c23]\n\t" \
    "ds_read_b128 v[140:143], v192 offset:4608\n\t" \
    "v_mfma_f32_16x16x32_bf16 %[c30], v[172:175], v[176:179], %[c30]\n\t" \
    "v_mfma_f32_16x16x32_bf16 %[c31], v[172:175], v[180:183], %[c31]\n\t" \
    "v_mfma_f32_16x16x32_bf16 %[c32], v[172:175], v[184:187], %[c32]\n\t" \
    "v_mfma_f32_16x16x32_bf16 %[c33], v[172:175], v[188:191], %[c33]\n\t" \
    "s_waitcnt lgkmcnt(9)\n\t" \
    "v_mfma_f32_16x16x32_bf16 %[c00], v[128:131], v[144:147], %[c00]\n\t" \
    "ds_read_b128 v[160:163], v193 offset:0\n\t" \
    "s_waitcnt lgkmcnt(8)\n\t" \
    "v_mfma_f32_16x16x32_bf16 %[c01], v[128:131], v[148:151], %[c01]\n\t" \
    "ds_read_b64_tr_b16 v[176:177], v194 offset:8192\n\t" \
    "s_waitcnt lgkmcnt(8)\n\t" \
    "v_mfma_f32_16x16x32_bf16 %[c10], v[132:135], v[144:147], %[c10]\n\t" \
    "ds_read_b64_tr_b16 v[178:179], v194 offset:9216\n\t" \
    "v_mfma_f32_16x16x32_bf16 %[c11], v[132:135], v[148:151], %[c11]\n\t" \
    "ds_read_b64_tr_b16 v[180:181], v195 offset:8192\n\t" \
    "s_waitcnt lgkmcnt(8)\n\t" \
    "v_mfma_f32_16x16x32_bf16 %[c02], v[128:131], v[152:155], %[c02]\n\t" \
    "ds_read_b64_tr_b16 v[182:183], v195 offset:9216\n\t" \
    "v_mfma_f32_16x16x32_bf16 %[c12], v[132:135], v[152:155], %[c12]\n\t" \
    "ds_read_b128 v[164:167], v193 offset:512\n\t" \
    "s_waitcnt lgkmcnt(8)\n\t" \
    "v_mfma_f32_16x16x32_bf16 %[c03], v[128:131], v[156:159], %[c03]\n\t" \
    "ds_read_b64_tr_b16 v[184:185], v196 offset:8192\n\t" \
    "v_mfma_f32_16x16x32_bf16 %[c13], v[132:135], v[156:159], %[c13]\n\t" \
    "ds_read_b64_tr_b16 v[186:187], v196 offset:9216\n\t" \
    "s_waitcnt lgkmcnt(9)\n\t" \
    "v_mfma_f32_16x16x32_bf16 %[c20], v[136:139], v[144:147], %[c20]\n\t" \
    "ds_read_b64_tr_b16 v[188:189], v197 offset:8192\n\t" \
    "v_mfma_f32_16x16x32_bf16 %[c21], v[136:139], v[148:151], %[c21]\n\t" \
    "ds_read_b64_tr_b16 v[190:191], v197 offset:9216\n\t" \
    "v_mfma_f32_16x16x32_bf16 %[c22], v[136:139], v[152:155], %[c22]\n\t" \
    "ds_read_b128 v[168:171], v193 offset:4096\n\t" \
    "v_mfma_f32_16x16x32_bf16 %[c23], v[136:139], v[156:159], %[c23]\n\t" \
    "ds_read_b128 v[172:175], v193 offset:4608\n\t" \
    "s_waitcnt lgkmcnt(12)\n\t" \
    "v_mfma_f32_16x16x32_bf16 %[c30], v[140:143], v[144:147], %[c30]\n\t" \
    "v_mfma_f32_16x16x32_bf16 %[c31], v[140:143], v[148:151], %[c31]\n\t" \
    "v_mfma_f32_16x16x32_bf16 %[c32], v[140:143], v[152:155], %[c32]\n\t" \
    "v_mfma_f32_16x16x32_bf16 %[c33], v[140:143], v[156:159], %[c33]\n\t" \
    "s_waitcnt lgkmcnt(9)\n\t" \
    "v_mfma_f32_16x16x32_bf16 %[c00], v[160:163], v[176:179], %[c00]\n\t" \
    "s_waitcnt lgkmcnt(7)\n\t" \
    "v_mfma_f32_16x16x32_bf16 %[c01], v[160:163], v[180:183], %[c01]\n\t" \
    "s_waitcnt lgkmcnt(6)\n\t" \
    "v_mfma_f32_16x16x32_bf16 %[c10], v[164:167], v[176:179], %[c10]\n\t" \
    "v_mfma_f32_16x16x32_bf16 %[c11], v[164:167], v[180:183], %[c11]\n\t" \
    "s_waitcnt lgkmcnt(4)\n\t" \
    "v_mfma_f32_16x16x32_bf16 %[c02], v[160:163], v[184:187], %[c02]\n\t" \
    "v_mfma_f32_16x16x32_bf16 %[c12], v[164:167], v[184:187], %[c12]\n\t" \
    "s_waitcnt lgkmcnt(2)\n\t" \
    "v_mfma_f32_16x16x32_bf16 %[c03], v[160:163], v[188:191], %[c03]\n\t" \
    "v_mfma_f32_16x16x32_bf16 %[c13], v[164:167], v[188:191], %[c13]\n\t" \
    "s_waitcnt lgkmcnt(1)\n\t" \
    "v_mfma_f32_16x16x32_bf16 %[c20], v[168:171], v[176:179], %[c20]\n\t" \
    "v_mfma_f32_16x16x32_bf16 %[c21], v[168:171], v[180:183], %[c21]\n\t" \
    "v_mfma_f32_16x16x32_bf16 %[c22], v[168:171], v[184:187], %[c22]\n\t" \
    "v_mfma_f32_16x16x32_bf16 %[c23], v[168:171], v[188:191], %[c23]\n\t" \
    "s_waitcnt lgkmcnt(0)\n\t" \
    "v_mfma_f32_16x16x32_bf16 %[c30], v[172:175], v[176:179], %[c30]\n\t" \
    "v_mfma_f32_16x16x32_bf16 %[c31], v[172:175], v[180:183], %[c31]\n\t" \
    "v_mfma_f32_16x16x32_bf16 %[c32], v[172:175], v[184:187], %[c32]\n\t" \
    "v_mfma_f32_16x16x32_bf16 %[c33], v[172:175], v[188:191], %[c33]\n\t" \
    "s_nop 7\n\t" \
    "s_nop 7\n\t" \
    "s_nop 7\n\t"
#define P1_CLOB "v128", "v129", "v130", "v131", "v132", "v133", "v134", "v135", "v136", "v137", "v138", "v139", "v140", "v141", "v142", "v143", "v144", "v145", "v146", "v147", "v148", "v149", "v150", "v151", "v152", "v153", "v154", "v155", "v156", "v157", "v158", "v159", "v160", "v161", "v162", "v163", "v164", "v165", "v166", "v167", "v168", "v169", "v170", "v171", "v172", "v173", "v174", "v175", "v176", "v177", "v178", "v179", "v180", "v181", "v182", "v183", "v184", "v185", "v186", "v187", "v188", "v189", "v190", "v191", "v192", "v193", "v194", "v195", "v196", "v197"
__global__ void __launch_bounds__(512, 2) fused(Params p) {
  extern __shared__ __attribute__((aligned(16))) char smem[];
  LAS char* lds = (LAS char*)smem;
  const int wave = __builtin_amdgcn_readfirstlane(threadIdx.x >> 6), lane = threadIdx.x & 63;
  const int b = blockIdx.x, xcd = b & 7, loc = b >> 3;
  const bool heavy = loc >= 4;
  const int w4 = wave & 3, tid4 = w4 * 64 + lane;
  const bool isH = wave >= 4;

  u64 tag;
  const int arb = loc >> 2, aj = 4 * xcd + (loc & 3);
  const int hl = loc - 4;
  const int li = xcd * 4 + loc;
  const int hi = xcd * 28 + hl;
  const int ttb = heavy ? hi / 7 : li, tcb = heavy ? hi - 7 * (hi / 7) : 7;
  const int cch = heavy ? 2 * hi : 448 + 4 * li;
  const int kvidx = F_KV + (cch >> 4) * 32 + (lane & 31);
  int zidx = -1;
  LAS char* att = lds + L_ATT;
  const int qb = 4 * xcd + (loc & 3), ns = loc >> 2;
  AttAddr aa;

  if (__builtin_expect(heavy, 1)) {
    const int la = (hl & 3) ^ AX;
    const int rb = (xcd >> 1) * 4 + la, c = (xcd & 1) * 7 + (hl >> 2);
    if (__builtin_expect(isH, 1)) {
      const int hw = w4;
      const u32x4 rx = mk_rs(p.x + (size_t)rb * 128 * D, 128 * D * 2), rw = mk_rs(p.wqkv, D * 3 * D * 2);
      const unsigned vx = (lane >> 3) * 1152 + (((lane & 7) ^ ((lane >> 3) & 3)) << 4);
      const int cm = (c == 13) ? 7 : 15;
      const unsigned vw0 = (lane >> 4) * 3456 + c * 256 + ((((lane & 15) ^ ((lane >> 4) << 1)) & cm) << 4);
      const unsigned vw1 = (lane >> 4) * 3456 + c * 256 + ((((lane & 15) ^ (((lane >> 4) | 4) << 1)) & cm) << 4);
      const int s0 = ROT == 2 ? 2 * la + 2 * (hl >> 2) : (ROT ? (2 * la + 4 * (hl >> 2)) % 9 : 0);
      u64 gen = 0;
      if (hw == 0) asm volatile("global_load_dwordx2 %0, %1, off sc1" : "=v"(gen) : "v"(flagp(p, F_GEN)) : "memory");
      FENCE();
#pragma unroll
      for (int s = 0; s < PRO; s++) p1_dma_stage(rx, rw, s, s0, hw, vx, vw0, vw1);
      FENCE();
      auto prefetch = [&]() {
        if (PF8) {
          dma16(rw, L_DUMMY16, (c < 13 || (lane & 15) < 8) ? (unsigned)((512 + 16 * la + 4 * hw + (lane >> 4)) * 3456 + c * 256 + (lane & 15) * 16) : OOB, 0u);
          dma16(rx, L_DUMMY16, (hl >> 2) < 4 ? (unsigned)((32 * (hl >> 2) + 8 * hw + (lane >> 3)) * 1152 + 1024 + (lane & 7) * 16) : OOB, 0u);
          return;
        }
        const bool lo = lane < 32;
        if (hw == 0) dma4(mk_rs(p.wqkv, D * 3 * D * 2), L_DUMMY, (ROT == 2 && lo) ? (unsigned)((512 + 16 * la + (lane >> 1)) * 3456 + c * 256 + (lane & 1) * 128) : OOB);
        else if (hw == 1) dma4(mk_rs(p.x, S * D * 2), L_DUMMY, (ROT == 2 && lo && (hl >> 2) < 4) ? (unsigned)((rb * 128 + 32 * (hl >> 2) + lane) * 1152 + 1024) : OOB);
        else dma4(mk_rs(p.hk, D * S * 2), L_DUMMY, lo ? (unsigned)((cch + hw - 2) * 4096 + lane * 128) : OOB);
      };
      if (RSL < 0) { b1_raw_dma(p, 0, ttb, tcb, hw, lane); FENCE(); }
      if (PFS < 0) { prefetch(); FENCE(); }
      if (hw == 0) {
        asm volatile("s_waitcnt vmcnt(%1)" : "+v"(gen) : "n"(8 * PRO + (RSL < 0 ? 5 : 0) + (PFS < 0 ? 2 : 0)) : "memory");
        tag = mk_tag(((u64)(unsigned)__builtin_amdgcn_readfirstlane((unsigned)(gen >> 32)) << 32) | (unsigned)__builtin_amdgcn_readfirstlane((unsigned)gen));
        if (lane < 20) *(LAS unsigned*)(lds + L_MISC + lane * 4) = lane == 10 ? (unsigned)tag : (lane == 11 ? (unsigned)(tag >> 32) : 0u);
      }
      HkRegs hr;
#define H_SLOT(k) do { \
 \
        WAIT_LGKM0(); \
        WAIT_VMN(h_allow(k)); \
 \
        barrier_raw(); \
        FENCE(); \
        if ((k) == 0) { tag = lds_tag(lds); FENCE(); } \
        if ((k) == KF && lane == 0) st_flag(flagp(p, F_KV + (tcb * 4 + hw) * 32 + ttb), tag); \
        if ((k) == SPL) { b1_store(p, lds + L_B1, ttb, tcb, hw, lane); FENCE(); } \
        if ((k) == 0) { _Pragma("unroll") for (int s = PRO; s < 3 + S03; s++) p1_dma_stage(rx, rw, s, s0, hw, vx, vw0, vw1); } \
        else if ((k) == 1) { if (!S03) p1_dma_stage(rx, rw, 3, s0, hw, vx, vw0, vw1); p1_dma_stage(rx, rw, 4, s0, hw, vx, vw0, vw1); } \
        else if ((k) <= 5) p1_dma_stage(rx, rw, (k) + 3, s0, hw, vx, vw0, vw1); \
        FENCE(); \
        if ((k) == RSL) { b1_raw_dma(p, 0, ttb, tcb, hw, lane); FENCE(); } \
        if ((k) == PFS) { prefetch(); FENCE(); } \
        if ((k) == HK) { hk_load_asm(p, hr, cch, tid4); FENCE(); }       \
      } while (0)
      H_SLOT(0); H_SLOT(1); H_SLOT(2); H_SLOT(3); H_SLOT(4); H_SLOT(5); H_SLOT(6); H_SLOT(7);
      WAIT_LGKM0();
      WAIT_VMN(h_allow(8));
      FENCE();
      barrier_raw();
      FENCE();

      if (KF == 8 && lane == 0) st_flag(flagp(p, F_KV + (tcb * 4 + hw) * 32 + ttb), tag);
      FENCE();

      WAIT_LGKM0();
      barrier_raw();
      const __amdgpu_buffer_rsrc_t rq = __builtin_amdgcn_make_buffer_rsrc((void*)(p.ws + WS_QKV), 0, 1728 * S * 2, 0x00020000);
      const bool cvalid = c * 128 + hw * 32 < 3 * D;
#pragma unroll
      for (int u = 0; u < 8; u++) {
        const int n = hw * 32 + u * 4 + (lane >> 4);
        u32x4 v = *(const LAS u32x4*)(lds + L_CIMG + n * 256 + (((lane & 15) ^ (n & 15)) << 4));
        __builtin_amdgcn_raw_buffer_store_b128(v, rq, cvalid ? (unsigned)((c * 128 + n) * (S * 2) + rb * 256 + (lane & 15) * 16) : 0x7ffffff0u, 0, AUX_SC1);
      }
      FENCE();

      WAIT_VMN(h_younger(5, 400));
      hk_pin(hr);
      hk_write_main(hr, lds + L_CONV, tid4, 0);
      hk_write_edge(hr, lds + L_CONV, tid4);
      hk_write_main(hr, lds + L_CONV, tid4, 1);
      FENCE();
      WAIT_LGKM0();
      if (lane == 0) *(volatile LAS unsigned*)(lds + L_HRDY + 4 * hw) = 1u;

      WAIT_VM(0);
      if (lane == 0 && cvalid) st_flag(flagp(p, F_Q + rb * 64 + 4 * c + hw), tag);

      att_setup(aa, p, att, ns, wave, lane);
      FENCE();
      PollRegs pr = {0, 0, 0};
      att_fetch(pr, p, lds, arb, aj, qb, hw, lane, tag);
      WAIT_VM(0);
      poll_pin(pr);

    } else {
      const int wm = wave & 1, wn = wave >> 1, g = lane >> 4;
      unsigned dbits = p.Dd[cch + (w4 >> 1)];
      f32x4 acc[4][4];
      {
        const unsigned xa0 = 32768 + (64 * wm + 8 * ((lane & 15) >> 2) + (lane & 3)) * 128 + ((g ^ (lane & 7)) << 4);
        const int q = (lane >> 2) & 3, pp = lane & 3, f = q | ((g & 1) << 2);
        unsigned wb[4];
#pragma unroll
        for (int nt = 0; nt < 4; nt++) wb[nt] = 32768 + 16384 + (8 * g + q) * 256 + (((4 * wn + nt) ^ f) << 5) + pp * 8;
        asm volatile("" :: "v"(xa0), "v"(wb[0]), "v"(wb[1]), "v"(wb[2]), "v"(wb[3]));
        FENCE();
        B1Addr ba;
        b1_addr(ba, w4, lane);
        if (MKP == 0) {
          if (MSL) __builtin_amdgcn_s_sleep(MSL);
          o_mark(p, arb, aj, 2 * w4, lane); o_mark(p, arb, aj, 2 * w4 + 1, lane);
        }
        FENCE();
        barrier_raw();
        FENCE();

        FENCE();
        asm volatile(
          P1_ASM_A
          : [c00] "=&v"(acc[0][0]), [c01] "=&v"(acc[0][1]), [c02] "=&v"(acc[0][2]), [c03] "=&v"(acc[0][3]),
            [c10] "=&v"(acc[1][0]), [c11] "=&v"(acc[1][1]), [c12] "=&v"(acc[1][2]), [c13] "=&v"(acc[1][3]),
            [c20] "=&v"(acc[2][0]), [c21] "=&v"(acc[2][1]), [c22] "=&v"(acc[2][2]), [c23] "=&v"(acc[2][3]),
            [c30] "=&v"(acc[3][0]), [c31] "=&v"(acc[3][1]), [c32] "=&v"(acc[3][2]), [c33] "=&v"(acc[3][3])
          : [xa0] "v"(xa0), [xa1] "v"(xa0 ^ 64u), [wb0] "v"(wb[0]), [wb1] "v"(wb[1]), [wb2] "v"(wb[2]), [wb3] "v"(wb[3])
          : "memory", P1_CLOB);
        FENCE();
        if (MKP == 1) { o_mark(p, arb, aj, 2 * w4, lane); o_mark(p, arb, aj, 2 * w4 + 1, lane); FENCE(); }
        tag = lds_tag(lds);
        {
          B1In bi;
          b1_read2(bi, ba, lds);
          SCHED_FENCE();
          b1_math<true>(bi, lds, w4, lane, ba.st[0], ba.st[1]);
        }
        GAS u64* const kvf = lane == 1 && w4 == 0 ? flagp(p, F_SEENC) + b : flagp(p, F_SEEN + 4 * b + w4);
        FENCE();
        asm volatile(
          P1_ASM_B
          : [c00] "+v"(acc[0][0]), [c01] "+v"(acc[0][1]), [c02] "+v"(acc[0][2]), [c03] "+v"(acc[0][3]),
            [c10] "+v"(acc[1][0]), [c11] "+v"(acc[1][1]), [c12] "+v"(acc[1][2]), [c13] "+v"(acc[1][3]),
            [c20] "+v"(acc[2][0]), [c21] "+v"(acc[2][1]), [c22] "+v"(acc[2][2]), [c23] "+v"(acc[2][3]),
            [c30] "+v"(acc[3][0]), [c31] "+v"(acc[3][1]), [c32] "+v"(acc[3][2]), [c33] "+v"(acc[3][3])
          : [xa0] "v"(xa0), [xa1] "v"(xa0 ^ 64u), [wb0] "v"(wb[0]), [wb1] "v"(wb[1]), [wb2] "v"(wb[2]), [wb3] "v"(wb[3]), [fa] "v"(kvf), [tg] "v"(tag)
          : "memory", P1_CLOB);
      }
      SCHED_FENCE();

      const int kvidx = F_KV + (cch >> 4) * 32 + 16 * (1 - (w4 & 1)) + (lane & 15);
      u64 kvl;
      asm volatile("global_load_dwordx2 %0, %1, off sc1" : "=v"(kvl) : "v"(flagp(p, kvidx)) : "memory");
      FENCE();
      {
        const float qs = 0.23570226039551584f * 1.4426950408889634f;
        LAS char* cw = lds + L_CIMG + (64 * wn + (lane & 15)) * 256;
        auto c_write = [&](float sc, bool scale) {
#pragma unroll
          for (int nt = 0; nt < 4; nt++)
#pragma unroll
            for (int u = 0; u < 2; u++) {
              f32x4 x = acc[2 * u][nt], y = acc[2 * u + 1][nt];
              if (scale) { x *= sc; y *= sc; }
              *(LAS u32x4*)(cw + nt * 4096 + (((8 * wm + 4 * u + g) ^ (lane & 15)) << 4)) = (u32x4){pk_bf16(x[0], x[1]), pk_bf16(x[2], x[3]), pk_bf16(y[0], y[1]), pk_bf16(y[2], y[3])};
            }
        };
        if (c * 128 + 64 * wn < D) c_write(qs, true); else c_write(1.f, false);
      }
      WAIT_LGKM0();
      barrier_raw();

      asm volatile("" : "+v"(dbits));
      asm volatile("s_waitcnt vmcnt(0)" : "+v"(kvl) :: "memory");
      if (__builtin_amdgcn_ballot_w64(kvl != tag) != 0) poll_block<KVSL>(p, kvidx, true, tag);

      ur_dma_h(p, cch, w4, lane, true);
      FENCE();
      att_setup(aa, p, att, ns, wave, lane);
      FENCE();
      WAIT_VM(0);
      if (lane == 0) *(volatile LAS unsigned*)(lds + L_RAWRDY + 4 * w4) = 1u;
#pragma unroll 1
      for (int it = 0; it < (1 << 24); it++) {
        const u32x4 rd = *(volatile LAS u32x4*)(lds + L_HRDY), re = *(volatile LAS u32x4*)(lds + L_RAWRDY);
        if (__builtin_amdgcn_readfirstlane(rd[0] & rd[1] & rd[2] & rd[3] & re[0] & re[1] & re[2] & re[3]) == 1u) break;
      }
      FENCE();
      if (w4 < 3) dma4(mk_rs(p.wo, D * D * 2), L_DUMMY, PFWO ? (unsigned)(((hl * 3 + w4) * 64 + lane) * 128) : OOB);
      else dma4(mk_rs(p.q, S * D * 2), L_DUMMY, PFWO ? (unsigned)((64 * ttb + lane) * 1152 + tcb * 128) : OOB);
      FENCE();

      {
        f32x4 ca[8];
        conv_mfma<true>(p, lds + L_CONV, cch, w4, lane, ca, dbits);

        conv_finish(p, lds + L_CONV, lds + L_XCH, lds + L_XF0, cch, w4, lane, ca, dbits);

      }

      zidx = F_Z + (w4 & 1) * D + cch + (w4 >> 1);
    }
  } else {
    const int grp = wave >> 2;
    LAS char* cv = lds + (grp ? L_CONV2 : L_CONV);
    LAS char* xc = lds + L_ATT + grp * 16384;
    const int ch = cch + 2 * grp;
    u64 gen = 0;
    o_mark(p, arb, aj, wave, lane);
    FENCE();
    if (wave == 0) asm volatile("global_load_dwordx2 %0, %1, off sc1" : "=v"(gen) : "v"(flagp(p, F_GEN)) : "memory");
    FENCE();
    b1_raw_dma(p, grp ? L_RAW2 : 0, ttb, tcb + grp, w4, lane);
    FENCE();
    HkRegs hr;
    hk_load_asm(p, hr, ch, tid4);
    FENCE();
    if (wave == 0) {
      asm volatile("s_waitcnt vmcnt(11)" : "+v"(gen) :: "memory");
      tag = mk_tag(((u64)(unsigned)__builtin_amdgcn_readfirstlane((unsigned)(gen >> 32)) << 32) | (unsigned)__builtin_amdgcn_readfirstlane((unsigned)gen));
      if (lane < 20) *(LAS unsigned*)(lds + L_MISC + lane * 4) = lane == 10 ? (unsigned)tag : (lane == 11 ? (unsigned)(tag >> 32) : 0u);
    }

    WAIT_VM(6);
    WAIT_LGKM0();
    barrier_raw();
    {
      LAS char* base = lds + (grp ? L_RAW2 : 0);
      B1In bi;
      b1_read(bi, base, base + L_RS, w4, lane);
      SCHED_FENCE();
      b1_math_store(p, bi, base + L_B1, ttb, tcb + grp, w4, lane);
    }
    tag = lds_tag(lds);
    WAIT_LGKM0(); barrier_raw();
    WAIT_VM(2);
    hk_pin(hr);
    hk_write_main(hr, cv, tid4, 0);
    hk_write_main(hr, cv, tid4, 1);
    hk_write_edge(hr, cv, tid4);
    WAIT_VM(0);
    if (lane == 0) st_flag(flagp(p, F_KV + ((tcb + grp) * 4 + w4) * 32 + ttb), tag);
    if (wave == 1 && lane < 5) st_flag(lane < 4 ? flagp(p, F_SEEN + 4 * b + lane) : flagp(p, F_SEENC) + b, tag);
    att_setup(aa, p, att, ns, wave, lane);
    FENCE();
    WAIT_LGKM0();
    if (wave == 0) poll_block<4>(p, kvidx, true, tag);
    barrier_raw();
    ur_dma(p, cv, ch, w4, lane);
    __builtin_amdgcn_s_waitcnt(0x0F70);
    barrier_raw();

    if (PFWO && (wave == 4 || wave == 5)) dma4(mk_rs(p.q, S * D * 2), L_DUMMY, (unsigned)((64 * ttb + lane) * 1152 + (tcb + wave - 4) * 128));
    {
      f32x4 ca[8]; unsigned dbits;
      conv_mfma<false>(p, cv, ch, w4, lane, ca, dbits);

      conv_finish(p, cv, xc, lds + L_XF0 + 16 * grp, ch, w4, lane, ca, dbits);

    }

    PollRegs pr = {0, 0, 0};
    if (wave == 0) att_poll(pr, p, arb, aj, qb, lane, tag);
    if (b == 7 && wave == GENW) {
      GAS u64* a = flagp(p, F_SEENC) + lane;
#pragma unroll 1
      for (int it = 0; it < SPIN_MAX; it++) {
        const u64 s0 = ld_flag(a), s1 = ld_flag(a + 64), s2 = ld_flag(a + 128), s3 = ld_flag(a + 192);
        if (__builtin_amdgcn_ballot_w64(s0 != tag || s1 != tag || s2 != tag || s3 != tag) == 0) break;
        __builtin_amdgcn_s_sleep(8);
      }
      if (lane == 0) st_flag(flagp(p, F_GEN), tag);
    }
    WAIT_LGKM0(); barrier_raw();

    if (isH) { att_ones(lds, arb, tid4); att_dma_kq(p, arb, aj, w4, lane); att_dma_v(p, arb, aj, w4, lane); }
    WAIT_VM(0);
    poll_pin(pr);
    if (lane == 0) st_flag(flagp(p, F_Z + (w4 & 1) * D + ch + (w4 >> 1)), tag);
  }
  WAIT_LGKM0();

  barrier_raw();

  u64 zf = ~tag;
  if (PRI && isH) __builtin_amdgcn_s_setprio(PRI);
  const int zwait = b3_flag(ttb, tcb, w4, lane, !heavy);
  att_wave(p, att, aa, arb, aj, wave, lane, zidx, isH, zwait, zf, tag);
  FENCE();

  if (PRI && isH) __builtin_amdgcn_s_setprio(0);
  if (isH) {
    asm volatile("s_waitcnt vmcnt(2)" : "+v"(zf) :: "memory");
    const bool zok = __builtin_amdgcn_ballot_w64(zf != tag) == 0;
    B3Regs R;
    if (heavy) {
      if (zok) b3_load<1>(R, p, ttb, tcb, w4, lane);
      barrier_raw();
      if (!zok) { poll_block(p, zwait, true, tag); b3_load<1>(R, p, ttb, tcb, w4, lane); }
      b3_gate<1>(R, p, lds + L_Z + w4 * 2048, ttb, tcb, w4, lane);
    } else {
      if (zok) b3_load<2>(R, p, ttb, tcb, w4, lane);
      barrier_raw();
      if (!zok) { poll_block(p, zwait, true, tag); b3_load<2>(R, p, ttb, tcb, w4, lane); }
      b3_gate<2>(R, p, lds + L_Z + w4 * 2048, ttb, tcb, w4, lane);
    }

  } else {
    WAIT_VM(2);

    p3_wave(p, lds + L_WO, qb, ns, w4, lane);

  }
}

extern "C" void kernel_launch(void* const* d_in, const int* in_sizes, int n_in, void* d_out, int out_size, void* d_ws, size_t ws_size, hipStream_t stream) {
  Params p;
  p.x = (const u16*)d_in[0]; p.wqkv = (const u16*)d_in[1]; p.wo = (const u16*)d_in[2]; p.q = (const u16*)d_in[3]; p.k = (const u16*)d_in[4]; p.v = (const u16*)d_in[5];
  p.sk = (const u16*)d_in[6]; p.hk = (const u16*)d_in[7]; p.Dd = (const u16*)d_in[8]; p.out = (u16*)d_out; p.ws = (char*)d_ws;
  constexpr int LDS_BYTES = 160 * 1024;
  (void)hipFuncSetAttribute((const void*)fused, hipFuncAttributeMaxDynamicSharedMemorySize, LDS_BYTES);
  fused<<<256, 512, LDS_BYTES, stream>>>(p);
}
```
